```python
import torch

def _softmax(x, axis=-1):
    return torch.softmax(x, dim=axis)
def _gelu(x):
    return torch.nn.functional.gelu(x, approximate="tanh")
def _silu(x):
    return torch.nn.functional.silu(x)
def _sigmoid(x):
    return torch.sigmoid(x)
def _layernorm(x, axis=-1, eps=1e-5):
    m = x.mean(dim=axis, keepdim=True)
    v = x.var(dim=axis, keepdim=True, unbiased=False)
    return (x - m) / torch.sqrt(v + eps)
def _scatter_add(tgt, idx, src, axis=0):
    return tgt.clone().index_add_(axis, idx, src)
def _segment_sum(src, seg, n):
    out = torch.zeros((n,) + src.shape[1:], dtype=src.dtype)
    return out.index_add_(0, seg, src)
def _pad(x, pw):
    flat = tuple(v for pair in reversed(pw) for v in pair)
    return torch.nn.functional.pad(x, flat)

kernel_name = 'sequential_preLN__ssm_conv_h3__glu_gated_swiglu__prompt'

def setup_inputs():

    _w = (lambda a: (lambda t: t.to(torch.bfloat16) if t.is_floating_point() else t)(torch.as_tensor(a)))
    def _A_setup():
        B, S, D = 1, 2048, 1024
        ks = 4
        return {
            "q": _w((lambda *s: torch.randn(*s, dtype=torch.bfloat16) * 0.05)(B, S, D)),
            "k": _w((lambda *s: torch.randn(*s, dtype=torch.bfloat16) * 0.2)(B, S, D)),
            "v": _w((lambda *s: torch.randn(*s, dtype=torch.bfloat16) * 0.2)(B, S, D)),
            "shift_k": _w((lambda *s: torch.randn(*s, dtype=torch.bfloat16) * 0.2)(D, ks) * 0.5),
            "ssm_k": _w((lambda *s: torch.randn(*s, dtype=torch.bfloat16) * 0.4)(D, S) * (1.0 / S ** 0.5)),
            "ssm_D": _w((lambda *s: torch.randn(*s, dtype=torch.bfloat16) * 0.2)(D)),
        }
    _A_in = _A_setup()

    def _B_setup():
        B, S, D, F = 1, 2048, 1024, 3584
        return {
            "x": _w((lambda *s: torch.randn(*s, dtype=torch.bfloat16) * 0.05)(B, S, D)),
            "w1": _w((lambda *s: torch.randn(*s, dtype=torch.bfloat16) * 0.2)(D, F) * (2.0 / D) ** 0.5),
            "w3": _w((lambda *s: torch.randn(*s, dtype=torch.bfloat16) * 0.2)(D, F) * (2.0 / D) ** 0.5),
            "w2": _w((lambda *s: torch.randn(*s, dtype=torch.bfloat16) * 0.2)(F, D) * (2.0 / F) ** 0.5),
        }
    _B_in = _B_setup()

    return {
        "A_q": _A_in["q"],
        "A_k": _A_in["k"],
        "A_v": _A_in["v"],
        "A_shift_k": _A_in["shift_k"],
        "A_ssm_k": _A_in["ssm_k"],
        "A_ssm_D": _A_in["ssm_D"],
        "B_x": _B_in["x"],
        "B_w1": _B_in["w1"],
        "B_w3": _B_in["w3"],
        "B_w2": _B_in["w2"],
    }

def reference(A_q, A_k, A_v, A_shift_k, A_ssm_k, A_ssm_D, B_x, B_w1, B_w3, B_w2):
    def _A_ref(q, k, v, shift_k, ssm_k, ssm_D):
        B, S, D = q.shape
        ks = shift_k.shape[-1]
        k_pad = _pad(k, ((0, 0), (ks - 1, 0), (0, 0)))
        k_shift = torch.zeros_like(k)
        for t in range(ks):
            k_shift = k_shift + k_pad[:, t:t + S, :] * shift_k[:, ks - 1 - t][None, None, :]
        kv = k_shift * v
        L = 2 * S
        kv_pad = _pad(kv, ((0, 0), (0, S), (0, 0)))
        kf_pad = _pad(ssm_k, ((0, 0), (0, S)))
        kv_f = (lambda x, axes=(-2,-1): torch.fft.rfft2(x.float(), dim=axes))(kv_pad[:, None, :, :], axes=(1, 2))
        kf_f = (lambda x, axes=(-2,-1): torch.fft.rfft2(x.float(), dim=axes))(kf_pad[:, None, :], axes=(1, 2))
        kf_f = (lambda x, *d: x.permute(*d))(kf_f, 1, 2, 0)
        y_f = kv_f * kf_f[None, :, :, :]
        y = (lambda x, s, axes=(-2,-1): torch.fft.irfft2(x, s=s, dim=axes).to(torch.bfloat16))(y_f, (1, L), axes=(1, 2))[:, 0, :S, :]
        y = y + kv * ssm_D[None, None, :]
        return q * y

    def _B_ref(x, w1, w3, w2):
        gate = torch.einsum("bsd,df->bsf", x, w1)
        up = torch.einsum("bsd,df->bsf", x, w3)
        h = _silu(gate) * up
        out = torch.einsum("bsf,fd->bsd", h, w2)
        return out

    x = A_q
    h = x + _A_ref(_layernorm(x, axis=-1), A_k, A_v, A_shift_k, A_ssm_k, A_ssm_D)
    h2 = h + _B_ref(_layernorm(h, axis=-1), B_w1, B_w3, B_w2)
    return h2
```

```cpp
#include <hip/hip_runtime.h>
#include <hip/hip_bf16.h>
typedef unsigned short u16;
static __device__ __forceinline__ float b2f(u16 b){ return __uint_as_float(((unsigned)b)<<16); }
static __device__ __forceinline__ u16 f2b(float f){ unsigned u=__float_as_uint(f); u += 0x7FFFu + ((u>>16)&1u); return (u16)(u>>16); }
static __device__ __forceinline__ float rb(float f){ return b2f(f2b(f)); }
constexpr int S=2048, D=1024, F=3584;

__global__ void k_kv(const u16* k,const u16* v,const u16* sk,u16* kv){
  int i=blockIdx.x*256+threadIdx.x; int s=i/D,d=i%D; float acc=0;
  for(int t=0;t<4;t++){ int j=3-t; float kk = (s-j>=0)? b2f(k[(s-j)*D+d]):0.f; acc = rb(acc + rb(kk*b2f(sk[d*4+j]))); }
  kv[i]=f2b(acc*b2f(v[i]));
}
__global__ void k_conv(const u16* kv,const u16* ssmk,const u16* ssmD,float* y){
  int i=blockIdx.x*256+threadIdx.x; int s=i/D,d=i%D; float acc=0;
  for(int j=0;j<=s;j++) acc += b2f(kv[(s-j)*D+d])*b2f(ssmk[d*S+j]);
  y[i]= rb(rb(acc) + rb(b2f(kv[i])*b2f(ssmD[d])));
}
__device__ float bsum(float v,float* sh){ for(int o=32;o>0;o>>=1) v+=__shfl_xor(v,o); __syncthreads(); if((threadIdx.x&63)==0) sh[threadIdx.x>>6]=v; __syncthreads(); return sh[0]+sh[1]+sh[2]+sh[3]; }
__global__ void k_h(const u16* x,const float* y,u16* h,u16* hn){
  __shared__ float sh[4]; int s=blockIdx.x; float xv[4],hv[4]; float a=0;
  for(int j=0;j<4;j++){ xv[j]=b2f(x[s*D+threadIdx.x*4+j]); a+=xv[j]; }
  float m=bsum(a,sh)/D; a=0; for(int j=0;j<4;j++) a+=(xv[j]-m)*(xv[j]-m); float var=bsum(a,sh)/D; float r=rsqrtf(var+1e-5f);
  a=0; for(int j=0;j<4;j++){ hv[j]=rb(xv[j]+rb(rb((xv[j]-m)*r)*y[s*D+threadIdx.x*4+j])); h[s*D+threadIdx.x*4+j]=f2b(hv[j]); a+=hv[j]; }
  m=bsum(a,sh)/D; a=0; for(int j=0;j<4;j++) a+=(hv[j]-m)*(hv[j]-m); var=bsum(a,sh)/D; r=rsqrtf(var+1e-5f);
  for(int j=0;j<4;j++) hn[s*D+threadIdx.x*4+j]=f2b((hv[j]-m)*r);
}
__global__ void k_g1(const u16* hn,const u16* w1,const u16* w3,u16* H){
  int f=blockIdx.x*256+threadIdx.x; int s=blockIdx.y; float g=0,u=0;
  for(int k=0;k<D;k++){ float a=b2f(hn[s*D+k]); g+=a*b2f(w1[k*F+f]); u+=a*b2f(w3[k*F+f]); }
  g=rb(g); u=rb(u); float si=rb(g/(1.f+__expf(-g))); H[s*F+f]=f2b(si*u);
}
__global__ void k_g2(const u16* H,const u16* w2,const u16* h,u16* out){
  int d=blockIdx.x*256+threadIdx.x; int s=blockIdx.y; float g=0;
  for(int k=0;k<F;k++) g+=b2f(H[s*F+k])*b2f(w2[k*D+d]);
  out[s*D+d]=f2b(b2f(h[s*D+d])+rb(g));
}
extern "C" void kernel_launch(void* const* d_in,const int* in_sizes,int n_in,void* d_out,int out_size,void* d_ws,size_t ws_size,hipStream_t stream){
  const u16 *q=(const u16*)d_in[0],*k=(const u16*)d_in[1],*v=(const u16*)d_in[2],*sk=(const u16*)d_in[3],*ssmk=(const u16*)d_in[4],*ssmD=(const u16*)d_in[5],*w1=(const u16*)d_in[7],*w3=(const u16*)d_in[8],*w2=(const u16*)d_in[9];
  char* ws=(char*)d_ws; u16* kv=(u16*)ws; float* y=(float*)(ws+(8<<20)); u16* h=(u16*)(ws+(24<<20)); u16* hn=(u16*)(ws+(32<<20)); u16* H=(u16*)(ws+(40<<20));
  k_kv<<<S*D/256,256,0,stream>>>(k,v,sk,kv);
  k_conv<<<S*D/256,256,0,stream>>>(kv,ssmk,ssmD,y);
  k_h<<<S,256,0,stream>>>(q,y,h,hn);
  k_g1<<<dim3(F/256,S),256,0,stream>>>(hn,w1,w3,H);
  k_g2<<<dim3(D/256,S),256,0,stream>>>(H,w2,h,(u16*)d_out);
}
```

```cpp
#include <hip/hip_runtime.h>
typedef unsigned short u16;
struct Args { const void *xq, *kk, *vv, *shk, *ssmk, *ssmD, *w1, *w3, *w2; void *out, *ws; };

__global__ __launch_bounds__(256, 1) __attribute__((aligned(4096))) void h3_block(Args) {
  extern __shared__ __attribute__((aligned(16))) char smem[];
  unsigned long long ka = (unsigned long long)__builtin_amdgcn_kernarg_segment_ptr();
  unsigned b = blockIdx.x, lds = (unsigned)(size_t)(__attribute__((address_space(3))) char*)smem, t = threadIdx.x;
  asm volatile(
    "s_load_dwordx16 s[84:99], s[0:1], 0x0\n"
    "s_load_dwordx4 s[40:43], s[0:1], 0x40\n"
    "s_load_dwordx2 s[8:9], s[0:1], 0x50\n"
    "v_readfirstlane_b32 s37, v0\n"
    "s_nop 0\n"
    "s_lshr_b32 s37, s37, 6\n"
    "s_getpc_b64 s[56:57]\n"
    ".Lh3_pc0:\n"
    "s_and_b32 s57, s57, 0xffff\n"
    "s_mov_b32 s58, .Lh3_end0-.Lh3_pc0\n"
    "s_mov_b32 s59, 0x20000\n"
    "s_lshr_b32 s36, s2, 3\n"
    "s_lshl_b32 s36, s36, 11\n"
    "v_lshrrev_b32 v1, 4, v0\n"
    "v_and_b32 v2, 60, v0\n"
    "v_and_or_b32 v1, v0, 3, v1\n"
    "v_min_u32 v2, 1, v2\n"
    "v_lshl_add_u32 v1, v1, 7, s36\n"
    "v_lshl_or_b32 v1, v2, 31, v1\n"
    "buffer_load_dword a254, v1, s[56:59], 0 offen\n"
    "s_and_b32 s4, s2, 7\n"
    "s_lshr_b32 s5, s2, 3\n"
    "v_and_b32 v3, 63, v0\n"
    "s_and_b32 s56, s4, 3\n"
    "s_lshl_b32 s56, s56, 5\n"
    "s_add_u32 s56, s56, s5\n"
    "s_lshl_b32 s56, s56, 2\n"
    "s_add_u32 s56, s56, s37\n"
    "s_lshl_b32 s56, s56, 13\n"
    "s_lshr_b32 s57, s4, 2\n"
    "s_lshl_b32 s58, s57, 10\n"
    "s_add_u32 s56, s56, s58\n"
    "v_lshl_add_u32 v1, v3, 4, s56\n"
    "s_lshl_b32 s58, s57, 12\n"
    "v_lshl_add_u32 v2, v3, 6, s58\n"
    "v_lshlrev_b32 v3, 4, v3\n"
    "s_lshl_b32 s59, s4, 5\n"
    "s_add_u32 s59, s59, s5\n"
    "s_lshl_b32 s59, s59, 2\n"
    "s_add_u32 s59, s59, s37\n"
    "s_waitcnt lgkmcnt(0)\n"
    "s_mov_b32 s12, s86\n"
    "s_and_b32 s13, s87, 0xffff\n"
    "s_mov_b32 s14, 4194304\n"
    "s_mov_b32 s15, 0x20000\n"
    "s_mov_b32 s16, s88\n"
    "s_and_b32 s17, s89, 0xffff\n"
    "s_mov_b32 s18, 4194304\n"
    "s_mov_b32 s19, 0x20000\n"
    "s_mov_b32 s24, s90\n"
    "s_and_b32 s25, s91, 0xffff\n"
    "s_mov_b32 s26, 8192\n"
    "s_mov_b32 s27, 0x20000\n"
    "s_mov_b32 s20, s84\n"
    "s_and_b32 s21, s85, 0xffff\n"
    "s_mov_b32 s22, 4194304\n"
    "s_mov_b32 s23, 0x20000\n"
    "s_lshl_b32 s56, s59, 12\n"
    "s_add_u32 s48, s92, s56\n"
    "s_addc_u32 s49, s93, 0\n"
    "s_and_b32 s49, s49, 0xffff\n"
    "s_mov_b32 s50, 4096\n"
    "s_mov_b32 s51, 0x20000\n"
    "s_and_b32 s9, s9, 0xffff\n"
    "s_mov_b32 s10, 75497472\n"
    "s_mov_b32 s11, 0x20000\n"
    "s_lshl_b32 s56, s59, 1\n"
    "s_add_u32 s56, s94, s56\n"
    "s_addc_u32 s57, s95, 0\n"
    "v_mov_b32 v4, s56\n"
    "v_mov_b32 v5, s57\n"
    "v_add_u32 v6, 0xffffe800, v1\n"
    "v_add_u32 v7, 0xfffff000, v1\n"
    "v_add_u32 v8, 0xfffff800, v1\n"
    "v_add_u32 v9, 4096, v1\n"
    "v_add_u32 v10, -8, v3\n"
    "buffer_load_dwordx4 v[48:51], v6, s[12:15], 0 offen\n"
    "buffer_load_dwordx4 v[52:55], v7, s[12:15], 0 offen\n"
    "buffer_load_dwordx4 v[56:59], v8, s[12:15], 0 offen\n"
    "buffer_load_dwordx4 v[60:63], v1, s[12:15], 0 offen offset:0\n"
    "buffer_load_dwordx4 v[64:67], v1, s[12:15], 0 offen offset:2048\n"
    "buffer_load_dwordx4 v[68:71], v9, s[12:15], 0 offen offset:0\n"
    "buffer_load_dwordx4 v[72:75], v9, s[12:15], 0 offen offset:2048\n"
    "buffer_load_dwordx4 v[76:79], v1, s[16:19], 0 offen offset:0\n"
    "buffer_load_dwordx4 v[80:83], v1, s[16:19], 0 offen offset:2048\n"
    "buffer_load_dwordx4 v[84:87], v9, s[16:19], 0 offen offset:0\n"
    "buffer_load_dwordx4 v[88:91], v9, s[16:19], 0 offen offset:2048\n"
    "buffer_load_dwordx4 v[108:111], v2, s[24:27], 0 offen offset:0\n"
    "buffer_load_dwordx4 v[112:115], v2, s[24:27], 0 offen offset:16\n"
    "buffer_load_dwordx4 v[116:119], v2, s[24:27], 0 offen offset:32\n"
    "buffer_load_dwordx4 v[120:123], v2, s[24:27], 0 offen offset:48\n"
    "buffer_load_dwordx2 v[140:141], off, s[8:11], 0 sc1\n"
    "s_cmp_lg_u32 s37, 3\n"
    "s_cbranch_scc1 P0_S%=\n"
    "s_mov_b64 exec, 0\n"
    "s_branch P0_H0%=\n"
    "P0_R%=:\n"
    "s_mov_b64 exec, -1\n"
    "P0_S%=:\n"
    "s_mov_b32 s88, s20\n"
    "s_mov_b32 s89, s21\n"
    "s_mov_b32 s90, s22\n"
    "s_mov_b32 s91, s23\n"
    "s_mov_b32 s92, s48\n"
    "s_mov_b32 s93, s49\n"
    "s_mov_b32 s94, s50\n"
    "s_mov_b32 s95, s51\n"
    "v_mov_b32 v216, v1\n"
    "v_mov_b32 v217, v9\n"
    "v_mov_b32 v218, v3\n"
    "v_mov_b32 v219, v10\n"
    "v_mov_b32 v220, v4\n"
    "v_mov_b32 v221, v5\n"
    "v_mov_b32 v44, v0\n"
    "v_mbcnt_lo_u32_b32 v208, -1, 0\n"
    "v_mbcnt_hi_u32_b32 v208, -1, v208\n"
    "v_lshrrev_b32 v209, 4, v208\n"
    "v_and_b32 v210, 15, v208\n"
    "v_bfe_u32 v211, v208, 2, 2\n"
    "v_and_b32 v212, 3, v208\n"
    "v_and_b32 v213, 7, v208\n"
    "v_lshrrev_b32 v214, 3, v208\n"
    "s_lshr_b32 s6, s4, 2\n"
    "s_lshl_b32 s6, s6, 2\n"
    "s_lshr_b32 s56, s5, 3\n"
    "s_add_u32 s6, s6, s56\n"
    "s_and_b32 s7, s4, 3\n"
    "s_lshl_b32 s7, s7, 3\n"
    "s_and_b32 s56, s5, 7\n"
    "s_add_u32 s7, s7, s56\n"
    "s_and_b32 s52, s4, 3\n"
    "s_lshl_b32 s52, s52, 5\n"
    "s_add_u32 s52, s52, s5\n"
    "s_lshr_b32 s53, s4, 2\n"
    "s_lshl_b32 s63, s4, 5\n"
    "s_add_u32 s63, s63, s5\n"
    "s_mul_i32 s59, s37, 16640\n"
    "s_cmp_lt_u32 s37, 3\n"
    "s_cselect_b32 s59, s59, 128640\n"
    "s_add_u32 s59, s59, s3\n"
    "s_mul_i32 s56, s2, 384\n"
    "s_add_u32 s56, s56, 131072\n"
    "v_lshl_add_u32 v172, v0, 4, s56\n"
    "v_add_u32 v215, 232, v0\n"
    "v_lshrrev_b32 v215, 8, v215\n"
    "v_lshl_or_b32 v172, v215, 31, v172\n"
    "s_mul_i32 s56, s2, 1856\n"
    "s_add_u32 s56, s56, 524288\n"
    "v_lshl_add_u32 v173, v0, 4, s56\n"
    "v_add_u32 v215, 12, v0\n"
    "v_lshrrev_b32 v215, 7, v215\n"
    "v_min_u32 v215, 1, v215\n"
    "v_lshl_or_b32 v173, v215, 31, v173\n"
    "v_mov_b32 v180, 0\n"
    "v_mov_b32 v181, 0\n"
    "v_mov_b32 v182, 0\n"
    "v_mov_b32 v183, 0\n"
    "buffer_store_dwordx4 v[180:183], v172, s[8:11], 0 offen sc1\n"
    "buffer_store_dwordx4 v[180:183], v173, s[8:11], 0 offen sc1\n"
    "s_and_b32 s56, s37, 1\n"
    "s_lshl_b32 s56, s56, 3\n"
    "s_add_u32 s56, s56, s3\n"
    "s_add_u32 s56, s56, 65536\n"
    "v_lshl_add_u32 v215, v208, 8, s56\n"
    "s_lshr_b32 s57, s37, 1\n"
    "s_lshl_b32 s57, s57, 2\n"
    "v_xor_b32 v176, s57, v213\n"
    "v_xor_b32 v177, 0, v176\n"
    "v_lshl_add_u32 v160, v177, 4, v215\n"
    "v_xor_b32 v177, 1, v176\n"
    "v_lshl_add_u32 v161, v177, 4, v215\n"
    "v_xor_b32 v177, 2, v176\n"
    "v_lshl_add_u32 v162, v177, 4, v215\n"
    "v_xor_b32 v177, 3, v176\n"
    "v_lshl_add_u32 v163, v177, 4, v215\n"
    "s_add_u32 s56, s3, 81920\n"
    "v_lshl_add_u32 v215, v208, 8, s56\n"
    "s_lshl_b32 s57, s37, 1\n"
    "v_xor_b32 v176, s57, v213\n"
    "v_xor_b32 v177, 0, v176\n"
    "v_lshl_add_u32 v164, v177, 4, v215\n"
    "v_xor_b32 v177, 1, v176\n"
    "v_lshl_add_u32 v165, v177, 4, v215\n"
    "s_lshl_b32 s56, s37, 10\n"
    "s_add_u32 s56, s56, s3\n"
    "s_add_u32 s56, s56, 65536\n"
    "v_lshl_add_u32 v215, v214, 7, s56\n"
    "s_and_b32 s57, s37, 1\n"
    "s_lshl_b32 s57, s57, 2\n"
    "v_add_u32 v176, s57, v209\n"
    "v_xor_b32 v176, v176, v213\n"
    "v_lshl_add_u32 v166, v176, 4, v215\n"
    "s_lshl_b32 s56, s53, 4\n"
    "s_add_u32 s56, s56, s37\n"
    "s_lshl_b32 s56, s56, 10\n"
    "s_add_u32 s56, s56, s52\n"
    "s_lshl_b32 s56, s56, 7\n"
    "v_lshl_add_u32 v215, v214, 14, s56\n"
    "v_lshl_add_u32 v167, v213, 4, v215\n"
    "s_lshl_b32 s56, s52, 2\n"
    "s_add_u32 s56, s56, s37\n"
    "s_lshl_b32 s56, s56, 6\n"
    "s_lshl_b32 s57, s53, 3\n"
    "s_add_u32 s56, s56, s57\n"
    "s_add_u32 s56, s56, 458752\n"
    "v_lshl_add_u32 v215, v209, 4, s56\n"
    "v_min_u32 v176, 1, v210\n"
    "v_lshl_or_b32 v168, v176, 31, v215\n"
    "v_sub_u32 v176, 63, v208\n"
    "v_lshl_add_u32 v169, v176, 4, s59\n"
    "v_bfe_u32 v176, v208, 1, 2\n"
    "v_mul_u32_u24 v176, 4160, v176\n"
    "v_and_b32 v177, 1, v208\n"
    "v_lshl_add_u32 v176, v177, 4, v176\n"
    "s_add_u32 s56, s59, 4096\n"
    "v_add_u32 v170, s56, v176\n"
    "s_lshl_b32 s56, s37, 9\n"
    "s_add_u32 s56, s56, s3\n"
    "s_add_u32 s56, s56, 145280\n"
    "v_and_b32 v176, 31, v208\n"
    "v_lshl_add_u32 v171, v176, 4, s56\n"
    "v_sub_u32 v176, 3, v210\n"
    "v_and_b32 v176, 3, v176\n"
    "v_mul_u32_u24 v177, 4158, v176\n"
    "v_lshl_add_u32 v178, v209, 3, v210\n"
    "v_lshlrev_b32 v179, 1, v178\n"
    "v_sub_u32 v177, v177, v179\n"
    "s_add_u32 s56, s59, 78\n"
    "v_add_u32 v32, s56, v177\n"
    "v_lshlrev_b32 v176, 1, v210\n"
    "v_sub_u32 v176, v176, v209\n"
    "s_add_u32 s56, s3, 145280\n"
    "v_add_u32 v177, 1, v176\n"
    "v_bfe_u32 v179, v177, 2, 2\n"
    "v_xor_b32 v179, s37, v179\n"
    "v_lshl_add_u32 v177, v177, 2, v179\n"
    "v_lshl_add_u32 v33, v177, 4, s56\n"
    "v_add_u32 v177, 5, v176\n"
    "v_bfe_u32 v179, v177, 2, 2\n"
    "v_xor_b32 v179, s37, v179\n"
    "v_lshl_add_u32 v177, v177, 2, v179\n"
    "v_lshl_add_u32 v34, v177, 4, s56\n"
    "v_add_u32 v177, 9, v176\n"
    "v_bfe_u32 v179, v177, 2, 2\n"
    "v_xor_b32 v179, s37, v179\n"
    "v_lshl_add_u32 v177, v177, 2, v179\n"
    "v_lshl_add_u32 v35, v177, 4, s56\n"
    "v_add_u32 v177, 13, v176\n"
    "v_bfe_u32 v179, v177, 2, 2\n"
    "v_xor_b32 v179, s37, v179\n"
    "v_lshl_add_u32 v177, v177, 2, v179\n"
    "v_lshl_add_u32 v36, v177, 4, s56\n"
    "s_lshl_b32 s57, s63, 14\n"
    "s_lshl_b32 s56, s37, 12\n"
    "s_add_u32 s56, s56, s57\n"
    "s_add_u32 s56, s56, 1048576\n"
    "v_xor_b32 v176, v212, v209\n"
    "v_and_or_b32 v176, v208, 60, v176\n"
    "v_lshl_add_u32 v37, v176, 4, s56\n"
    "s_add_u32 s56, s57, 5242880\n"
    "v_lshl_add_u32 v38, v0, 4, s56\n"
    "s_mov_b32 s56, 458752\n"
    "v_lshl_add_u32 v39, v0, 5, s56\n"
    "s_add_u32 s56, s57, 10485760\n"
    "v_lshl_add_u32 v47, v0, 4, s56\n"
    "v_add_u32 v178, -8, v178\n"
    "v_and_b32 v176, 1, v178\n"
    "v_lshlrev_b32 v176, 4, v176\n"
    "v_lshlrev_b32 v176, v176, 1\n"
    "v_lshrrev_b32 v178, 1, v178\n"
    "v_cmp_eq_u32 vcc, 0, v178\n"
    "s_nop 1\n"
    "v_cndmask_b32 v40, 0, v176, vcc\n"
    "v_cmp_eq_u32 vcc, 1, v178\n"
    "s_nop 1\n"
    "v_cndmask_b32 v41, 0, v176, vcc\n"
    "v_cmp_eq_u32 vcc, 2, v178\n"
    "s_nop 1\n"
    "v_cndmask_b32 v42, 0, v176, vcc\n"
    "v_cmp_eq_u32 vcc, 3, v178\n"
    "s_nop 1\n"
    "v_cndmask_b32 v43, 0, v176, vcc\n"
    "v_bfe_u32 v176, v210, 1, 2\n"
    "v_xor_b32 v176, v176, v209\n"
    "v_lshl_add_u32 v176, v210, 2, v176\n"
    "v_lshl_add_u32 v45, v176, 4, s59\n"
    "v_lshrrev_b32 v176, 1, v0\n"
    "v_bfe_u32 v177, v0, 4, 2\n"
    "v_xor_b32 v176, v176, v177\n"
    "v_and_b32 v177, 1, v0\n"
    "v_lshl_add_u32 v176, v176, 1, v177\n"
    "v_lshl_add_u32 v46, v176, 3, s3\n"
    "v_add_u32 v148, 128640, v46\n"
    "s_lshl_b32 s56, s63, 3\n"
    "s_add_u32 s56, s56, 4096\n"
    "v_lshl_add_u32 v176, v208, 11, s56\n"
    "s_lshl_b32 s56, s4, 11\n"
    "s_add_u32 s56, s56, 4096\n"
    "v_lshl_add_u32 v145, v208, 4, s56\n"
    "v_lshrrev_b32 v177, 3, v0\n"
    "v_min_u32 v177, 1, v177\n"
    "v_lshl_or_b32 v144, v177, 31, v176\n"
    "s_lshl_b32 s56, s4, 7\n"
    "s_add_u32 s56, s56, 131072\n"
    "v_mov_b32 v146, s56\n"
    "s_add_u32 s56, s56, 1024\n"
    "v_lshl_add_u32 v147, v213, 2, s56\n"
    "s_lshl_b32 s56, s4, 2\n"
    "s_add_u32 s56, s56, 132096\n"
    "v_lshl_add_u32 v151, v213, 7, s56\n"
    "s_and_b32 s56, s2, 15\n"
    "s_lshl_b32 s56, s56, 7\n"
    "s_add_u32 s56, s56, 135168\n"
    "v_lshl_add_u32 v174, v213, 11, s56\n"
    "s_lshl_b32 s56, s4, 11\n"
    "s_add_u32 s56, s56, 135168\n"
    "v_lshl_add_u32 v175, v210, 7, s56\n"
    "v_add_u32 v176, s2, v0\n"
    "v_min_u32 v176, 1, v176\n"
    "v_lshlrev_b32 v149, 31, v176\n"
    "s_lshl_b32 s56, s2, 2\n"
    "s_add_u32 s56, s56, s37\n"
    "s_lshl_b32 s56, s56, 4\n"
    "s_add_u32 s56, s56, 7168\n"
    "v_add_lshl_u32 v176, v208, s56, 7\n"
    "v_min_u32 v177, 1, v209\n"
    "v_lshl_or_b32 v150, v177, 31, v176\n"
    "s_mov_b32 s12, s96\n"
    "s_and_b32 s13, s97, 0xffff\n"
    "s_mov_b32 s14, 7340032\n"
    "s_mov_b32 s15, 0x20000\n"
    "s_mov_b32 s20, s98\n"
    "s_and_b32 s21, s99, 0xffff\n"
    "s_mov_b32 s22, 7340032\n"
    "s_mov_b32 s23, 0x20000\n"
    "s_cmp_lt_u32 s37, 3\n"
    "s_cselect_b32 s16, s12, s20\n"
    "s_cselect_b32 s17, s13, s21\n"
    "s_mov_b32 s18, 7340032\n"
    "s_mov_b32 s19, 0x20000\n"
    "s_mov_b32 s24, s20\n"
    "s_mov_b32 s25, s21\n"
    "s_mov_b32 s26, 7340032\n"
    "s_mov_b32 s27, 0x20000\n"
    "s_mov_b32 s54, s42\n"
    "s_mov_b32 s55, s43\n"
    "s_and_b32 s41, s41, 0xffff\n"
    "s_mov_b32 s42, 7340032\n"
    "s_mov_b32 s43, 0x20000\n"
    "s_lshl_b32 s28, s37, 12\n"
    "s_add_u32 s28, s28, s3\n"
    "s_mul_i32 s58, s7, 224\n"
    "s_mov_b32 s59, 6944\n"
    "s_add_u32 s56, s37, 0\n"
    "s_cmp_ge_u32 s56, 7\n"
    "s_cselect_b32 s57, 7, 0\n"
    "s_cselect_b32 s29, 7184, 0\n"
    "s_sub_u32 s57, s56, s57\n"
    "v_lshl_add_u32 v176, s57, 6, v208\n"
    "s_lshl_b32 s57, s57, 10\n"
    "s_add_u32 s29, s29, s57\n"
    "s_add_u32 s29, s29, s3\n"
    "s_add_u32 s29, s29, 65536\n"
    "v_mul_u32_u24 v177, 4682, v176\n"
    "v_lshrrev_b32 v177, 16, v177\n"
    "v_lshl_add_u32 v176, v176, 4, s58\n"
    "v_mad_u32_u24 v225, v177, s59, v176\n"
    "s_add_u32 s56, s37, 4\n"
    "s_cmp_ge_u32 s56, 7\n"
    "s_cselect_b32 s57, 7, 0\n"
    "s_cselect_b32 s30, 7184, 0\n"
    "s_sub_u32 s57, s56, s57\n"
    "v_lshl_add_u32 v176, s57, 6, v208\n"
    "s_lshl_b32 s57, s57, 10\n"
    "s_add_u32 s30, s30, s57\n"
    "s_add_u32 s30, s30, s3\n"
    "s_add_u32 s30, s30, 65536\n"
    "v_mul_u32_u24 v177, 4682, v176\n"
    "v_lshrrev_b32 v177, 16, v177\n"
    "v_lshl_add_u32 v176, v176, 4, s58\n"
    "v_mad_u32_u24 v226, v177, s59, v176\n"
    "s_add_u32 s56, s37, 8\n"
    "s_cmp_ge_u32 s56, 7\n"
    "s_cselect_b32 s57, 7, 0\n"
    "s_cselect_b32 s31, 7184, 0\n"
    "s_sub_u32 s57, s56, s57\n"
    "v_lshl_add_u32 v176, s57, 6, v208\n"
    "s_lshl_b32 s57, s57, 10\n"
    "s_add_u32 s31, s31, s57\n"
    "s_add_u32 s31, s31, s3\n"
    "s_add_u32 s31, s31, 65536\n"
    "v_mul_u32_u24 v177, 4682, v176\n"
    "v_lshrrev_b32 v177, 16, v177\n"
    "v_lshl_add_u32 v176, v176, 4, s58\n"
    "v_mad_u32_u24 v227, v177, s59, v176\n"
    "s_add_u32 s56, s37, 12\n"
    "s_cmp_ge_u32 s56, 7\n"
    "s_cselect_b32 s57, 7, 0\n"
    "s_cselect_b32 s32, 7184, 0\n"
    "s_sub_u32 s57, s56, s57\n"
    "v_lshl_add_u32 v176, s57, 6, v208\n"
    "s_lshl_b32 s57, s57, 10\n"
    "s_add_u32 s32, s32, s57\n"
    "s_cmp_ge_u32 s56, 14\n"
    "s_cselect_b32 s32, 14368, s32\n"
    "s_cselect_b32 s57, 2147483648, 0\n"
    "s_add_u32 s58, s58, s57\n"
    "s_add_u32 s32, s32, s3\n"
    "s_add_u32 s32, s32, 65536\n"
    "v_mul_u32_u24 v177, 4682, v176\n"
    "v_lshrrev_b32 v177, 16, v177\n"
    "v_lshl_add_u32 v176, v176, 4, s58\n"
    "v_mad_u32_u24 v228, v177, s59, v176\n"
    "s_mul_i32 s34, s6, 917504\n"
    "s_lshl_b32 s52, s37, 12\n"
    "s_add_u32 s52, s52, s3\n"
    "s_add_u32 s52, s52, 147328\n"
    "s_waitcnt vmcnt(2) lgkmcnt(0)\n"
    "v_readfirstlane_b32 s56, v140\n"
    "v_readfirstlane_b32 s57, v141\n"
    "s_nop 0\n"
    "buffer_load_dwordx4 v[92:95], v216, s[88:91], 0 offen offset:0\n"
    "s_lshr_b64 s[58:59], s[56:57], 29\n"
    "s_xor_b64 s[58:59], s[58:59], s[56:57]\n"
    "s_mul_i32 s38, s58, 0x1ce4e5b9\n"
    "s_mul_hi_u32 s39, s58, 0x1ce4e5b9\n"
    "s_mul_i32 s59, s59, 0x1ce4e5b9\n"
    "s_mul_i32 s58, s58, 0xbf58476d\n"
    "s_add_u32 s39, s39, s59\n"
    "s_add_u32 s39, s39, s58\n"
    "s_add_u32 s38, s38, 0x7f4a7c15\n"
    "s_addc_u32 s39, s39, 0x9e3779b9\n"
    "s_lshr_b32 s58, s39, 3\n"
    "s_xor_b32 s38, s38, s58\n"
    "buffer_load_dwordx4 v[96:99], v216, s[88:91], 0 offen offset:2048\n"
    "s_cmp_eq_u64 s[38:39], s[56:57]\n"
    "s_cselect_b32 s58, 1, 0\n"
    "s_xor_b32 s38, s38, s58\n"
    "s_mov_b32 s56, 0x05040100\n"
    "s_mov_b32 s57, 0x07060302\n"
    "v_perm_b32 v176, v48, v52, s56\n"
    "v_perm_b32 v177, v52, v56, s56\n"
    "v_perm_b32 v178, v56, v60, s56\n"
    "v_perm_b32 v179, v60, v64, s56\n"
    "v_perm_b32 v180, v64, v68, s56\n"
    "v_perm_b32 v181, v68, v72, s56\n"
    "v_perm_b32 v182, v48, v52, s57\n"
    "buffer_load_dwordx4 v[100:103], v217, s[88:91], 0 offen offset:0\n"
    "v_perm_b32 v183, v52, v56, s57\n"
    "v_perm_b32 v184, v56, v60, s57\n"
    "v_perm_b32 v185, v60, v64, s57\n"
    "v_perm_b32 v186, v64, v68, s57\n"
    "v_perm_b32 v187, v68, v72, s57\n"
    "v_dot2_f32_bf16 v0, v176, v109, 0\n"
    "v_dot2_f32_bf16 v1, v182, v111, 0\n"
    "v_dot2_f32_bf16 v2, v177, v109, 0\n"
    "v_dot2_f32_bf16 v3, v183, v111, 0\n"
    "v_dot2_f32_bf16 v4, v178, v109, 0\n"
    "v_dot2_f32_bf16 v5, v184, v111, 0\n"
    "v_dot2_f32_bf16 v6, v179, v109, 0\n"
    "buffer_load_dwordx4 v[104:107], v217, s[88:91], 0 offen offset:2048\n"
    "v_dot2_f32_bf16 v7, v185, v111, 0\n"
    "v_dot2_f32_bf16 v0, v178, v108, v0\n"
    "v_dot2_f32_bf16 v1, v184, v110, v1\n"
    "v_dot2_f32_bf16 v2, v179, v108, v2\n"
    "v_dot2_f32_bf16 v3, v185, v110, v3\n"
    "v_dot2_f32_bf16 v4, v180, v108, v4\n"
    "v_dot2_f32_bf16 v5, v186, v110, v5\n"
    "v_dot2_f32_bf16 v6, v181, v108, v6\n"
    "v_dot2_f32_bf16 v7, v187, v110, v7\n"
    "v_lshlrev_b32 v200, 16, v76\n"
    "v_and_b32 v201, 0xffff0000, v76\n"
    "v_lshlrev_b32 v202, 16, v80\n"
    "buffer_load_dwordx4 v[124:127], v218, s[92:95], 0 offen offset:0\n"
    "v_and_b32 v203, 0xffff0000, v80\n"
    "v_lshlrev_b32 v204, 16, v84\n"
    "v_and_b32 v205, 0xffff0000, v84\n"
    "v_lshlrev_b32 v206, 16, v88\n"
    "v_and_b32 v207, 0xffff0000, v88\n"
    "v_pk_mul_f32 v[0:1], v[0:1], v[200:201]\n"
    "v_pk_mul_f32 v[2:3], v[2:3], v[202:203]\n"
    "v_pk_mul_f32 v[4:5], v[4:5], v[204:205]\n"
    "v_pk_mul_f32 v[6:7], v[6:7], v[206:207]\n"
    "v_cvt_pk_bf16_f32 v208, v0, v2\n"
    "v_cvt_pk_bf16_f32 v209, v4, v6\n"
    "v_cvt_pk_bf16_f32 v210, v1, v3\n"
    "buffer_load_dwordx2 v[152:153], v219, s[92:95], 0 offen\n"
    "v_cvt_pk_bf16_f32 v211, v5, v7\n"
    "ds_write_b64 v160, v[208:209] offset:0\n"
    "ds_write_b64 v161, v[210:211] offset:0\n"
    "v_perm_b32 v176, v49, v53, s56\n"
    "v_perm_b32 v177, v53, v57, s56\n"
    "v_perm_b32 v178, v57, v61, s56\n"
    "v_perm_b32 v179, v61, v65, s56\n"
    "v_perm_b32 v180, v65, v69, s56\n"
    "v_perm_b32 v181, v69, v73, s56\n"
    "v_perm_b32 v182, v49, v53, s57\n"
    "v_perm_b32 v183, v53, v57, s57\n"
    "v_perm_b32 v184, v57, v61, s57\n"
    "buffer_load_dwordx4 v[128:131], v218, s[92:95], 0 offen offset:1024\n"
    "v_perm_b32 v185, v61, v65, s57\n"
    "v_perm_b32 v186, v65, v69, s57\n"
    "v_perm_b32 v187, v69, v73, s57\n"
    "v_dot2_f32_bf16 v8, v176, v113, 0\n"
    "v_dot2_f32_bf16 v9, v182, v115, 0\n"
    "v_dot2_f32_bf16 v10, v177, v113, 0\n"
    "v_dot2_f32_bf16 v11, v183, v115, 0\n"
    "v_dot2_f32_bf16 v12, v178, v113, 0\n"
    "v_dot2_f32_bf16 v13, v184, v115, 0\n"
    "v_dot2_f32_bf16 v14, v179, v113, 0\n"
    "v_dot2_f32_bf16 v15, v185, v115, 0\n"
    "v_dot2_f32_bf16 v8, v178, v112, v8\n"
    "buffer_load_dwordx2 v[154:155], v218, s[92:95], 0 offen offset:1016\n"
    "v_dot2_f32_bf16 v9, v184, v114, v9\n"
    "v_dot2_f32_bf16 v10, v179, v112, v10\n"
    "v_dot2_f32_bf16 v11, v185, v114, v11\n"
    "v_dot2_f32_bf16 v12, v180, v112, v12\n"
    "v_dot2_f32_bf16 v13, v186, v114, v13\n"
    "v_dot2_f32_bf16 v14, v181, v112, v14\n"
    "v_dot2_f32_bf16 v15, v187, v114, v15\n"
    "v_lshlrev_b32 v200, 16, v77\n"
    "v_and_b32 v201, 0xffff0000, v77\n"
    "v_lshlrev_b32 v202, 16, v81\n"
    "v_and_b32 v203, 0xffff0000, v81\n"
    "v_lshlrev_b32 v204, 16, v85\n"
    "buffer_load_dwordx4 v[132:135], v218, s[92:95], 0 offen offset:2048\n"
    "v_and_b32 v205, 0xffff0000, v85\n"
    "v_lshlrev_b32 v206, 16, v89\n"
    "v_and_b32 v207, 0xffff0000, v89\n"
    "v_pk_mul_f32 v[8:9], v[8:9], v[200:201]\n"
    "v_pk_mul_f32 v[10:11], v[10:11], v[202:203]\n"
    "v_pk_mul_f32 v[12:13], v[12:13], v[204:205]\n"
    "v_pk_mul_f32 v[14:15], v[14:15], v[206:207]\n"
    "v_cvt_pk_bf16_f32 v212, v8, v10\n"
    "v_cvt_pk_bf16_f32 v213, v12, v14\n"
    "v_cvt_pk_bf16_f32 v214, v9, v11\n"
    "v_cvt_pk_bf16_f32 v215, v13, v15\n"
    "ds_write_b64 v162, v[212:213] offset:0\n"
    "buffer_load_dwordx2 v[156:157], v218, s[92:95], 0 offen offset:2040\n"
    "ds_write_b64 v163, v[214:215] offset:0\n"
    "v_perm_b32 v176, v50, v54, s56\n"
    "v_perm_b32 v177, v54, v58, s56\n"
    "v_perm_b32 v178, v58, v62, s56\n"
    "v_perm_b32 v179, v62, v66, s56\n"
    "v_perm_b32 v180, v66, v70, s56\n"
    "v_perm_b32 v181, v70, v74, s56\n"
    "v_perm_b32 v182, v50, v54, s57\n"
    "v_perm_b32 v183, v54, v58, s57\n"
    "v_perm_b32 v184, v58, v62, s57\n"
    "v_perm_b32 v185, v62, v66, s57\n"
    "v_perm_b32 v186, v66, v70, s57\n"
    "buffer_load_dwordx4 v[136:139], v218, s[92:95], 0 offen offset:3072\n"
    "v_perm_b32 v187, v70, v74, s57\n"
    "v_dot2_f32_bf16 v16, v176, v117, 0\n"
    "v_dot2_f32_bf16 v17, v182, v119, 0\n"
    "v_dot2_f32_bf16 v18, v177, v117, 0\n"
    "v_dot2_f32_bf16 v19, v183, v119, 0\n"
    "v_dot2_f32_bf16 v20, v178, v117, 0\n"
    "v_dot2_f32_bf16 v21, v184, v119, 0\n"
    "v_dot2_f32_bf16 v22, v179, v117, 0\n"
    "v_dot2_f32_bf16 v23, v185, v119, 0\n"
    "v_dot2_f32_bf16 v16, v178, v116, v16\n"
    "v_dot2_f32_bf16 v17, v184, v118, v17\n"
    "v_dot2_f32_bf16 v18, v179, v116, v18\n"
    "buffer_load_dwordx2 v[158:159], v218, s[92:95], 0 offen offset:3064\n"
    "v_dot2_f32_bf16 v19, v185, v118, v19\n"
    "v_dot2_f32_bf16 v20, v180, v116, v20\n"
    "v_dot2_f32_bf16 v21, v186, v118, v21\n"
    "v_dot2_f32_bf16 v22, v181, v116, v22\n"
    "v_dot2_f32_bf16 v23, v187, v118, v23\n"
    "v_lshlrev_b32 v200, 16, v78\n"
    "v_and_b32 v201, 0xffff0000, v78\n"
    "v_lshlrev_b32 v202, 16, v82\n"
    "v_and_b32 v203, 0xffff0000, v82\n"
    "v_lshlrev_b32 v204, 16, v86\n"
    "v_and_b32 v205, 0xffff0000, v86\n"
    "v_lshlrev_b32 v206, 16, v90\n"
    "global_load_ushort v142, v[220:221], off\n"
    "v_and_b32 v207, 0xffff0000, v90\n"
    "v_pk_mul_f32 v[16:17], v[16:17], v[200:201]\n"
    "v_pk_mul_f32 v[18:19], v[18:19], v[202:203]\n"
    "v_pk_mul_f32 v[20:21], v[20:21], v[204:205]\n"
    "v_pk_mul_f32 v[22:23], v[22:23], v[206:207]\n"
    "v_cvt_pk_bf16_f32 v208, v16, v18\n"
    "v_cvt_pk_bf16_f32 v209, v20, v22\n"
    "v_cvt_pk_bf16_f32 v210, v17, v19\n"
    "v_cvt_pk_bf16_f32 v211, v21, v23\n"
    "ds_write_b64 v160, v[208:209] offset:128\n"
    "ds_write_b64 v161, v[210:211] offset:128\n"
    "v_perm_b32 v176, v51, v55, s56\n"
    "v_perm_b32 v177, v55, v59, s56\n"
    "v_perm_b32 v178, v59, v63, s56\n"
    "v_perm_b32 v179, v63, v67, s56\n"
    "v_perm_b32 v180, v67, v71, s56\n"
    "v_perm_b32 v181, v71, v75, s56\n"
    "v_perm_b32 v182, v51, v55, s57\n"
    "v_perm_b32 v183, v55, v59, s57\n"
    "v_perm_b32 v184, v59, v63, s57\n"
    "v_perm_b32 v185, v63, v67, s57\n"
    "v_perm_b32 v186, v67, v71, s57\n"
    "v_perm_b32 v187, v71, v75, s57\n"
    "v_dot2_f32_bf16 v24, v176, v121, 0\n"
    "v_dot2_f32_bf16 v25, v182, v123, 0\n"
    "v_dot2_f32_bf16 v26, v177, v121, 0\n"
    "v_dot2_f32_bf16 v27, v183, v123, 0\n"
    "v_dot2_f32_bf16 v28, v178, v121, 0\n"
    "v_dot2_f32_bf16 v29, v184, v123, 0\n"
    "v_dot2_f32_bf16 v30, v179, v121, 0\n"
    "v_dot2_f32_bf16 v31, v185, v123, 0\n"
    "v_dot2_f32_bf16 v24, v178, v120, v24\n"
    "v_dot2_f32_bf16 v25, v184, v122, v25\n"
    "v_dot2_f32_bf16 v26, v179, v120, v26\n"
    "v_dot2_f32_bf16 v27, v185, v122, v27\n"
    "v_dot2_f32_bf16 v28, v180, v120, v28\n"
    "v_dot2_f32_bf16 v29, v186, v122, v29\n"
    "v_dot2_f32_bf16 v30, v181, v120, v30\n"
    "v_dot2_f32_bf16 v31, v187, v122, v31\n"
    "v_lshlrev_b32 v200, 16, v79\n"
    "v_and_b32 v201, 0xffff0000, v79\n"
    "v_lshlrev_b32 v202, 16, v83\n"
    "v_and_b32 v203, 0xffff0000, v83\n"
    "v_lshlrev_b32 v204, 16, v87\n"
    "v_and_b32 v205, 0xffff0000, v87\n"
    "v_lshlrev_b32 v206, 16, v91\n"
    "v_and_b32 v207, 0xffff0000, v91\n"
    "v_pk_mul_f32 v[24:25], v[24:25], v[200:201]\n"
    "v_pk_mul_f32 v[26:27], v[26:27], v[202:203]\n"
    "v_pk_mul_f32 v[28:29], v[28:29], v[204:205]\n"
    "v_pk_mul_f32 v[30:31], v[30:31], v[206:207]\n"
    "v_cvt_pk_bf16_f32 v212, v24, v26\n"
    "v_cvt_pk_bf16_f32 v213, v28, v30\n"
    "v_cvt_pk_bf16_f32 v214, v25, v27\n"
    "v_cvt_pk_bf16_f32 v215, v29, v31\n"
    "ds_write_b64 v162, v[212:213] offset:128\n"
    "ds_write_b64 v163, v[214:215] offset:128\n"
    "s_waitcnt lgkmcnt(0)\n"
    "s_barrier\n"
    "ds_read_b128 v[0:3], v166 offset:0\n"
    "ds_read_b128 v[4:7], v166 offset:4096\n"
    "ds_read_b128 v[8:11], v166 offset:8192\n"
    "ds_read_b128 v[12:15], v166 offset:12288\n"
    "s_mov_b32 s58, 1048576\n"
    "s_waitcnt lgkmcnt(3)\n"
    "buffer_store_dwordx4 v[0:3], v167, s[8:11], s58 offen sc1\n"
    "s_mov_b32 s58, 1572864\n"
    "s_waitcnt lgkmcnt(2)\n"
    "P0_H0%=:\n"
    "s_cbranch_execz P0_R%=\n"
    "buffer_store_dwordx4 v[4:7], v167, s[8:11], s58 offen sc1\n"
    "s_mov_b32 s58, 2097152\n"
    "s_waitcnt lgkmcnt(1)\n"
    "buffer_store_dwordx4 v[8:11], v167, s[8:11], s58 offen sc1\n"
    "s_mov_b32 s58, 2621440\n"
    "s_waitcnt lgkmcnt(0)\n"
    "buffer_store_dwordx4 v[12:15], v167, s[8:11], s58 offen sc1\n"
    "s_waitcnt vmcnt(0)\n"
    "s_barrier\n"
    "v_mov_b32 v0, s38\n"
    "v_mov_b32 v1, s39\n"
    "s_mov_b32 s58, 0\n"
    "buffer_store_dwordx2 v[0:1], v144, s[8:11], s58 offen sc1\n"
    "ds_write_b64 v164, v[92:93] offset:0\n"
    "ds_write_b64 v164, v[94:95] offset:128\n"
    "ds_write_b64 v164, v[96:97] offset:8\n"
    "ds_write_b64 v164, v[98:99] offset:136\n"
    "ds_write_b64 v165, v[100:101] offset:0\n"
    "ds_write_b64 v165, v[102:103] offset:128\n"
    "ds_write_b64 v165, v[104:105] offset:8\n"
    "ds_write_b64 v165, v[106:107] offset:136\n"
    "v_mov_b32 v16, 0x3f803f80\n"
    "v_dot2_f32_bf16 v0, v92, v16, 0\n"
    "v_dot2_f32_bf16 v4, v92, v92, 0\n"
    "v_dot2_f32_bf16 v1, v96, v16, 0\n"
    "v_dot2_f32_bf16 v5, v96, v96, 0\n"
    "v_dot2_f32_bf16 v2, v100, v16, 0\n"
    "v_dot2_f32_bf16 v6, v100, v100, 0\n"
    "v_dot2_f32_bf16 v3, v104, v16, 0\n"
    "v_dot2_f32_bf16 v7, v104, v104, 0\n"
    "v_dot2_f32_bf16 v0, v93, v16, v0\n"
    "v_dot2_f32_bf16 v4, v93, v93, v4\n"
    "v_dot2_f32_bf16 v1, v97, v16, v1\n"
    "v_dot2_f32_bf16 v5, v97, v97, v5\n"
    "v_dot2_f32_bf16 v2, v101, v16, v2\n"
    "v_dot2_f32_bf16 v6, v101, v101, v6\n"
    "v_dot2_f32_bf16 v3, v105, v16, v3\n"
    "v_dot2_f32_bf16 v7, v105, v105, v7\n"
    "v_dot2_f32_bf16 v0, v94, v16, v0\n"
    "v_dot2_f32_bf16 v4, v94, v94, v4\n"
    "v_dot2_f32_bf16 v1, v98, v16, v1\n"
    "v_dot2_f32_bf16 v5, v98, v98, v5\n"
    "v_dot2_f32_bf16 v2, v102, v16, v2\n"
    "v_dot2_f32_bf16 v6, v102, v102, v6\n"
    "v_dot2_f32_bf16 v3, v106, v16, v3\n"
    "v_dot2_f32_bf16 v7, v106, v106, v7\n"
    "v_dot2_f32_bf16 v0, v95, v16, v0\n"
    "v_dot2_f32_bf16 v4, v95, v95, v4\n"
    "v_dot2_f32_bf16 v1, v99, v16, v1\n"
    "v_dot2_f32_bf16 v5, v99, v99, v5\n"
    "v_dot2_f32_bf16 v2, v103, v16, v2\n"
    "v_dot2_f32_bf16 v6, v103, v103, v6\n"
    "v_dot2_f32_bf16 v3, v107, v16, v3\n"
    "v_dot2_f32_bf16 v7, v107, v107, v7\n"
    "v_permlane32_swap_b32 v0, v2\n"
    "s_nop 0\n"
    "v_permlane32_swap_b32 v1, v3\n"
    "v_permlane32_swap_b32 v4, v6\n"
    "v_permlane32_swap_b32 v5, v7\n"
    "v_add_f32 v0, v0, v2\n"
    "v_add_f32 v1, v1, v3\n"
    "v_add_f32 v4, v4, v6\n"
    "v_add_f32 v5, v5, v7\n"
    "v_permlane16_swap_b32 v0, v1\n"
    "s_nop 0\n"
    "v_permlane16_swap_b32 v4, v5\n"
    "v_add_f32 v0, v0, v1\n"
    "v_add_f32 v1, v4, v5\n"
    "s_nop 0\n"
    "v_add_f32_dpp v0, v0, v0 row_ror:8 row_mask:0xf bank_mask:0xf\n"
    "v_add_f32_dpp v1, v1, v1 row_ror:8 row_mask:0xf bank_mask:0xf\n"
    "s_nop 0\n"
    "v_add_f32_dpp v0, v0, v0 row_ror:4 row_mask:0xf bank_mask:0xf\n"
    "v_add_f32_dpp v1, v1, v1 row_ror:4 row_mask:0xf bank_mask:0xf\n"
    "s_nop 0\n"
    "v_add_f32_dpp v0, v0, v0 row_ror:2 row_mask:0xf bank_mask:0xf\n"
    "v_add_f32_dpp v1, v1, v1 row_ror:2 row_mask:0xf bank_mask:0xf\n"
    "s_nop 0\n"
    "v_add_f32_dpp v0, v0, v0 row_ror:1 row_mask:0xf bank_mask:0xf\n"
    "v_add_f32_dpp v1, v1, v1 row_ror:1 row_mask:0xf bank_mask:0xf\n"
    "buffer_store_dwordx2 v[0:1], v168, s[8:11], 0 offen sc1\n"
    "s_waitcnt lgkmcnt(0)\n"
    "s_barrier\n"
    "ds_read_b128 v[0:3], v166 offset:16384\n"
    "ds_read_b128 v[4:7], v166 offset:20480\n"
    "ds_read_b128 v[8:11], v166 offset:24576\n"
    "ds_read_b128 v[12:15], v166 offset:28672\n"
    "s_mov_b32 s58, 5242880\n"
    "s_waitcnt lgkmcnt(3)\n"
    "buffer_store_dwordx4 v[0:3], v167, s[8:11], s58 offen sc1\n"
    "s_mov_b32 s58, 5767168\n"
    "s_waitcnt lgkmcnt(2)\n"
    "buffer_store_dwordx4 v[4:7], v167, s[8:11], s58 offen sc1\n"
    "s_mov_b32 s58, 6291456\n"
    "s_waitcnt lgkmcnt(1)\n"
    "buffer_store_dwordx4 v[8:11], v167, s[8:11], s58 offen sc1\n"
    "s_mov_b32 s58, 6815744\n"
    "s_waitcnt lgkmcnt(0)\n"
    "buffer_store_dwordx4 v[12:15], v167, s[8:11], s58 offen sc1\n"
    "s_mov_b32 s56, 0xffff\n"
    "v_mov_b32 v176, 0\n"
    "v_mov_b32 v177, 0\n"
    "v_mov_b32 v178, 0\n"
    "v_mov_b32 v179, 0\n"
    "ds_write_b128 v171, v[176:179]\n"
    "ds_write_b128 v170, v[176:179]\n"
    "v_alignbit_b32 v180, v127, v127, 16\n"
    "v_alignbit_b32 v181, v126, v126, 16\n"
    "v_alignbit_b32 v182, v125, v125, 16\n"
    "v_alignbit_b32 v183, v124, v124, 16\n"
    "ds_write_b128 v169, v[180:183] offset:3072\n"
    "v_bfi_b32 v184, s56, v127, v126\n"
    "v_bfi_b32 v185, s56, v126, v125\n"
    "v_bfi_b32 v186, s56, v125, v124\n"
    "v_bfi_b32 v187, s56, v124, v153\n"
    "ds_write_b128 v169, v[184:187] offset:7232\n"
    "v_alignbit_b32 v188, v126, v126, 16\n"
    "v_alignbit_b32 v189, v125, v125, 16\n"
    "v_alignbit_b32 v190, v124, v124, 16\n"
    "v_alignbit_b32 v191, v153, v153, 16\n"
    "ds_write_b128 v169, v[188:191] offset:11392\n"
    "v_bfi_b32 v192, s56, v126, v125\n"
    "v_bfi_b32 v193, s56, v125, v124\n"
    "v_bfi_b32 v194, s56, v124, v153\n"
    "v_bfi_b32 v195, s56, v153, v152\n"
    "ds_write_b128 v169, v[192:195] offset:15552\n"
    "v_alignbit_b32 v196, v131, v131, 16\n"
    "v_alignbit_b32 v197, v130, v130, 16\n"
    "v_alignbit_b32 v198, v129, v129, 16\n"
    "v_alignbit_b32 v199, v128, v128, 16\n"
    "ds_write_b128 v169, v[196:199] offset:2048\n"
    "v_bfi_b32 v200, s56, v131, v130\n"
    "v_bfi_b32 v201, s56, v130, v129\n"
    "v_bfi_b32 v202, s56, v129, v128\n"
    "v_bfi_b32 v203, s56, v128, v155\n"
    "ds_write_b128 v169, v[200:203] offset:6208\n"
    "v_alignbit_b32 v204, v130, v130, 16\n"
    "v_alignbit_b32 v205, v129, v129, 16\n"
    "v_alignbit_b32 v206, v128, v128, 16\n"
    "v_alignbit_b32 v207, v155, v155, 16\n"
    "ds_write_b128 v169, v[204:207] offset:10368\n"
    "v_bfi_b32 v208, s56, v130, v129\n"
    "v_bfi_b32 v209, s56, v129, v128\n"
    "v_bfi_b32 v210, s56, v128, v155\n"
    "v_bfi_b32 v211, s56, v155, v154\n"
    "ds_write_b128 v169, v[208:211] offset:14528\n"
    "v_alignbit_b32 v180, v135, v135, 16\n"
    "v_alignbit_b32 v181, v134, v134, 16\n"
    "v_alignbit_b32 v182, v133, v133, 16\n"
    "v_alignbit_b32 v183, v132, v132, 16\n"
    "ds_write_b128 v169, v[180:183] offset:1024\n"
    "v_bfi_b32 v184, s56, v135, v134\n"
    "v_bfi_b32 v185, s56, v134, v133\n"
    "v_bfi_b32 v186, s56, v133, v132\n"
    "v_bfi_b32 v187, s56, v132, v157\n"
    "ds_write_b128 v169, v[184:187] offset:5184\n"
    "v_alignbit_b32 v188, v134, v134, 16\n"
    "v_alignbit_b32 v189, v133, v133, 16\n"
    "v_alignbit_b32 v190, v132, v132, 16\n"
    "v_alignbit_b32 v191, v157, v157, 16\n"
    "ds_write_b128 v169, v[188:191] offset:9344\n"
    "v_bfi_b32 v192, s56, v134, v133\n"
    "v_bfi_b32 v193, s56, v133, v132\n"
    "v_bfi_b32 v194, s56, v132, v157\n"
    "v_bfi_b32 v195, s56, v157, v156\n"
    "ds_write_b128 v169, v[192:195] offset:13504\n"
    "v_alignbit_b32 v196, v139, v139, 16\n"
    "v_alignbit_b32 v197, v138, v138, 16\n"
    "v_alignbit_b32 v198, v137, v137, 16\n"
    "v_alignbit_b32 v199, v136, v136, 16\n"
    "ds_write_b128 v169, v[196:199] offset:0\n"
    "v_bfi_b32 v200, s56, v139, v138\n"
    "v_bfi_b32 v201, s56, v138, v137\n"
    "v_bfi_b32 v202, s56, v137, v136\n"
    "v_bfi_b32 v203, s56, v136, v159\n"
    "ds_write_b128 v169, v[200:203] offset:4160\n"
    "v_alignbit_b32 v204, v138, v138, 16\n"
    "v_alignbit_b32 v205, v137, v137, 16\n"
    "v_alignbit_b32 v206, v136, v136, 16\n"
    "v_alignbit_b32 v207, v159, v159, 16\n"
    "ds_write_b128 v169, v[204:207] offset:8320\n"
    "v_bfi_b32 v208, s56, v138, v137\n"
    "v_bfi_b32 v209, s56, v137, v136\n"
    "v_bfi_b32 v210, s56, v136, v159\n"
    "v_bfi_b32 v211, s56, v159, v158\n"
    "ds_write_b128 v169, v[208:211] offset:12480\n"
    "v_mul_u32_u24 v40, v40, v142\n"
    "v_mul_u32_u24 v41, v41, v142\n"
    "v_mul_u32_u24 v42, v42, v142\n"
    "v_mul_u32_u24 v43, v43, v142\n"
    "s_cmp_lg_u32 s37, 0\n"
    "s_cbranch_scc1 S1_W%=\n"
    "s_mov_b32 s35, 0\n"
    "S1_L%=:\n"
    "buffer_load_dwordx4 v[0:3], v145, s[8:11], 0 offen sc1\n"
    "buffer_load_dwordx4 v[4:7], v145, s[8:11], 0 offen offset:1024 sc1\n"
    "s_waitcnt vmcnt(0)\n"
    "v_xor_b32 v0, s38, v0\n"
    "v_xor_b32 v1, s39, v1\n"
    "v_xor_b32 v2, s38, v2\n"
    "v_xor_b32 v3, s39, v3\n"
    "v_xor_b32 v4, s38, v4\n"
    "v_xor_b32 v5, s39, v5\n"
    "v_xor_b32 v6, s38, v6\n"
    "v_xor_b32 v7, s39, v7\n"
    "v_or3_b32 v0, v0, v1, v2\n"
    "v_or3_b32 v3, v3, v4, v5\n"
    "v_or3_b32 v0, v0, v6, v7\n"
    "v_or_b32 v0, v0, v3\n"
    "v_cmp_ne_u32 vcc, 0, v0\n"
    "s_add_u32 s35, s35, 1\n"
    "s_nop 3\n"
    "s_cbranch_vccz S1_D%=\n"
    "s_sleep 2\n"
    "s_cmp_lt_u32 s35, 0x40000\n"
    "s_cbranch_scc1 S1_L%=\n"
    "S1_D%=:\n"
    "s_branch S1_E%=\n"
    "S1_W%=:\n"
    "s_mov_b64 exec, 0\n"
    "s_cmp_eq_u32 s37, 1\n"
    "s_cbranch_scc1 S1_H0%=\n"
    "s_cmp_eq_u32 s37, 2\n"
    "s_cbranch_scc1 S1_H1%=\n"
    "s_branch S1_R%=\n"
    "S1_R%=:\n"
    "s_mov_b64 exec, -1\n"
    "S1_E%=:\n"
    "s_waitcnt vmcnt(0)\n"
    "s_barrier\n"
    "s_mov_b32 m0, s52\n"
    "s_nop 0\n"
    "buffer_load_dwordx4 v37, s[8:11], 0 offen offset:0 sc1 lds\n"
    "buffer_load_dwordx4 v37, s[8:11], 0 offen offset:1024 sc1 lds\n"
    "buffer_load_dwordx4 v37, s[8:11], 0 offen offset:2048 sc1 lds\n"
    "buffer_load_dwordx4 v37, s[8:11], 0 offen offset:3072 sc1 lds\n"
    "v_mov_b32 v0, s38\n"
    "v_mov_b32 v1, s39\n"
    "s_mov_b32 s58, 16384\n"
    "buffer_store_dwordx2 v[0:1], v144, s[8:11], s58 offen sc1\n"
    "v_mbcnt_lo_u32_b32 v208, -1, 0\n"
    "v_mbcnt_hi_u32_b32 v208, -1, v208\n"
    "v_lshrrev_b32 v209, 4, v208\n"
    "v_and_b32 v210, 15, v208\n"
    "v_bfe_u32 v211, v208, 2, 2\n"
    "v_and_b32 v212, 3, v208\n"
    "v_and_b32 v213, 7, v208\n"
    "v_lshrrev_b32 v214, 3, v208\n"
    "v_lshl_add_u32 v176, v209, 2, v211\n"
    "v_mul_u32_u24 v176, 224, v176\n"
    "v_lshl_add_u32 v176, v212, 3, v176\n"
    "s_lshr_b32 s56, s37, 1\n"
    "s_mul_i32 s56, s56, 7184\n"
    "s_and_b32 s57, s37, 1\n"
    "s_lshl_b32 s57, s57, 7\n"
    "s_add_u32 s56, s56, s57\n"
    "s_add_u32 s56, s56, s3\n"
    "s_add_u32 s56, s56, 65536\n"
    "v_add_u32 v251, s56, v176\n"
    "v_mbcnt_lo_u32_b32 v240, -1, 0\n"
    "v_mbcnt_hi_u32_b32 v240, -1, v240\n"
    "s_and_b32 s56, s37, 1\n"
    "s_lshl_b32 s56, s56, 6\n"
    "v_add_u32 v234, s56, v240\n"
    "v_add_u32 v235, 16, v234\n"
    "v_lshrrev_b32 v235, 7, v235\n"
    "s_lshr_b32 s56, s37, 1\n"
    "s_mul_i32 s56, s56, 3584\n"
    "s_mul_i32 s57, s6, 7168\n"
    "s_add_u32 s56, s56, s57\n"
    "s_mul_i32 s57, s7, 112\n"
    "s_add_u32 s56, s56, s57\n"
    "s_add_u32 s56, s56, 67584\n"
    "v_add_lshl_u32 v234, v234, s56, 3\n"
    "v_lshl_or_b32 v243, v235, 31, v234\n"
    "s_lshl_b32 s56, s37, 6\n"
    "v_add_u32 v240, s56, v240\n"
    "s_add_u32 s56, s3, 127104\n"
    "v_lshl_add_u32 v250, v240, 2, s56\n"
    "v_min_u32 v240, 223, v240\n"
    "v_add_u32 v234, 16, v240\n"
    "v_lshrrev_b32 v234, 7, v234\n"
    "v_mul_u32_u24 v234, 3472, v234\n"
    "v_add3_u32 v240, v240, v234, s57\n"
    "v_lshlrev_b32 v245, 3, v240\n"
    "v_mov_b32 v156, v243\n"
    "v_mov_b32 v152, v174\n"
    "v_mov_b32 v153, v175\n"
    "s_lshl_b32 s56, s6, 2\n"
    "s_add_u32 s56, s56, s37\n"
    "s_lshl_b32 s56, s56, 9\n"
    "v_lshl_add_u32 v177, v214, 14, s56\n"
    "v_lshl_add_u32 v224, v213, 4, v177\n"
    "s_lshr_b32 s58, s37, 1\n"
    "s_and_b32 s59, s37, 1\n"
    "s_lshl_b32 s56, s58, 13\n"
    "s_add_u32 s56, s56, s3\n"
    "v_lshl_add_u32 v229, v208, 3, s56\n"
    "s_lshl_b32 s56, s59, 7\n"
    "s_add_u32 s56, s56, s3\n"
    "s_add_u32 s56, s56, 65536\n"
    "v_add_u32 v230, s56, v176\n"
    "v_lshrrev_b32 v177, 1, v212\n"
    "v_mul_u32_u24 v177, 7168, v177\n"
    "s_lshl_b32 s56, s59, 4\n"
    "s_add_u32 s56, s56, s3\n"
    "s_add_u32 s56, s56, 65632\n"
    "v_add3_u32 v231, v176, v177, s56\n"
    "s_cmp_eq_u32 s37, 0\n"
    "s_cselect_b32 s48, s12, s20\n"
    "s_cselect_b32 s49, s13, s21\n"
    "s_cmp_lt_u32 s37, 2\n"
    "s_cselect_b32 s48, s48, s40\n"
    "s_cselect_b32 s49, s49, s41\n"
    "s_mov_b32 s50, 7340032\n"
    "s_mov_b32 s51, 0x20000\n"
    "s_cselect_b32 s56, 7, 9\n"
    "s_cselect_b32 s57, 2147483648, 0\n"
    "s_lshl_b32 s63, s56, 15\n"
    "s_mul_i32 s36, s56, s2\n"
    "s_lshl_b32 s36, s36, 7\n"
    "s_add_u32 s57, s57, s36\n"
    "v_lshl_add_u32 v177, v208, 7, s57\n"
    "s_cmp_eq_u32 s37, 3\n"
    "s_cselect_b32 s56, 0, s56\n"
    "s_sub_u32 s56, 64, s56\n"
    "v_add_u32 v178, s56, v208\n"
    "v_lshrrev_b32 v178, 6, v178\n"
    "v_lshl_or_b32 v232, v178, 31, v177\n"
    "s_lshl_b32 s56, s6, 1\n"
    "s_add_u32 s56, s56, s58\n"
    "s_lshl_b32 s57, s56, 10\n"
    "s_add_u32 s57, s57, 524288\n"
    "v_lshl_add_u32 v241, v210, 3, s57\n"
    "s_mul_i32 s56, s56, 896\n"
    "s_lshr_b32 s57, s7, 1\n"
    "s_mul_i32 s57, s57, 7\n"
    "s_add_u32 s56, s56, s57\n"
    "s_and_b32 s36, s7, 1\n"
    "s_lshl_b32 s57, s36, 2\n"
    "s_add_u32 s57, s57, s56\n"
    "v_and_b32 v177, 1, v209\n"
    "v_lshrrev_b32 v178, 1, v209\n"
    "v_lshlrev_b32 v179, 4, v210\n"
    "s_add_u32 s53, s57, s59\n"
    "s_lshl_b32 s53, s53, 10\n"
    "v_lshl_add_u32 v180, v208, 4, s53\n"
    "v_accvgpr_write_b32 a240, v180\n"
    "s_add_u32 s53, s57, 2\n"
    "s_lshl_b32 s53, s53, 1\n"
    "s_add_u32 s53, s53, s59\n"
    "s_lshl_b32 s53, s53, 9\n"
    "v_mul_u32_u24 v180, 114688, v177\n"
    "v_lshl_add_u32 v180, v178, 8, v180\n"
    "v_add3_u32 v242, v180, v179, s53\n"
    "s_add_u32 s53, s56, 3\n"
    "s_lshl_b32 s53, s53, 1\n"
    "s_add_u32 s53, s53, s36\n"
    "s_lshl_b32 s53, s53, 1\n"
    "s_add_u32 s53, s53, s59\n"
    "s_lshl_b32 s53, s53, 8\n"
    "v_lshl_add_u32 v180, v177, 1, v178\n"
    "v_mul_u32_u24 v180, 114688, v180\n"
    "v_add3_u32 v180, v180, v179, s53\n"
    "v_accvgpr_write_b32 a241, v180\n"
    "s_lshl_b32 s53, s59, 8\n"
    "s_add_u32 s53, s53, s3\n"
    "s_add_u32 s53, s53, 127104\n"
    "v_lshl_add_u32 v180, v209, 4, s53\n"
    "v_accvgpr_write_b32 a243, v180\n"
    "v_add_u32 v244, 128, v180\n"
    "s_lshl_b32 s53, s59, 5\n"
    "s_add_u32 s53, s53, s3\n"
    "s_add_u32 s53, s53, 127296\n"
    "v_lshl_add_u32 v180, v177, 4, s53\n"
    "v_accvgpr_write_b32 a242, v180\n"
    "s_lshl_b32 s56, s6, 5\n"
    "s_add_u32 s56, s56, s7\n"
    "s_lshl_b32 s56, s56, 6\n"
    "s_lshl_b32 s57, s37, 20\n"
    "s_add_u32 s57, s57, s56\n"
    "v_lshrrev_b32 v180, 2, v208\n"
    "v_lshl_add_u32 v180, v180, 14, s57\n"
    "v_lshl_add_u32 v240, v212, 4, v180\n"
    "s_add_u32 s56, s56, 524288\n"
    "v_lshl_add_u32 v180, v208, 3, s56\n"
    "v_lshrrev_b32 v181, 1, v208\n"
    "v_xor_b32 v181, s37, v181\n"
    "v_min_u32 v181, 1, v181\n"
    "v_lshl_or_b32 v243, v181, 31, v180\n"
    "v_lshrrev_b32 v180, 5, v208\n"
    "v_lshl_add_u32 v180, v180, 2, v212\n"
    "v_lshl_add_u32 v180, v180, 1, v177\n"
    "s_lshl_b32 s56, s37, 2\n"
    "s_add_u32 s56, s56, s3\n"
    "s_add_u32 s56, s56, 128128\n"
    "v_lshl_add_u32 v245, v180, 4, s56\n"
    "s_add_u32 s56, s3, 128128\n"
    "v_lshl_add_u32 v250, v213, 4, s56\n"
    "s_lshl_b32 s44, s37, 11\n"
    "s_add_u32 s44, s44, s3\n"
    "s_and_b32 s36, s4, 3\n"
    "s_mul_i32 s45, s36, 1835008\n"
    "s_mul_i32 s46, s36, 28672\n"
    "s_add_u32 s46, s46, 14680064\n"
    "s_and_b32 s60, s37, 1\n"
    "s_mul_i32 s61, s60, 229376\n"
    "s_mul_i32 s60, s60, 131072\n"
    "s_add_u32 s60, s60, s45\n"
    "s_add_u32 s61, s61, s45\n"
    "s_mov_b32 s58, 16384\n"
    "s_waitcnt vmcnt(1) lgkmcnt(0)\n"
    "s_barrier\n"
    "ds_read_b128 a[32:35], v33 offset:2048\n"
    "ds_read_b64 a[0:1], v32 offset:4032\n"
    "ds_read_b64 a[2:3], v32 offset:4040\n"
    "ds_read_b128 a[36:39], v33 offset:4096\n"
    "ds_read_b64 a[4:5], v32 offset:3520\n"
    "ds_read_b64 a[6:7], v32 offset:3528\n"
    "ds_read_b128 a[40:43], v33 offset:6144\n"
    "ds_read_b64 a[8:9], v32 offset:3008\n"
    "ds_read_b64 a[10:11], v32 offset:3016\n"
    "ds_read_b128 a[44:47], v33 offset:8192\n"
    "ds_read_b64 a[12:13], v32 offset:2496\n"
    "ds_read_b64 a[14:15], v32 offset:2504\n"
    "ds_read_b128 a[48:51], v33 offset:10240\n"
    "ds_read_b64 a[16:17], v32 offset:1984\n"
    "ds_read_b64 a[18:19], v32 offset:1992\n"
    "ds_read_b128 a[52:55], v33 offset:12288\n"
    "ds_read_b64 a[20:21], v32 offset:1472\n"
    "ds_read_b64 a[22:23], v32 offset:1480\n"
    "ds_read_b128 a[56:59], v33 offset:14336\n"
    "ds_read_b64 a[24:25], v32 offset:960\n"
    "ds_read_b64 a[26:27], v32 offset:968\n"
    "ds_read_b128 a[60:63], v33 offset:16384\n"
    "ds_read_b64 a[28:29], v32 offset:448\n"
    "ds_read_b64 a[30:31], v32 offset:456\n"
    "s_waitcnt lgkmcnt(15)\n"
    "v_mfma_f32_16x16x32_bf16 v[0:3], v[40:43], a[32:35], 0\n"
    "v_mfma_f32_16x16x32_bf16 v[0:3], a[0:3], a[32:35], v[0:3]\n"
    "s_waitcnt lgkmcnt(15)\n"
    "v_mfma_f32_16x16x32_bf16 v[4:7], v[40:43], a[36:39], 0\n"
    "v_mfma_f32_16x16x32_bf16 v[4:7], a[0:3], a[36:39], v[4:7]\n"
    "v_mfma_f32_16x16x32_bf16 v[4:7], a[4:7], a[32:35], v[4:7]\n"
    "s_waitcnt lgkmcnt(15)\n"
    "v_mfma_f32_16x16x32_bf16 v[8:11], v[40:43], a[40:43], 0\n"
    "v_mfma_f32_16x16x32_bf16 v[8:11], a[0:3], a[40:43], v[8:11]\n"
    "v_mfma_f32_16x16x32_bf16 v[8:11], a[4:7], a[36:39], v[8:11]\n"
    "v_mfma_f32_16x16x32_bf16 v[8:11], a[8:11], a[32:35], v[8:11]\n"
    "s_waitcnt lgkmcnt(12)\n"
    "v_mfma_f32_16x16x32_bf16 v[12:15], v[40:43], a[44:47], 0\n"
    "buffer_load_dwordx4 v[160:163], v145, s[8:11], s58 offen sc1\n"
    "v_mfma_f32_16x16x32_bf16 v[12:15], a[0:3], a[44:47], v[12:15]\n"
    "buffer_load_dwordx4 v[164:167], v145, s[8:11], s58 offen offset:1024 sc1\n"
    "v_mfma_f32_16x16x32_bf16 v[12:15], a[4:7], a[40:43], v[12:15]\n"
    "v_mfma_f32_16x16x32_bf16 v[12:15], a[8:11], a[36:39], v[12:15]\n"
    "v_mfma_f32_16x16x32_bf16 v[12:15], a[12:15], a[32:35], v[12:15]\n"
    "s_waitcnt lgkmcnt(9)\n"
    "v_mfma_f32_16x16x32_bf16 v[16:19], v[40:43], a[48:51], 0\n"
    "ds_read_b128 a[96:99], v36 offset:1024\n"
    "v_mfma_f32_16x16x32_bf16 v[16:19], a[0:3], a[48:51], v[16:19]\n"
    "ds_read_b64 a[64:65], v32 offset:3968\n"
    "v_mfma_f32_16x16x32_bf16 v[16:19], a[4:7], a[44:47], v[16:19]\n"
    "ds_read_b64 a[66:67], v32 offset:3976\n"
    "v_mfma_f32_16x16x32_bf16 v[16:19], a[8:11], a[40:43], v[16:19]\n"
    "ds_read_b128 a[100:103], v36 offset:3072\n"
    "v_mfma_f32_16x16x32_bf16 v[16:19], a[12:15], a[36:39], v[16:19]\n"
    "ds_read_b64 a[68:69], v32 offset:3456\n"
    "v_mfma_f32_16x16x32_bf16 v[16:19], a[16:19], a[32:35], v[16:19]\n"
    "ds_read_b64 a[70:71], v32 offset:3464\n"
    "s_waitcnt lgkmcnt(12)\n"
    "v_mfma_f32_16x16x32_bf16 v[20:23], v[40:43], a[52:55], 0\n"
    "ds_read_b128 a[104:107], v36 offset:5120\n"
    "v_mfma_f32_16x16x32_bf16 v[20:23], a[0:3], a[52:55], v[20:23]\n"
    "ds_read_b64 a[72:73], v32 offset:2944\n"
    "v_mfma_f32_16x16x32_bf16 v[20:23], a[4:7], a[48:51], v[20:23]\n"
    "ds_read_b64 a[74:75], v32 offset:2952\n"
    "v_mfma_f32_16x16x32_bf16 v[20:23], a[8:11], a[44:47], v[20:23]\n"
    "ds_read_b128 a[108:111], v36 offset:7168\n"
    "v_mfma_f32_16x16x32_bf16 v[20:23], a[12:15], a[40:43], v[20:23]\n"
    "ds_read_b64 a[76:77], v32 offset:2432\n"
    "v_mfma_f32_16x16x32_bf16 v[20:23], a[16:19], a[36:39], v[20:23]\n"
    "ds_read_b64 a[78:79], v32 offset:2440\n"
    "v_mfma_f32_16x16x32_bf16 v[20:23], a[20:23], a[32:35], v[20:23]\n"
    "ds_read_b128 a[112:115], v36 offset:9216\n"
    "s_waitcnt lgkmcnt(15)\n"
    "v_mfma_f32_16x16x32_bf16 v[24:27], v[40:43], a[56:59], 0\n"
    "ds_read_b64 a[80:81], v32 offset:1920\n"
    "v_mfma_f32_16x16x32_bf16 v[24:27], a[0:3], a[56:59], v[24:27]\n"
    "ds_read_b64 a[82:83], v32 offset:1928\n"
    "v_mfma_f32_16x16x32_bf16 v[24:27], a[4:7], a[52:55], v[24:27]\n"
    "ds_read_b128 a[116:119], v36 offset:11264\n"
    "v_mfma_f32_16x16x32_bf16 v[24:27], a[8:11], a[48:51], v[24:27]\n"
    "ds_read_b64 a[84:85], v32 offset:1408\n"
    "v_mfma_f32_16x16x32_bf16 v[24:27], a[12:15], a[44:47], v[24:27]\n"
    "ds_read_b64 a[86:87], v32 offset:1416\n"
    "v_mfma_f32_16x16x32_bf16 v[24:27], a[16:19], a[40:43], v[24:27]\n"
    "ds_read_b128 a[120:123], v36 offset:13312\n"
    "v_mfma_f32_16x16x32_bf16 v[24:27], a[20:23], a[36:39], v[24:27]\n"
    "ds_read_b64 a[88:89], v32 offset:896\n"
    "v_mfma_f32_16x16x32_bf16 v[24:27], a[24:27], a[32:35], v[24:27]\n"
    "ds_read_b64 a[90:91], v32 offset:904\n"
    "s_waitcnt lgkmcnt(15)\n"
    "v_mfma_f32_16x16x32_bf16 v[28:31], v[40:43], a[60:63], 0\n"
    "ds_read_b128 a[124:127], v36 offset:15360\n"
    "v_mfma_f32_16x16x32_bf16 v[28:31], a[0:3], a[60:63], v[28:31]\n"
    "ds_read_b64 a[92:93], v32 offset:384\n"
    "v_mfma_f32_16x16x32_bf16 v[28:31], a[4:7], a[56:59], v[28:31]\n"
    "ds_read_b64 a[94:95], v32 offset:392\n"
    "v_mfma_f32_16x16x32_bf16 v[28:31], a[8:11], a[52:55], v[28:31]\n"
    "v_mfma_f32_16x16x32_bf16 v[28:31], a[12:15], a[48:51], v[28:31]\n"
    "v_mfma_f32_16x16x32_bf16 v[28:31], a[16:19], a[44:47], v[28:31]\n"
    "v_mfma_f32_16x16x32_bf16 v[28:31], a[20:23], a[40:43], v[28:31]\n"
    "v_mfma_f32_16x16x32_bf16 v[28:31], a[24:27], a[36:39], v[28:31]\n"
    "v_mfma_f32_16x16x32_bf16 v[28:31], a[28:31], a[32:35], v[28:31]\n"
    "s_waitcnt vmcnt(0) lgkmcnt(0)\n"
    "s_mov_b32 s35, 0\n"
    "SB_L%=:\n"
    "v_xor_b32 v160, s38, v160\n"
    "v_xor_b32 v161, s39, v161\n"
    "v_xor_b32 v162, s38, v162\n"
    "v_xor_b32 v163, s39, v163\n"
    "v_xor_b32 v164, s38, v164\n"
    "v_xor_b32 v165, s39, v165\n"
    "v_xor_b32 v166, s38, v166\n"
    "v_xor_b32 v167, s39, v167\n"
    "v_or3_b32 v160, v160, v161, v162\n"
    "v_or3_b32 v163, v163, v164, v165\n"
    "v_or3_b32 v160, v160, v166, v167\n"
    "v_or_b32 v160, v160, v163\n"
    "v_cmp_ne_u32 vcc, 0, v160\n"
    "s_add_u32 s35, s35, 1\n"
    "s_nop 3\n"
    "s_cbranch_vccz SB_E%=\n"
    "s_cmp_lt_u32 s35, 0x40000\n"
    "s_cbranch_scc0 SB_E%=\n"
    "buffer_load_dwordx4 v[160:163], v145, s[8:11], s58 offen sc1\n"
    "buffer_load_dwordx4 v[164:167], v145, s[8:11], s58 offen offset:1024 sc1\n"
    "s_waitcnt vmcnt(0)\n"
    "s_branch SB_L%=\n"
    "SB_E%=:\n"
    "s_waitcnt lgkmcnt(0)\n"
    "v_mfma_f32_16x16x32_bf16 v[0:3], a[64:67], a[96:99], v[0:3]\n"
    "ds_read_b128 a[32:35], v35 offset:1024\n"
    "s_mov_b32 s53, 0\n"
    "v_mfma_f32_16x16x32_bf16 v[4:7], a[64:67], a[100:103], v[4:7]\n"
    "ds_read_b64 a[0:1], v32 offset:3904\n"
    "buffer_load_dwordx4 v[48:51], v38, s[8:11], s53 offen sc1\n"
    "v_mfma_f32_16x16x32_bf16 v[4:7], a[68:71], a[96:99], v[4:7]\n"
    "ds_read_b64 a[2:3], v32 offset:3912\n"
    "v_mfma_f32_16x16x32_bf16 v[8:11], a[64:67], a[104:107], v[8:11]\n"
    "v_mfma_f32_16x16x32_bf16 v[8:11], a[68:71], a[100:103], v[8:11]\n"
    "ds_read_b128 a[36:39], v35 offset:3072\n"
    "s_mov_b32 s53, 0\n"
    "v_mfma_f32_16x16x32_bf16 v[8:11], a[72:75], a[96:99], v[8:11]\n"
    "ds_read_b64 a[4:5], v32 offset:3392\n"
    "buffer_load_dwordx4 v[64:67], v39, s[8:11], s53 offen sc1\n"
    "v_mfma_f32_16x16x32_bf16 v[12:15], a[64:67], a[108:111], v[12:15]\n"
    "ds_read_b64 a[6:7], v32 offset:3400\n"
    "v_mfma_f32_16x16x32_bf16 v[12:15], a[68:71], a[104:107], v[12:15]\n"
    "v_mfma_f32_16x16x32_bf16 v[12:15], a[72:75], a[100:103], v[12:15]\n"
    "ds_read_b128 a[40:43], v35 offset:5120\n"
    "buffer_load_dwordx4 v[68:71], v39, s[8:11], s53 offen offset:16 sc1\n"
    "v_mfma_f32_16x16x32_bf16 v[12:15], a[76:79], a[96:99], v[12:15]\n"
    "ds_read_b64 a[8:9], v32 offset:2880\n"
    "v_mfma_f32_16x16x32_bf16 v[16:19], a[64:67], a[112:115], v[16:19]\n"
    "ds_read_b64 a[10:11], v32 offset:2888\n"
    "v_mfma_f32_16x16x32_bf16 v[16:19], a[68:71], a[108:111], v[16:19]\n"
    "v_mfma_f32_16x16x32_bf16 v[16:19], a[72:75], a[104:107], v[16:19]\n"
    "ds_read_b128 a[44:47], v35 offset:7168\n"
    "s_mov_b32 s53, 4096\n"
    "v_mfma_f32_16x16x32_bf16 v[16:19], a[76:79], a[100:103], v[16:19]\n"
    "ds_read_b64 a[12:13], v32 offset:2368\n"
    "buffer_load_dwordx4 v[52:55], v38, s[8:11], s53 offen sc1\n"
    "v_mfma_f32_16x16x32_bf16 v[16:19], a[80:83], a[96:99], v[16:19]\n"
    "ds_read_b64 a[14:15], v32 offset:2376\n"
    "v_mfma_f32_16x16x32_bf16 v[20:23], a[64:67], a[116:119], v[20:23]\n"
    "v_mfma_f32_16x16x32_bf16 v[20:23], a[68:71], a[112:115], v[20:23]\n"
    "ds_read_b128 a[48:51], v35 offset:9216\n"
    "v_mfma_f32_16x16x32_bf16 v[20:23], a[72:75], a[108:111], v[20:23]\n"
    "ds_read_b64 a[16:17], v32 offset:1856\n"
    "s_mov_b32 s53, 8192\n"
    "v_mfma_f32_16x16x32_bf16 v[20:23], a[76:79], a[104:107], v[20:23]\n"
    "ds_read_b64 a[18:19], v32 offset:1864\n"
    "buffer_load_dwordx4 v[72:75], v39, s[8:11], s53 offen sc1\n"
    "v_mfma_f32_16x16x32_bf16 v[20:23], a[80:83], a[100:103], v[20:23]\n"
    "v_mfma_f32_16x16x32_bf16 v[20:23], a[84:87], a[96:99], v[20:23]\n"
    "ds_read_b128 a[52:55], v35 offset:11264\n"
    "v_mfma_f32_16x16x32_bf16 v[24:27], a[64:67], a[120:123], v[24:27]\n"
    "ds_read_b64 a[20:21], v32 offset:1344\n"
    "buffer_load_dwordx4 v[76:79], v39, s[8:11], s53 offen offset:16 sc1\n"
    "v_mfma_f32_16x16x32_bf16 v[24:27], a[68:71], a[116:119], v[24:27]\n"
    "ds_read_b64 a[22:23], v32 offset:1352\n"
    "v_mfma_f32_16x16x32_bf16 v[24:27], a[72:75], a[112:115], v[24:27]\n"
    "v_mfma_f32_16x16x32_bf16 v[24:27], a[76:79], a[108:111], v[24:27]\n"
    "ds_read_b128 a[56:59], v35 offset:13312\n"
    "v_mfma_f32_16x16x32_bf16 v[24:27], a[80:83], a[104:107], v[24:27]\n"
    "ds_read_b64 a[24:25], v32 offset:832\n"
    "s_mov_b32 s53, 8192\n"
    "v_mfma_f32_16x16x32_bf16 v[24:27], a[84:87], a[100:103], v[24:27]\n"
    "ds_read_b64 a[26:27], v32 offset:840\n"
    "buffer_load_dwordx4 v[56:59], v38, s[8:11], s53 offen sc1\n"
    "v_mfma_f32_16x16x32_bf16 v[24:27], a[88:91], a[96:99], v[24:27]\n"
    "v_mfma_f32_16x16x32_bf16 v[28:31], a[64:67], a[124:127], v[28:31]\n"
    "ds_read_b128 a[60:63], v35 offset:15360\n"
    "v_mfma_f32_16x16x32_bf16 v[28:31], a[68:71], a[120:123], v[28:31]\n"
    "ds_read_b64 a[28:29], v32 offset:320\n"
    "s_mov_b32 s53, 16384\n"
    "v_mfma_f32_16x16x32_bf16 v[28:31], a[72:75], a[116:119], v[28:31]\n"
    "ds_read_b64 a[30:31], v32 offset:328\n"
    "buffer_load_dwordx4 v[80:83], v39, s[8:11], s53 offen sc1\n"
    "v_mfma_f32_16x16x32_bf16 v[28:31], a[76:79], a[112:115], v[28:31]\n"
    "v_mfma_f32_16x16x32_bf16 v[28:31], a[80:83], a[108:111], v[28:31]\n"
    "v_mfma_f32_16x16x32_bf16 v[28:31], a[84:87], a[104:107], v[28:31]\n"
    "v_mfma_f32_16x16x32_bf16 v[28:31], a[88:91], a[100:103], v[28:31]\n"
    "v_mfma_f32_16x16x32_bf16 v[28:31], a[92:95], a[96:99], v[28:31]\n"
    "s_waitcnt lgkmcnt(0)\n"
    "v_mfma_f32_16x16x32_bf16 v[0:3], a[0:3], a[32:35], v[0:3]\n"
    "ds_read_b128 a[96:99], v34 offset:1024\n"
    "buffer_load_dwordx4 v[84:87], v39, s[8:11], s53 offen offset:16 sc1\n"
    "v_mfma_f32_16x16x32_bf16 v[4:7], a[0:3], a[36:39], v[4:7]\n"
    "ds_read_b64 a[64:65], v32 offset:3840\n"
    "S1_H0%=:\n"
    "s_cbranch_execz S1_R%=\n"
    "v_mfma_f32_16x16x32_bf16 v[4:7], a[4:7], a[32:35], v[4:7]\n"
    "ds_read_b64 a[66:67], v32 offset:3848\n"
    "v_mfma_f32_16x16x32_bf16 v[8:11], a[0:3], a[40:43], v[8:11]\n"
    "v_mfma_f32_16x16x32_bf16 v[8:11], a[4:7], a[36:39], v[8:11]\n"
    "ds_read_b128 a[100:103], v34 offset:3072\n"
    "s_mov_b32 s53, 12288\n"
    "v_mfma_f32_16x16x32_bf16 v[8:11], a[8:11], a[32:35], v[8:11]\n"
    "ds_read_b64 a[68:69], v32 offset:3328\n"
    "buffer_load_dwordx4 v[60:63], v38, s[8:11], s53 offen sc1\n"
    "v_mfma_f32_16x16x32_bf16 v[12:15], a[0:3], a[44:47], v[12:15]\n"
    "ds_read_b64 a[70:71], v32 offset:3336\n"
    "v_mfma_f32_16x16x32_bf16 v[12:15], a[4:7], a[40:43], v[12:15]\n"
    "v_mfma_f32_16x16x32_bf16 v[12:15], a[8:11], a[36:39], v[12:15]\n"
    "ds_read_b128 a[104:107], v34 offset:5120\n"
    "s_mov_b32 s53, 24576\n"
    "v_mfma_f32_16x16x32_bf16 v[12:15], a[12:15], a[32:35], v[12:15]\n"
    "ds_read_b64 a[72:73], v32 offset:2816\n"
    "buffer_load_dwordx4 v[88:91], v39, s[8:11], s53 offen sc1\n"
    "v_mfma_f32_16x16x32_bf16 v[16:19], a[0:3], a[48:51], v[16:19]\n"
    "ds_read_b64 a[74:75], v32 offset:2824\n"
    "v_mfma_f32_16x16x32_bf16 v[16:19], a[4:7], a[44:47], v[16:19]\n"
    "v_mfma_f32_16x16x32_bf16 v[16:19], a[8:11], a[40:43], v[16:19]\n"
    "ds_read_b128 a[108:111], v34 offset:7168\n"
    "buffer_load_dwordx4 v[92:95], v39, s[8:11], s53 offen offset:16 sc1\n"
    "v_mfma_f32_16x16x32_bf16 v[16:19], a[12:15], a[36:39], v[16:19]\n"
    "ds_read_b64 a[76:77], v32 offset:2304\n"
    "v_mfma_f32_16x16x32_bf16 v[16:19], a[16:19], a[32:35], v[16:19]\n"
    "ds_read_b64 a[78:79], v32 offset:2312\n"
    "v_mfma_f32_16x16x32_bf16 v[20:23], a[0:3], a[52:55], v[20:23]\n"
    "v_mfma_f32_16x16x32_bf16 v[20:23], a[4:7], a[48:51], v[20:23]\n"
    "ds_read_b128 a[112:115], v34 offset:9216\n"
    "s_add_u32 m0, s29, 0\n"
    "v_mfma_f32_16x16x32_bf16 v[20:23], a[8:11], a[44:47], v[20:23]\n"
    "ds_read_b64 a[80:81], v32 offset:1792\n"
    "buffer_load_dwordx4 v225, s[12:15], s34 offen lds\n"
    "v_mfma_f32_16x16x32_bf16 v[20:23], a[12:15], a[40:43], v[20:23]\n"
    "ds_read_b64 a[82:83], v32 offset:1800\n"
    "v_mfma_f32_16x16x32_bf16 v[20:23], a[16:19], a[36:39], v[20:23]\n"
    "v_mfma_f32_16x16x32_bf16 v[20:23], a[20:23], a[32:35], v[20:23]\n"
    "ds_read_b128 a[116:119], v34 offset:11264\n"
    "s_add_u32 m0, s30, 0\n"
    "v_mfma_f32_16x16x32_bf16 v[24:27], a[0:3], a[56:59], v[24:27]\n"
    "ds_read_b64 a[84:85], v32 offset:1280\n"
    "buffer_load_dwordx4 v226, s[16:19], s34 offen lds\n"
    "v_mfma_f32_16x16x32_bf16 v[24:27], a[4:7], a[52:55], v[24:27]\n"
    "ds_read_b64 a[86:87], v32 offset:1288\n"
    "v_mfma_f32_16x16x32_bf16 v[24:27], a[8:11], a[48:51], v[24:27]\n"
    "v_mfma_f32_16x16x32_bf16 v[24:27], a[12:15], a[44:47], v[24:27]\n"
    "ds_read_b128 a[120:123], v34 offset:13312\n"
    "s_add_u32 m0, s31, 0\n"
    "v_mfma_f32_16x16x32_bf16 v[24:27], a[16:19], a[40:43], v[24:27]\n"
    "ds_read_b64 a[88:89], v32 offset:768\n"
    "buffer_load_dwordx4 v227, s[20:23], s34 offen lds\n"
    "v_mfma_f32_16x16x32_bf16 v[24:27], a[20:23], a[36:39], v[24:27]\n"
    "ds_read_b64 a[90:91], v32 offset:776\n"
    "v_mfma_f32_16x16x32_bf16 v[24:27], a[24:27], a[32:35], v[24:27]\n"
    "v_mfma_f32_16x16x32_bf16 v[28:31], a[0:3], a[60:63], v[28:31]\n"
    "ds_read_b128 a[124:127], v34 offset:15360\n"
    "s_add_u32 m0, s32, 0\n"
    "v_mfma_f32_16x16x32_bf16 v[28:31], a[4:7], a[56:59], v[28:31]\n"
    "ds_read_b64 a[92:93], v32 offset:256\n"
    "buffer_load_dwordx4 v228, s[24:27], s34 offen lds\n"
    "v_mfma_f32_16x16x32_bf16 v[28:31], a[8:11], a[52:55], v[28:31]\n"
    "ds_read_b64 a[94:95], v32 offset:264\n"
    "s_add_u32 s34, s34, 229376\n"
    "v_mfma_f32_16x16x32_bf16 v[28:31], a[12:15], a[48:51], v[28:31]\n"
    "v_mfma_f32_16x16x32_bf16 v[28:31], a[16:19], a[44:47], v[28:31]\n"
    "v_mfma_f32_16x16x32_bf16 v[28:31], a[20:23], a[40:43], v[28:31]\n"
    "v_mfma_f32_16x16x32_bf16 v[28:31], a[24:27], a[36:39], v[28:31]\n"
    "v_mfma_f32_16x16x32_bf16 v[28:31], a[28:31], a[32:35], v[28:31]\n"
    "s_waitcnt lgkmcnt(0)\n"
    "v_mfma_f32_16x16x32_bf16 v[0:3], a[64:67], a[96:99], v[0:3]\n"
    "ds_read_b128 a[32:35], v33 offset:1024\n"
    "s_add_u32 m0, s29, 15392\n"
    "v_mfma_f32_16x16x32_bf16 v[4:7], a[64:67], a[100:103], v[4:7]\n"
    "ds_read_b64 a[0:1], v32 offset:3776\n"
    "buffer_load_dwordx4 v225, s[12:15], s34 offen lds\n"
    "v_mfma_f32_16x16x32_bf16 v[4:7], a[68:71], a[96:99], v[4:7]\n"
    "ds_read_b64 a[2:3], v32 offset:3784\n"
    "v_mfma_f32_16x16x32_bf16 v[8:11], a[64:67], a[104:107], v[8:11]\n"
    "v_mfma_f32_16x16x32_bf16 v[8:11], a[68:71], a[100:103], v[8:11]\n"
    "ds_read_b128 a[36:39], v33 offset:3072\n"
    "v_mfma_f32_16x16x32_bf16 v[8:11], a[72:75], a[96:99], v[8:11]\n"
    "ds_read_b64 a[4:5], v32 offset:3264\n"
    "s_add_u32 m0, s30, 15392\n"
    "v_mfma_f32_16x16x32_bf16 v[12:15], a[64:67], a[108:111], v[12:15]\n"
    "ds_read_b64 a[6:7], v32 offset:3272\n"
    "buffer_load_dwordx4 v226, s[16:19], s34 offen lds\n"
    "v_mfma_f32_16x16x32_bf16 v[12:15], a[68:71], a[104:107], v[12:15]\n"
    "v_mfma_f32_16x16x32_bf16 v[12:15], a[72:75], a[100:103], v[12:15]\n"
    "ds_read_b128 a[40:43], v33 offset:5120\n"
    "v_mfma_f32_16x16x32_bf16 v[12:15], a[76:79], a[96:99], v[12:15]\n"
    "ds_read_b64 a[8:9], v32 offset:2752\n"
    "v_mfma_f32_16x16x32_bf16 v[16:19], a[64:67], a[112:115], v[16:19]\n"
    "ds_read_b64 a[10:11], v32 offset:2760\n"
    "v_mfma_f32_16x16x32_bf16 v[16:19], a[68:71], a[108:111], v[16:19]\n"
    "s_add_u32 m0, s31, 15392\n"
    "v_mfma_f32_16x16x32_bf16 v[16:19], a[72:75], a[104:107], v[16:19]\n"
    "ds_read_b128 a[44:47], v33 offset:7168\n"
    "buffer_load_dwordx4 v227, s[20:23], s34 offen lds\n"
    "v_mfma_f32_16x16x32_bf16 v[16:19], a[76:79], a[100:103], v[16:19]\n"
    "ds_read_b64 a[12:13], v32 offset:2240\n"
    "v_mfma_f32_16x16x32_bf16 v[16:19], a[80:83], a[96:99], v[16:19]\n"
    "ds_read_b64 a[14:15], v32 offset:2248\n"
    "v_mfma_f32_16x16x32_bf16 v[20:23], a[64:67], a[116:119], v[20:23]\n"
    "v_mfma_f32_16x16x32_bf16 v[20:23], a[68:71], a[112:115], v[20:23]\n"
    "ds_read_b128 a[48:51], v33 offset:9216\n"
    "s_add_u32 m0, s32, 15392\n"
    "v_mfma_f32_16x16x32_bf16 v[20:23], a[72:75], a[108:111], v[20:23]\n"
    "ds_read_b64 a[16:17], v32 offset:1728\n"
    "buffer_load_dwordx4 v228, s[24:27], s34 offen lds\n"
    "v_mfma_f32_16x16x32_bf16 v[20:23], a[76:79], a[104:107], v[20:23]\n"
    "ds_read_b64 a[18:19], v32 offset:1736\n"
    "s_add_u32 s34, s34, 229376\n"
    "v_mfma_f32_16x16x32_bf16 v[20:23], a[80:83], a[100:103], v[20:23]\n"
    "v_mfma_f32_16x16x32_bf16 v[20:23], a[84:87], a[96:99], v[20:23]\n"
    "ds_read_b128 a[52:55], v33 offset:11264\n"
    "v_mfma_f32_16x16x32_bf16 v[24:27], a[64:67], a[120:123], v[24:27]\n"
    "ds_read_b64 a[20:21], v32 offset:1216\n"
    "v_mfma_f32_16x16x32_bf16 v[24:27], a[68:71], a[116:119], v[24:27]\n"
    "ds_read_b64 a[22:23], v32 offset:1224\n"
    "s_add_u32 m0, s29, 30784\n"
    "v_mfma_f32_16x16x32_bf16 v[24:27], a[72:75], a[112:115], v[24:27]\n"
    "buffer_load_dwordx4 v225, s[12:15], s34 offen lds\n"
    "v_mfma_f32_16x16x32_bf16 v[24:27], a[76:79], a[108:111], v[24:27]\n"
    "ds_read_b128 a[56:59], v33 offset:13312\n"
    "v_mfma_f32_16x16x32_bf16 v[24:27], a[80:83], a[104:107], v[24:27]\n"
    "ds_read_b64 a[24:25], v32 offset:704\n"
    "v_mfma_f32_16x16x32_bf16 v[24:27], a[84:87], a[100:103], v[24:27]\n"
    "ds_read_b64 a[26:27], v32 offset:712\n"
    "v_mfma_f32_16x16x32_bf16 v[24:27], a[88:91], a[96:99], v[24:27]\n"
    "s_add_u32 m0, s30, 30784\n"
    "v_mfma_f32_16x16x32_bf16 v[28:31], a[64:67], a[124:127], v[28:31]\n"
    "ds_read_b128 a[60:63], v33 offset:15360\n"
    "buffer_load_dwordx4 v226, s[16:19], s34 offen lds\n"
    "v_mfma_f32_16x16x32_bf16 v[28:31], a[68:71], a[120:123], v[28:31]\n"
    "ds_read_b64 a[28:29], v32 offset:192\n"
    "v_mfma_f32_16x16x32_bf16 v[28:31], a[72:75], a[116:119], v[28:31]\n"
    "ds_read_b64 a[30:31], v32 offset:200\n"
    "v_mfma_f32_16x16x32_bf16 v[28:31], a[76:79], a[112:115], v[28:31]\n"
    "v_mfma_f32_16x16x32_bf16 v[28:31], a[80:83], a[108:111], v[28:31]\n"
    "v_mfma_f32_16x16x32_bf16 v[28:31], a[84:87], a[104:107], v[28:31]\n"
    "v_mfma_f32_16x16x32_bf16 v[28:31], a[88:91], a[100:103], v[28:31]\n"
    "v_mfma_f32_16x16x32_bf16 v[28:31], a[92:95], a[96:99], v[28:31]\n"
    "s_waitcnt vmcnt(10) lgkmcnt(0)\n"
    "v_mfma_f32_16x16x32_bf16 v[0:3], a[0:3], a[32:35], v[0:3]\n"
    "ds_read_b128 a[96:99], v36 offset:0\n"
    "s_add_u32 m0, s31, 30784\n"
    "v_mfma_f32_16x16x32_bf16 v[4:7], a[0:3], a[36:39], v[4:7]\n"
    "ds_read_b64 a[64:65], v32 offset:3712\n"
    "buffer_load_dwordx4 v227, s[20:23], s34 offen lds\n"
    "v_mfma_f32_16x16x32_bf16 v[4:7], a[4:7], a[32:35], v[4:7]\n"
    "ds_read_b64 a[66:67], v32 offset:3720\n"
    "v_add_f32 v64, v64, v66\n"
    "v_mfma_f32_16x16x32_bf16 v[8:11], a[0:3], a[40:43], v[8:11]\n"
    "v_add_f32 v65, v65, v67\n"
    "v_mfma_f32_16x16x32_bf16 v[8:11], a[4:7], a[36:39], v[8:11]\n"
    "ds_read_b128 a[100:103], v36 offset:2048\n"
    "v_mul_f32 v66, 0xba800000, v64\n"
    "v_mfma_f32_16x16x32_bf16 v[8:11], a[8:11], a[32:35], v[8:11]\n"
    "ds_read_b64 a[68:69], v32 offset:3200\n"
    "s_add_u32 m0, s32, 30784\n"
    "v_mfma_f32_16x16x32_bf16 v[12:15], a[0:3], a[44:47], v[12:15]\n"
    "ds_read_b64 a[70:71], v32 offset:3208\n"
    "buffer_load_dwordx4 v228, s[24:27], s34 offen lds\n"
    "v_mfma_f32_16x16x32_bf16 v[12:15], a[4:7], a[40:43], v[12:15]\n"
    "s_add_u32 s34, s34, 229376\n"
    "v_mul_f32 v65, 0x3a800000, v65\n"
    "v_mfma_f32_16x16x32_bf16 v[12:15], a[8:11], a[36:39], v[12:15]\n"
    "ds_read_b128 a[104:107], v36 offset:4096\n"
    "v_mfma_f32_16x16x32_bf16 v[12:15], a[12:15], a[32:35], v[12:15]\n"
    "ds_read_b64 a[72:73], v32 offset:2688\n"
    "v_fma_f32 v65, -v66, v66, v65\n"
    "v_mfma_f32_16x16x32_bf16 v[16:19], a[0:3], a[48:51], v[16:19]\n"
    "ds_read_b64 a[74:75], v32 offset:2696\n"
    "v_mfma_f32_16x16x32_bf16 v[16:19], a[4:7], a[44:47], v[16:19]\n"
    "s_nop 0\n"
    "v_max_f32 v65, 0, v65\n"
    "v_mfma_f32_16x16x32_bf16 v[16:19], a[8:11], a[40:43], v[16:19]\n"
    "ds_read_b128 a[108:111], v36 offset:6144\n"
    "v_add_f32 v65, 0x3727c5ac, v65\n"
    "v_mfma_f32_16x16x32_bf16 v[16:19], a[12:15], a[36:39], v[16:19]\n"
    "ds_read_b64 a[76:77], v32 offset:2176\n"
    "v_mfma_f32_16x16x32_bf16 v[16:19], a[16:19], a[32:35], v[16:19]\n"
    "ds_read_b64 a[78:79], v32 offset:2184\n"
    "v_rsq_f32 v64, v65\n"
    "v_mfma_f32_16x16x32_bf16 v[20:23], a[0:3], a[52:55], v[20:23]\n"
    "v_lshlrev_b32 v160, 16, v48\n"
    "v_mfma_f32_16x16x32_bf16 v[20:23], a[4:7], a[48:51], v[20:23]\n"
    "ds_read_b128 a[112:115], v36 offset:8192\n"
    "s_nop 0\n"
    "v_mfma_f32_16x16x32_bf16 v[20:23], a[8:11], a[44:47], v[20:23]\n"
    "ds_read_b64 a[80:81], v32 offset:1664\n"
    "v_and_b32 v161, 0xffff0000, v48\n"
    "v_mfma_f32_16x16x32_bf16 v[20:23], a[12:15], a[40:43], v[20:23]\n"
    "ds_read_b64 a[82:83], v32 offset:1672\n"
    "v_mfma_f32_16x16x32_bf16 v[20:23], a[16:19], a[36:39], v[20:23]\n"
    "v_lshlrev_b32 v162, 16, v49\n"
    "v_and_b32 v163, 0xffff0000, v49\n"
    "v_mfma_f32_16x16x32_bf16 v[20:23], a[20:23], a[32:35], v[20:23]\n"
    "ds_read_b128 a[116:119], v36 offset:10240\n"
    "v_mfma_f32_16x16x32_bf16 v[24:27], a[0:3], a[56:59], v[24:27]\n"
    "ds_read_b64 a[84:85], v32 offset:1152\n"
    "v_mul_f32 v66, v66, v64\n"
    "v_mfma_f32_16x16x32_bf16 v[24:27], a[4:7], a[52:55], v[24:27]\n"
    "ds_read_b64 a[86:87], v32 offset:1160\n"
    "s_nop 0\n"
    "v_mfma_f32_16x16x32_bf16 v[24:27], a[8:11], a[48:51], v[24:27]\n"
    "v_fma_f32 v192, v160, v64, v66\n"
    "v_mfma_f32_16x16x32_bf16 v[24:27], a[12:15], a[44:47], v[24:27]\n"
    "ds_read_b128 a[120:123], v36 offset:12288\n"
    "v_fma_f32 v193, v161, v64, v66\n"
    "v_mfma_f32_16x16x32_bf16 v[24:27], a[16:19], a[40:43], v[24:27]\n"
    "ds_read_b64 a[88:89], v32 offset:640\n"
    "v_mfma_f32_16x16x32_bf16 v[24:27], a[20:23], a[36:39], v[24:27]\n"
    "ds_read_b64 a[90:91], v32 offset:648\n"
    "v_fma_f32 v194, v162, v64, v66\n"
    "v_mfma_f32_16x16x32_bf16 v[24:27], a[24:27], a[32:35], v[24:27]\n"
    "s_nop 0\n"
    "v_mfma_f32_16x16x32_bf16 v[28:31], a[0:3], a[60:63], v[28:31]\n"
    "ds_read_b128 a[124:127], v36 offset:14336\n"
    "v_fma_f32 v195, v163, v64, v66\n"
    "v_mfma_f32_16x16x32_bf16 v[28:31], a[4:7], a[56:59], v[28:31]\n"
    "ds_read_b64 a[92:93], v32 offset:128\n"
    "v_add_f32 v68, v68, v70\n"
    "v_mfma_f32_16x16x32_bf16 v[28:31], a[8:11], a[52:55], v[28:31]\n"
    "ds_read_b64 a[94:95], v32 offset:136\n"
    "v_mfma_f32_16x16x32_bf16 v[28:31], a[12:15], a[48:51], v[28:31]\n"
    "v_add_f32 v69, v69, v71\n"
    "v_mul_f32 v70, 0xba800000, v68\n"
    "v_mfma_f32_16x16x32_bf16 v[28:31], a[16:19], a[44:47], v[28:31]\n"
    "v_mul_f32 v69, 0x3a800000, v69\n"
    "v_mfma_f32_16x16x32_bf16 v[28:31], a[20:23], a[40:43], v[28:31]\n"
    "v_fma_f32 v69, -v70, v70, v69\n"
    "v_mfma_f32_16x16x32_bf16 v[28:31], a[24:27], a[36:39], v[28:31]\n"
    "v_max_f32 v69, 0, v69\n"
    "v_mfma_f32_16x16x32_bf16 v[28:31], a[28:31], a[32:35], v[28:31]\n"
    "v_add_f32 v69, 0x3727c5ac, v69\n"
    "s_waitcnt lgkmcnt(0)\n"
    "v_mfma_f32_16x16x32_bf16 v[0:3], a[64:67], a[96:99], v[0:3]\n"
    "ds_read_b128 a[32:35], v35 offset:0\n"
    "v_rsq_f32 v68, v69\n"
    "v_mfma_f32_16x16x32_bf16 v[4:7], a[64:67], a[100:103], v[4:7]\n"
    "ds_read_b64 a[0:1], v32 offset:3648\n"
    "v_lshlrev_b32 v164, 16, v50\n"
    "v_mfma_f32_16x16x32_bf16 v[4:7], a[68:71], a[96:99], v[4:7]\n"
    "ds_read_b64 a[2:3], v32 offset:3656\n"
    "v_and_b32 v165, 0xffff0000, v50\n"
    "v_mfma_f32_16x16x32_bf16 v[8:11], a[64:67], a[104:107], v[8:11]\n"
    "v_lshlrev_b32 v166, 16, v51\n"
    "v_mfma_f32_16x16x32_bf16 v[8:11], a[68:71], a[100:103], v[8:11]\n"
    "ds_read_b128 a[36:39], v35 offset:2048\n"
    "v_and_b32 v167, 0xffff0000, v51\n"
    "v_mfma_f32_16x16x32_bf16 v[8:11], a[72:75], a[96:99], v[8:11]\n"
    "ds_read_b64 a[4:5], v32 offset:3136\n"
    "v_mfma_f32_16x16x32_bf16 v[12:15], a[64:67], a[108:111], v[12:15]\n"
    "ds_read_b64 a[6:7], v32 offset:3144\n"
    "v_mul_f32 v70, v70, v68\n"
    "v_mfma_f32_16x16x32_bf16 v[12:15], a[68:71], a[104:107], v[12:15]\n"
    "v_fma_f32 v196, v164, v68, v70\n"
    "v_fma_f32 v197, v165, v68, v70\n"
    "v_mfma_f32_16x16x32_bf16 v[12:15], a[72:75], a[100:103], v[12:15]\n"
    "ds_read_b128 a[40:43], v35 offset:4096\n"
    "v_mfma_f32_16x16x32_bf16 v[12:15], a[76:79], a[96:99], v[12:15]\n"
    "ds_read_b64 a[8:9], v32 offset:2624\n"
    "v_fma_f32 v198, v166, v68, v70\n"
    "v_mfma_f32_16x16x32_bf16 v[16:19], a[64:67], a[112:115], v[16:19]\n"
    "ds_read_b64 a[10:11], v32 offset:2632\n"
    "v_fma_f32 v199, v167, v68, v70\n"
    "v_mfma_f32_16x16x32_bf16 v[16:19], a[68:71], a[108:111], v[16:19]\n"
    "v_add_f32 v72, v72, v74\n"
    "v_mfma_f32_16x16x32_bf16 v[16:19], a[72:75], a[104:107], v[16:19]\n"
    "ds_read_b128 a[44:47], v35 offset:6144\n"
    "v_add_f32 v73, v73, v75\n"
    "v_mfma_f32_16x16x32_bf16 v[16:19], a[76:79], a[100:103], v[16:19]\n"
    "ds_read_b64 a[12:13], v32 offset:2112\n"
    "v_mul_f32 v74, 0xba800000, v72\n"
    "v_mfma_f32_16x16x32_bf16 v[16:19], a[80:83], a[96:99], v[16:19]\n"
    "ds_read_b64 a[14:15], v32 offset:2120\n"
    "v_mfma_f32_16x16x32_bf16 v[20:23], a[64:67], a[116:119], v[20:23]\n"
    "v_mul_f32 v73, 0x3a800000, v73\n"
    "v_fma_f32 v73, -v74, v74, v73\n"
    "v_mfma_f32_16x16x32_bf16 v[20:23], a[68:71], a[112:115], v[20:23]\n"
    "ds_read_b128 a[48:51], v35 offset:8192\n"
    "v_mfma_f32_16x16x32_bf16 v[20:23], a[72:75], a[108:111], v[20:23]\n"
    "ds_read_b64 a[16:17], v32 offset:1600\n"
    "v_max_f32 v73, 0, v73\n"
    "v_mfma_f32_16x16x32_bf16 v[20:23], a[76:79], a[104:107], v[20:23]\n"
    "ds_read_b64 a[18:19], v32 offset:1608\n"
    "v_add_f32 v73, 0x3727c5ac, v73\n"
    "v_mfma_f32_16x16x32_bf16 v[20:23], a[80:83], a[100:103], v[20:23]\n"
    "v_rsq_f32 v72, v73\n"
    "v_mfma_f32_16x16x32_bf16 v[20:23], a[84:87], a[96:99], v[20:23]\n"
    "ds_read_b128 a[52:55], v35 offset:10240\n"
    "v_lshlrev_b32 v168, 16, v52\n"
    "v_mfma_f32_16x16x32_bf16 v[24:27], a[64:67], a[120:123], v[24:27]\n"
    "ds_read_b64 a[20:21], v32 offset:1088\n"
    "v_and_b32 v169, 0xffff0000, v52\n"
    "v_mfma_f32_16x16x32_bf16 v[24:27], a[68:71], a[116:119], v[24:27]\n"
    "ds_read_b64 a[22:23], v32 offset:1096\n"
    "v_mfma_f32_16x16x32_bf16 v[24:27], a[72:75], a[112:115], v[24:27]\n"
    "v_lshlrev_b32 v170, 16, v53\n"
    "v_and_b32 v171, 0xffff0000, v53\n"
    "v_mfma_f32_16x16x32_bf16 v[24:27], a[76:79], a[108:111], v[24:27]\n"
    "ds_read_b128 a[56:59], v35 offset:12288\n"
    "v_mfma_f32_16x16x32_bf16 v[24:27], a[80:83], a[104:107], v[24:27]\n"
    "ds_read_b64 a[24:25], v32 offset:576\n"
    "v_mul_f32 v74, v74, v72\n"
    "v_mfma_f32_16x16x32_bf16 v[24:27], a[84:87], a[100:103], v[24:27]\n"
    "ds_read_b64 a[26:27], v32 offset:584\n"
    "v_fma_f32 v200, v168, v72, v74\n"
    "v_mfma_f32_16x16x32_bf16 v[24:27], a[88:91], a[96:99], v[24:27]\n"
    "v_fma_f32 v201, v169, v72, v74\n"
    "v_mfma_f32_16x16x32_bf16 v[28:31], a[64:67], a[124:127], v[28:31]\n"
    "ds_read_b128 a[60:63], v35 offset:14336\n"
    "v_fma_f32 v202, v170, v72, v74\n"
    "v_mfma_f32_16x16x32_bf16 v[28:31], a[68:71], a[120:123], v[28:31]\n"
    "ds_read_b64 a[28:29], v32 offset:64\n"
    "v_fma_f32 v203, v171, v72, v74\n"
    "v_mfma_f32_16x16x32_bf16 v[28:31], a[72:75], a[116:119], v[28:31]\n"
    "ds_read_b64 a[30:31], v32 offset:72\n"
    "v_mfma_f32_16x16x32_bf16 v[28:31], a[76:79], a[112:115], v[28:31]\n"
    "v_add_f32 v76, v76, v78\n"
    "v_add_f32 v77, v77, v79\n"
    "v_mfma_f32_16x16x32_bf16 v[28:31], a[80:83], a[108:111], v[28:31]\n"
    "v_mul_f32 v78, 0xba800000, v76\n"
    "v_mfma_f32_16x16x32_bf16 v[28:31], a[84:87], a[104:107], v[28:31]\n"
    "v_mul_f32 v77, 0x3a800000, v77\n"
    "v_fma_f32 v77, -v78, v78, v77\n"
    "v_mfma_f32_16x16x32_bf16 v[28:31], a[88:91], a[100:103], v[28:31]\n"
    "v_max_f32 v77, 0, v77\n"
    "v_mfma_f32_16x16x32_bf16 v[28:31], a[92:95], a[96:99], v[28:31]\n"
    "v_add_f32 v77, 0x3727c5ac, v77\n"
    "s_waitcnt lgkmcnt(0)\n"
    "v_mfma_f32_16x16x32_bf16 v[0:3], a[0:3], a[32:35], v[0:3]\n"
    "ds_read_b128 a[96:99], v34 offset:0\n"
    "v_rsq_f32 v76, v77\n"
    "v_mfma_f32_16x16x32_bf16 v[4:7], a[0:3], a[36:39], v[4:7]\n"
    "ds_read_b64 a[64:65], v32 offset:3584\n"
    "v_lshlrev_b32 v172, 16, v54\n"
    "v_mfma_f32_16x16x32_bf16 v[4:7], a[4:7], a[32:35], v[4:7]\n"
    "ds_read_b64 a[66:67], v32 offset:3592\n"
    "v_and_b32 v173, 0xffff0000, v54\n"
    "v_mfma_f32_16x16x32_bf16 v[8:11], a[0:3], a[40:43], v[8:11]\n"
    "v_lshlrev_b32 v174, 16, v55\n"
    "v_and_b32 v175, 0xffff0000, v55\n"
    "v_mfma_f32_16x16x32_bf16 v[8:11], a[4:7], a[36:39], v[8:11]\n"
    "ds_read_b128 a[100:103], v34 offset:2048\n"
    "v_mul_f32 v78, v78, v76\n"
    "v_mfma_f32_16x16x32_bf16 v[8:11], a[8:11], a[32:35], v[8:11]\n"
    "ds_read_b64 a[68:69], v32 offset:3072\n"
    "v_fma_f32 v204, v172, v76, v78\n"
    "v_mfma_f32_16x16x32_bf16 v[12:15], a[0:3], a[44:47], v[12:15]\n"
    "ds_read_b64 a[70:71], v32 offset:3080\n"
    "v_mfma_f32_16x16x32_bf16 v[12:15], a[4:7], a[40:43], v[12:15]\n"
    "v_fma_f32 v205, v173, v76, v78\n"
    "v_fma_f32 v206, v174, v76, v78\n"
    "v_mfma_f32_16x16x32_bf16 v[12:15], a[8:11], a[36:39], v[12:15]\n"
    "ds_read_b128 a[104:107], v34 offset:4096\n"
    "v_fma_f32 v207, v175, v76, v78\n"
    "v_mfma_f32_16x16x32_bf16 v[12:15], a[12:15], a[32:35], v[12:15]\n"
    "ds_read_b64 a[72:73], v32 offset:2560\n"
    "v_add_f32 v80, v80, v82\n"
    "v_mfma_f32_16x16x32_bf16 v[16:19], a[0:3], a[48:51], v[16:19]\n"
    "ds_read_b64 a[74:75], v32 offset:2568\n"
    "v_add_f32 v81, v81, v83\n"
    "v_mfma_f32_16x16x32_bf16 v[16:19], a[4:7], a[44:47], v[16:19]\n"
    "v_mul_f32 v82, 0xba800000, v80\n"
    "v_mul_f32 v81, 0x3a800000, v81\n"
    "v_mfma_f32_16x16x32_bf16 v[16:19], a[8:11], a[40:43], v[16:19]\n"
    "ds_read_b128 a[108:111], v34 offset:6144\n"
    "v_mfma_f32_16x16x32_bf16 v[16:19], a[12:15], a[36:39], v[16:19]\n"
    "ds_read_b64 a[76:77], v32 offset:2048\n"
    "v_fma_f32 v81, -v82, v82, v81\n"
    "v_mfma_f32_16x16x32_bf16 v[16:19], a[16:19], a[32:35], v[16:19]\n"
    "ds_read_b64 a[78:79], v32 offset:2056\n"
    "v_max_f32 v81, 0, v81\n"
    "v_mfma_f32_16x16x32_bf16 v[20:23], a[0:3], a[52:55], v[20:23]\n"
    "v_add_f32 v81, 0x3727c5ac, v81\n"
    "v_rsq_f32 v80, v81\n"
    "v_mfma_f32_16x16x32_bf16 v[20:23], a[4:7], a[48:51], v[20:23]\n"
    "ds_read_b128 a[112:115], v34 offset:8192\n"
    "v_lshlrev_b32 v176, 16, v56\n"
    "v_mfma_f32_16x16x32_bf16 v[20:23], a[8:11], a[44:47], v[20:23]\n"
    "ds_read_b64 a[80:81], v32 offset:1536\n"
    "v_and_b32 v177, 0xffff0000, v56\n"
    "v_mfma_f32_16x16x32_bf16 v[20:23], a[12:15], a[40:43], v[20:23]\n"
    "ds_read_b64 a[82:83], v32 offset:1544\n"
    "v_lshlrev_b32 v178, 16, v57\n"
    "v_mfma_f32_16x16x32_bf16 v[20:23], a[16:19], a[36:39], v[20:23]\n"
    "v_and_b32 v179, 0xffff0000, v57\n"
    "v_mfma_f32_16x16x32_bf16 v[20:23], a[20:23], a[32:35], v[20:23]\n"
    "ds_read_b128 a[116:119], v34 offset:10240\n"
    "v_mul_f32 v82, v82, v80\n"
    "v_mfma_f32_16x16x32_bf16 v[24:27], a[0:3], a[56:59], v[24:27]\n"
    "ds_read_b64 a[84:85], v32 offset:1024\n"
    "v_fma_f32 v208, v176, v80, v82\n"
    "v_mfma_f32_16x16x32_bf16 v[24:27], a[4:7], a[52:55], v[24:27]\n"
    "ds_read_b64 a[86:87], v32 offset:1032\n"
    "v_fma_f32 v209, v177, v80, v82\n"
    "v_mfma_f32_16x16x32_bf16 v[24:27], a[8:11], a[48:51], v[24:27]\n"
    "v_fma_f32 v210, v178, v80, v82\n"
    "v_fma_f32 v211, v179, v80, v82\n"
    "v_mfma_f32_16x16x32_bf16 v[24:27], a[12:15], a[44:47], v[24:27]\n"
    "ds_read_b128 a[120:123], v34 offset:12288\n"
    "v_add_f32 v84, v84, v86\n"
    "v_mfma_f32_16x16x32_bf16 v[24:27], a[16:19], a[40:43], v[24:27]\n"
    "ds_read_b64 a[88:89], v32 offset:512\n"
    "v_mfma_f32_16x16x32_bf16 v[24:27], a[20:23], a[36:39], v[24:27]\n"
    "ds_read_b64 a[90:91], v32 offset:520\n"
    "v_add_f32 v85, v85, v87\n"
    "v_mfma_f32_16x16x32_bf16 v[24:27], a[24:27], a[32:35], v[24:27]\n"
    "v_mul_f32 v86, 0xba800000, v84\n"
    "v_mul_f32 v85, 0x3a800000, v85\n"
    "v_mfma_f32_16x16x32_bf16 v[28:31], a[0:3], a[60:63], v[28:31]\n"
    "ds_read_b128 a[124:127], v34 offset:14336\n"
    "v_fma_f32 v85, -v86, v86, v85\n"
    "v_mfma_f32_16x16x32_bf16 v[28:31], a[4:7], a[56:59], v[28:31]\n"
    "ds_read_b64 a[92:93], v32 offset:0\n"
    "v_max_f32 v85, 0, v85\n"
    "v_mfma_f32_16x16x32_bf16 v[28:31], a[8:11], a[52:55], v[28:31]\n"
    "ds_read_b64 a[94:95], v32 offset:8\n"
    "v_add_f32 v85, 0x3727c5ac, v85\n"
    "v_mfma_f32_16x16x32_bf16 v[28:31], a[12:15], a[48:51], v[28:31]\n"
    "v_rsq_f32 v84, v85\n"
    "v_mfma_f32_16x16x32_bf16 v[28:31], a[16:19], a[44:47], v[28:31]\n"
    "v_lshlrev_b32 v180, 16, v58\n"
    "v_and_b32 v181, 0xffff0000, v58\n"
    "v_mfma_f32_16x16x32_bf16 v[28:31], a[20:23], a[40:43], v[28:31]\n"
    "v_lshlrev_b32 v182, 16, v59\n"
    "v_and_b32 v183, 0xffff0000, v59\n"
    "v_mfma_f32_16x16x32_bf16 v[28:31], a[24:27], a[36:39], v[28:31]\n"
    "v_mul_f32 v86, v86, v84\n"
    "v_fma_f32 v212, v180, v84, v86\n"
    "v_mfma_f32_16x16x32_bf16 v[28:31], a[28:31], a[32:35], v[28:31]\n"
    "v_fma_f32 v213, v181, v84, v86\n"
    "s_waitcnt lgkmcnt(0)\n"
    "v_mfma_f32_16x16x32_bf16 v[0:3], a[64:67], a[96:99], v[0:3]\n"
    "v_fma_f32 v214, v182, v84, v86\n"
    "v_mfma_f32_16x16x32_bf16 v[4:7], a[64:67], a[100:103], v[4:7]\n"
    "v_fma_f32 v215, v183, v84, v86\n"
    "v_mfma_f32_16x16x32_bf16 v[4:7], a[68:71], a[96:99], v[4:7]\n"
    "v_add_f32 v88, v88, v90\n"
    "v_mfma_f32_16x16x32_bf16 v[8:11], a[64:67], a[104:107], v[8:11]\n"
    "v_add_f32 v89, v89, v91\n"
    "v_mfma_f32_16x16x32_bf16 v[8:11], a[68:71], a[100:103], v[8:11]\n"
    "v_mul_f32 v90, 0xba800000, v88\n"
    "v_mfma_f32_16x16x32_bf16 v[8:11], a[72:75], a[96:99], v[8:11]\n"
    "v_mul_f32 v89, 0x3a800000, v89\n"
    "v_mfma_f32_16x16x32_bf16 v[12:15], a[64:67], a[108:111], v[12:15]\n"
    "v_fma_f32 v89, -v90, v90, v89\n"
    "v_mfma_f32_16x16x32_bf16 v[12:15], a[68:71], a[104:107], v[12:15]\n"
    "v_max_f32 v89, 0, v89\n"
    "v_mfma_f32_16x16x32_bf16 v[12:15], a[72:75], a[100:103], v[12:15]\n"
    "v_add_f32 v89, 0x3727c5ac, v89\n"
    "v_mfma_f32_16x16x32_bf16 v[12:15], a[76:79], a[96:99], v[12:15]\n"
    "v_rsq_f32 v88, v89\n"
    "v_mfma_f32_16x16x32_bf16 v[16:19], a[64:67], a[112:115], v[16:19]\n"
    "v_lshlrev_b32 v184, 16, v60\n"
    "v_mfma_f32_16x16x32_bf16 v[16:19], a[68:71], a[108:111], v[16:19]\n"
    "v_and_b32 v185, 0xffff0000, v60\n"
    "v_mfma_f32_16x16x32_bf16 v[16:19], a[72:75], a[104:107], v[16:19]\n"
    "v_lshlrev_b32 v186, 16, v61\n"
    "v_mfma_f32_16x16x32_bf16 v[16:19], a[76:79], a[100:103], v[16:19]\n"
    "v_and_b32 v187, 0xffff0000, v61\n"
    "v_mfma_f32_16x16x32_bf16 v[16:19], a[80:83], a[96:99], v[16:19]\n"
    "v_mul_f32 v90, v90, v88\n"
    "v_mfma_f32_16x16x32_bf16 v[20:23], a[64:67], a[116:119], v[20:23]\n"
    "v_fma_f32 v216, v184, v88, v90\n"
    "v_mfma_f32_16x16x32_bf16 v[20:23], a[68:71], a[112:115], v[20:23]\n"
    "v_fma_f32 v217, v185, v88, v90\n"
    "v_mfma_f32_16x16x32_bf16 v[20:23], a[72:75], a[108:111], v[20:23]\n"
    "v_fma_f32 v218, v186, v88, v90\n"
    "v_mfma_f32_16x16x32_bf16 v[20:23], a[76:79], a[104:107], v[20:23]\n"
    "v_fma_f32 v219, v187, v88, v90\n"
    "v_mfma_f32_16x16x32_bf16 v[20:23], a[80:83], a[100:103], v[20:23]\n"
    "v_add_f32 v92, v92, v94\n"
    "v_mfma_f32_16x16x32_bf16 v[20:23], a[84:87], a[96:99], v[20:23]\n"
    "v_add_f32 v93, v93, v95\n"
    "v_mfma_f32_16x16x32_bf16 v[24:27], a[64:67], a[120:123], v[24:27]\n"
    "v_mul_f32 v94, 0xba800000, v92\n"
    "v_mfma_f32_16x16x32_bf16 v[24:27], a[68:71], a[116:119], v[24:27]\n"
    "v_mul_f32 v93, 0x3a800000, v93\n"
    "v_mfma_f32_16x16x32_bf16 v[24:27], a[72:75], a[112:115], v[24:27]\n"
    "v_fma_f32 v93, -v94, v94, v93\n"
    "v_mfma_f32_16x16x32_bf16 v[24:27], a[76:79], a[108:111], v[24:27]\n"
    "v_max_f32 v93, 0, v93\n"
    "v_mfma_f32_16x16x32_bf16 v[24:27], a[80:83], a[104:107], v[24:27]\n"
    "v_add_f32 v93, 0x3727c5ac, v93\n"
    "v_mfma_f32_16x16x32_bf16 v[24:27], a[84:87], a[100:103], v[24:27]\n"
    "v_rsq_f32 v92, v93\n"
    "v_mfma_f32_16x16x32_bf16 v[24:27], a[88:91], a[96:99], v[24:27]\n"
    "v_lshlrev_b32 v188, 16, v62\n"
    "v_mfma_f32_16x16x32_bf16 v[28:31], a[64:67], a[124:127], v[28:31]\n"
    "v_and_b32 v189, 0xffff0000, v62\n"
    "v_mfma_f32_16x16x32_bf16 v[28:31], a[68:71], a[120:123], v[28:31]\n"
    "v_lshlrev_b32 v190, 16, v63\n"
    "v_mfma_f32_16x16x32_bf16 v[28:31], a[72:75], a[116:119], v[28:31]\n"
    "v_and_b32 v191, 0xffff0000, v63\n"
    "v_mfma_f32_16x16x32_bf16 v[28:31], a[76:79], a[112:115], v[28:31]\n"
    "v_mul_f32 v94, v94, v92\n"
    "v_mfma_f32_16x16x32_bf16 v[28:31], a[80:83], a[108:111], v[28:31]\n"
    "v_fma_f32 v220, v188, v92, v94\n"
    "v_mfma_f32_16x16x32_bf16 v[28:31], a[84:87], a[104:107], v[28:31]\n"
    "v_fma_f32 v221, v189, v92, v94\n"
    "v_mfma_f32_16x16x32_bf16 v[28:31], a[88:91], a[100:103], v[28:31]\n"
    "v_fma_f32 v222, v190, v92, v94\n"
    "v_mfma_f32_16x16x32_bf16 v[28:31], a[92:95], a[96:99], v[28:31]\n"
    "v_fma_f32 v223, v191, v92, v94\n"
    "ds_write_b128 v45, v[0:3] offset:0\n"
    "ds_write_b128 v45, v[4:7] offset:1024\n"
    "ds_write_b128 v45, v[8:11] offset:2048\n"
    "ds_write_b128 v45, v[12:15] offset:3072\n"
    "ds_write_b128 v45, v[16:19] offset:4096\n"
    "ds_write_b128 v45, v[20:23] offset:5120\n"
    "ds_write_b128 v45, v[24:27] offset:6144\n"
    "ds_write_b128 v45, v[28:31] offset:7168\n"
    "s_waitcnt lgkmcnt(0)\n"
    "s_barrier\n"
    "ds_read_b64 v[0:1], v46 offset:0\n"
    "ds_read_b64 v[2:3], v46 offset:16640\n"
    "ds_read_b64 v[4:5], v46 offset:33280\n"
    "ds_read_b64 v[6:7], v148 offset:0\n"
    "ds_read_b64 v[8:9], v46 offset:2048\n"
    "ds_read_b64 v[10:11], v46 offset:18688\n"
    "ds_read_b64 v[12:13], v46 offset:35328\n"
    "ds_read_b64 v[14:15], v148 offset:2048\n"
    "ds_read_b64 v[16:17], v46 offset:4096\n"
    "ds_read_b64 v[18:19], v46 offset:20736\n"
    "ds_read_b64 v[20:21], v46 offset:37376\n"
    "ds_read_b64 v[22:23], v148 offset:4096\n"
    "ds_read_b64 v[24:25], v46 offset:6144\n"
    "ds_read_b64 v[26:27], v46 offset:22784\n"
    "ds_read_b64 v[28:29], v46 offset:39424\n"
    "ds_read_b64 v[30:31], v148 offset:6144\n"
    "s_mov_b32 s53, 0\n"
    "s_waitcnt lgkmcnt(12)\n"
    "v_fma_f32 v160, v192, v0, v160\n"
    "v_fma_f32 v161, v193, v2, v161\n"
    "v_fma_f32 v162, v194, v4, v162\n"
    "v_fma_f32 v163, v195, v6, v163\n"
    "v_fma_f32 v164, v196, v1, v164\n"
    "v_fma_f32 v165, v197, v3, v165\n"
    "v_fma_f32 v166, v198, v5, v166\n"
    "v_fma_f32 v167, v199, v7, v167\n"
    "v_cvt_pk_bf16_f32 v160, v160, v161\n"
    "v_cvt_pk_bf16_f32 v161, v162, v163\n"
    "v_cvt_pk_bf16_f32 v162, v164, v165\n"
    "S1_H1%=:\n"
    "s_cbranch_execz S1_R%=\n"
    "v_cvt_pk_bf16_f32 v163, v166, v167\n"
    "buffer_store_dwordx4 v[160:163], v47, s[8:11], s53 offen sc1\n"
    "s_mov_b32 s53, 4096\n"
    "s_waitcnt lgkmcnt(8)\n"
    "v_fma_f32 v168, v200, v8, v168\n"
    "v_fma_f32 v169, v201, v10, v169\n"
    "v_fma_f32 v170, v202, v12, v170\n"
    "v_fma_f32 v171, v203, v14, v171\n"
    "v_fma_f32 v172, v204, v9, v172\n"
    "v_fma_f32 v173, v205, v11, v173\n"
    "v_fma_f32 v174, v206, v13, v174\n"
    "v_fma_f32 v175, v207, v15, v175\n"
    "v_cvt_pk_bf16_f32 v168, v168, v169\n"
    "v_cvt_pk_bf16_f32 v169, v170, v171\n"
    "v_cvt_pk_bf16_f32 v170, v172, v173\n"
    "v_cvt_pk_bf16_f32 v171, v174, v175\n"
    "buffer_store_dwordx4 v[168:171], v47, s[8:11], s53 offen sc1\n"
    "s_mov_b32 s53, 8192\n"
    "s_waitcnt lgkmcnt(4)\n"
    "v_fma_f32 v176, v208, v16, v176\n"
    "v_fma_f32 v177, v209, v18, v177\n"
    "v_fma_f32 v178, v210, v20, v178\n"
    "v_fma_f32 v179, v211, v22, v179\n"
    "v_fma_f32 v180, v212, v17, v180\n"
    "v_fma_f32 v181, v213, v19, v181\n"
    "v_fma_f32 v182, v214, v21, v182\n"
    "v_fma_f32 v183, v215, v23, v183\n"
    "v_cvt_pk_bf16_f32 v176, v176, v177\n"
    "v_cvt_pk_bf16_f32 v177, v178, v179\n"
    "v_cvt_pk_bf16_f32 v178, v180, v181\n"
    "v_cvt_pk_bf16_f32 v179, v182, v183\n"
    "buffer_store_dwordx4 v[176:179], v47, s[8:11], s53 offen sc1\n"
    "s_mov_b32 s53, 12288\n"
    "s_waitcnt lgkmcnt(0)\n"
    "v_fma_f32 v184, v216, v24, v184\n"
    "v_fma_f32 v185, v217, v26, v185\n"
    "v_fma_f32 v186, v218, v28, v186\n"
    "v_fma_f32 v187, v219, v30, v187\n"
    "v_fma_f32 v188, v220, v25, v188\n"
    "v_fma_f32 v189, v221, v27, v189\n"
    "v_fma_f32 v190, v222, v29, v190\n"
    "v_fma_f32 v191, v223, v31, v191\n"
    "v_cvt_pk_bf16_f32 v184, v184, v185\n"
    "v_cvt_pk_bf16_f32 v185, v186, v187\n"
    "v_cvt_pk_bf16_f32 v186, v188, v189\n"
    "v_cvt_pk_bf16_f32 v187, v190, v191\n"
    "buffer_store_dwordx4 v[184:187], v47, s[8:11], s53 offen sc1\n"
    "v_mov_b32 v0, s38\n"
    "v_mov_b32 v1, s39\n"
    "buffer_store_dwordx2 v[0:1], v149, s[8:11], 0 offen sc1\n"
    "s_add_u32 m0, s29, 46176\n"
    "s_nop 0\n"
    "buffer_load_dwordx4 v225, s[12:15], s34 offen lds\n"
    "s_add_u32 m0, s30, 46176\n"
    "s_nop 0\n"
    "buffer_load_dwordx4 v226, s[16:19], s34 offen lds\n"
    "s_add_u32 m0, s31, 46176\n"
    "s_nop 0\n"
    "buffer_load_dwordx4 v227, s[20:23], s34 offen lds\n"
    "s_add_u32 m0, s32, 46176\n"
    "s_nop 0\n"
    "buffer_load_dwordx4 v228, s[24:27], s34 offen lds\n"
    "s_add_u32 s34, s34, 229376\n"
    "s_waitcnt vmcnt(4)\n"
    "s_barrier\n"
    "s_cmp_lg_u32 s37, 0\n"
    "s_cbranch_scc1 A2_A%=\n"
    "v_mov_b32 v158, 1\n"
    "s_mov_b64 exec, 1\n"
    "buffer_atomic_add v158, v146, s[8:11], 0 offen offset:0 sc0\n"
    "s_mov_b64 exec, -1\n"
    "A2_A%=:\n"
    "v_mov_b32 v157, v147\n"
    "v_mov_b32 v159, v151\n"
    "v_mov_b32 v6, 0x3f803f80\n"
    "ds_read_b64_tr_b16 v[48:49], v251 offset:0\n"
    "ds_read_b64_tr_b16 v[50:51], v251 offset:32\n"
    "ds_read_b64_tr_b16 v[52:53], v251 offset:64\n"
    "ds_read_b64_tr_b16 v[54:55], v251 offset:96\n"
    "ds_read_b64_tr_b16 v[56:57], v251 offset:3584\n"
    "ds_read_b64_tr_b16 v[58:59], v251 offset:3616\n"
    "ds_read_b64_tr_b16 v[60:61], v251 offset:3648\n"
    "ds_read_b64_tr_b16 v[62:63], v251 offset:3680\n"
    "ds_read_b64_tr_b16 v[64:65], v251 offset:15392\n"
    "ds_read_b64_tr_b16 v[66:67], v251 offset:15424\n"
    "ds_read_b64_tr_b16 v[68:69], v251 offset:15456\n"
    "ds_read_b64_tr_b16 v[70:71], v251 offset:15488\n"
    "ds_read_b64_tr_b16 v[72:73], v251 offset:18976\n"
    "ds_read_b64_tr_b16 v[74:75], v251 offset:19008\n"
    "ds_read_b64_tr_b16 v[76:77], v251 offset:19040\n"
    "ds_read_b64_tr_b16 v[78:79], v251 offset:19072\n"
    "ds_read_b64_tr_b16 v[80:81], v251 offset:30784\n"
    "ds_read_b64_tr_b16 v[82:83], v251 offset:30816\n"
    "ds_read_b64_tr_b16 v[84:85], v251 offset:30848\n"
    "ds_read_b64_tr_b16 v[86:87], v251 offset:30880\n"
    "ds_read_b64_tr_b16 v[88:89], v251 offset:34368\n"
    "ds_read_b64_tr_b16 v[90:91], v251 offset:34400\n"
    "ds_read_b64_tr_b16 v[92:93], v251 offset:34432\n"
    "ds_read_b64_tr_b16 v[94:95], v251 offset:34464\n"
    "s_mul_i32 s33, s6, 524288\n"
    "s_add_u32 s33, s33, 10485760\n"
    "s_sub_u32 s53, s33, 131072\n"
    "s_mov_b32 s56, 131072\n"
    "s_mov_b32 s57, 229376\n"
    "s_cmp_eq_u32 s6, 0\n"
    "s_cselect_b32 s58, 524288, 0\n"
    "s_cselect_b32 s34, 917504, 0\n"
    "s_add_u32 s58, s58, 10485760\n"
    "s_waitcnt lgkmcnt(0)\n"
    "v_dot2_f32_bf16 v0, v48, v6, 0\n"
    "v_dot2_f32_bf16 v1, v50, v6, 0\n"
    "v_dot2_f32_bf16 v2, v52, v6, 0\n"
    "v_dot2_f32_bf16 v3, v54, v6, 0\n"
    "v_dot2_f32_bf16 v0, v56, v6, v0\n"
    "v_dot2_f32_bf16 v1, v58, v6, v1\n"
    "v_dot2_f32_bf16 v2, v60, v6, v2\n"
    "v_dot2_f32_bf16 v3, v62, v6, v3\n"
    "v_dot2_f32_bf16 v0, v64, v6, v0\n"
    "v_dot2_f32_bf16 v1, v66, v6, v1\n"
    "v_dot2_f32_bf16 v2, v68, v6, v2\n"
    "v_dot2_f32_bf16 v3, v70, v6, v3\n"
    "v_dot2_f32_bf16 v0, v72, v6, v0\n"
    "v_dot2_f32_bf16 v1, v74, v6, v1\n"
    "v_dot2_f32_bf16 v2, v76, v6, v2\n"
    "v_dot2_f32_bf16 v3, v78, v6, v3\n"
    "v_dot2_f32_bf16 v0, v80, v6, v0\n"
    "v_dot2_f32_bf16 v1, v82, v6, v1\n"
    "v_dot2_f32_bf16 v2, v84, v6, v2\n"
    "v_dot2_f32_bf16 v3, v86, v6, v3\n"
    "v_dot2_f32_bf16 v0, v88, v6, v0\n"
    "v_dot2_f32_bf16 v1, v90, v6, v1\n"
    "v_dot2_f32_bf16 v2, v92, v6, v2\n"
    "v_dot2_f32_bf16 v3, v94, v6, v3\n"
    "v_dot2_f32_bf16 v0, v49, v6, v0\n"
    "v_dot2_f32_bf16 v1, v51, v6, v1\n"
    "v_dot2_f32_bf16 v2, v53, v6, v2\n"
    "v_dot2_f32_bf16 v3, v55, v6, v3\n"
    "v_dot2_f32_bf16 v0, v57, v6, v0\n"
    "v_dot2_f32_bf16 v1, v59, v6, v1\n"
    "v_dot2_f32_bf16 v2, v61, v6, v2\n"
    "v_dot2_f32_bf16 v3, v63, v6, v3\n"
    "v_dot2_f32_bf16 v0, v65, v6, v0\n"
    "v_dot2_f32_bf16 v1, v67, v6, v1\n"
    "v_dot2_f32_bf16 v2, v69, v6, v2\n"
    "v_dot2_f32_bf16 v3, v71, v6, v3\n"
    "v_dot2_f32_bf16 v0, v73, v6, v0\n"
    "v_dot2_f32_bf16 v1, v75, v6, v1\n"
    "v_dot2_f32_bf16 v2, v77, v6, v2\n"
    "v_dot2_f32_bf16 v3, v79, v6, v3\n"
    "v_dot2_f32_bf16 v0, v81, v6, v0\n"
    "v_dot2_f32_bf16 v1, v83, v6, v1\n"
    "v_dot2_f32_bf16 v2, v85, v6, v2\n"
    "v_dot2_f32_bf16 v3, v87, v6, v3\n"
    "v_dot2_f32_bf16 v0, v89, v6, v0\n"
    "v_dot2_f32_bf16 v1, v91, v6, v1\n"
    "v_dot2_f32_bf16 v2, v93, v6, v2\n"
    "v_dot2_f32_bf16 v3, v95, v6, v3\n"
    "s_cmp_lg_u32 s37, 0\n"
    "s_cbranch_scc1 A2_F%=\n"
    "s_waitcnt vmcnt(0)\n"
    "v_readfirstlane_b32 s35, v158\n"
    "v_mov_b32 v158, 1\n"
    "s_cmp_lg_u32 s35, 31\n"
    "s_cbranch_scc1 A2_F%=\n"
    "s_mov_b64 exec, 0xff\n"
    "buffer_store_dword v158, v159, s[8:11], 0 offen offset:0 sc1\n"
    "s_mov_b64 exec, -1\n"
    "A2_F%=:\n"
    "s_cmp_lg_u32 s37, 0\n"
    "s_cbranch_scc1 S2_W%=\n"
    "s_mov_b32 s35, 0\n"
    "S2_L%=:\n"
    "buffer_load_dword v233, v157, s[8:11], 0 offen sc1\n"
    "s_waitcnt vmcnt(0)\n"
    "v_cmp_gt_u32 vcc, 1, v233\n"
    "s_add_u32 s35, s35, 1\n"
    "s_nop 3\n"
    "s_cbranch_vccz S2_D%=\n"
    "s_sleep 2\n"
    "s_cmp_lt_u32 s35, 0x40000\n"
    "s_cbranch_scc1 S2_L%=\n"
    "S2_D%=:\n"
    "s_branch S2_E%=\n"
    "S2_W%=:\n"
    "s_mov_b64 exec, 0\n"
    "s_cmp_eq_u32 s37, 1\n"
    "s_cbranch_scc1 S2_H0%=\n"
    "s_cmp_eq_u32 s37, 2\n"
    "s_cbranch_scc1 S2_H1%=\n"
    "s_branch S2_H2%=\n"
    "S2_R%=:\n"
    "s_mov_b64 exec, -1\n"
    "S2_E%=:\n"
    "s_barrier\n"
    "s_add_u32 m0, s28, 0\n"
    "s_nop 0\n"
    "buffer_load_dwordx4 v224, s[8:11], s33 offen offset:0 sc1 lds\n"
    "s_add_u32 m0, s28, 896\n"
    "s_nop 0\n"
    "buffer_load_dwordx4 v224, s[8:11], s33 offen offset:128 sc1 lds\n"
    "s_add_u32 m0, s28, 1792\n"
    "s_nop 0\n"
    "buffer_load_dwordx4 v224, s[8:11], s33 offen offset:256 sc1 lds\n"
    "s_add_u32 m0, s28, 2688\n"
    "s_nop 0\n"
    "buffer_load_dwordx4 v224, s[8:11], s33 offen offset:384 sc1 lds\n"
    "s_add_u32 s33, s33, 131072\n"
    "s_add_u32 m0, s28, 16384\n"
    "s_nop 0\n"
    "buffer_load_dwordx4 v224, s[8:11], s33 offen offset:0 sc1 lds\n"
    "s_add_u32 m0, s28, 17280\n"
    "s_nop 0\n"
    "buffer_load_dwordx4 v224, s[8:11], s33 offen offset:128 sc1 lds\n"
    "s_add_u32 m0, s28, 18176\n"
    "s_nop 0\n"
    "buffer_load_dwordx4 v224, s[8:11], s33 offen offset:256 sc1 lds\n"
    "s_add_u32 m0, s28, 19072\n"
    "s_nop 0\n"
    "buffer_load_dwordx4 v224, s[8:11], s33 offen offset:384 sc1 lds\n"
    "s_add_u32 s33, s33, 131072\n"
    "s_add_u32 m0, s28, 32768\n"
    "s_nop 0\n"
    "buffer_load_dwordx4 v224, s[8:11], s33 offen offset:0 sc1 lds\n"
    "s_add_u32 m0, s28, 33664\n"
    "s_nop 0\n"
    "buffer_load_dwordx4 v224, s[8:11], s33 offen offset:128 sc1 lds\n"
    "s_add_u32 m0, s28, 34560\n"
    "s_nop 0\n"
    "buffer_load_dwordx4 v224, s[8:11], s33 offen offset:256 sc1 lds\n"
    "s_add_u32 m0, s28, 35456\n"
    "s_nop 0\n"
    "buffer_load_dwordx4 v224, s[8:11], s33 offen offset:384 sc1 lds\n"
    "s_add_u32 s33, s33, 131072\n"
    "s_add_u32 m0, s28, 49152\n"
    "s_nop 0\n"
    "buffer_load_dwordx4 v224, s[8:11], s33 offen offset:0 sc1 lds\n"
    "s_add_u32 m0, s28, 50048\n"
    "s_nop 0\n"
    "buffer_load_dwordx4 v224, s[8:11], s33 offen offset:128 sc1 lds\n"
    "s_add_u32 m0, s28, 50944\n"
    "s_nop 0\n"
    "buffer_load_dwordx4 v224, s[8:11], s33 offen offset:256 sc1 lds\n"
    "s_add_u32 m0, s28, 51840\n"
    "s_nop 0\n"
    "buffer_load_dwordx4 v224, s[8:11], s33 offen offset:384 sc1 lds\n"
    "s_mov_b32 s33, s58\n"
    "s_waitcnt vmcnt(12) lgkmcnt(0)\n"
    "s_barrier\n"
    "v_mov_b32 v6, 0x3f803f80\n"
    "ds_read_b64_tr_b16 v[48:49], v251 offset:46176\n"
    "ds_read_b64_tr_b16 v[50:51], v251 offset:46208\n"
    "ds_read_b64_tr_b16 v[52:53], v251 offset:46240\n"
    "ds_read_b64_tr_b16 v[54:55], v251 offset:46272\n"
    "ds_read_b64_tr_b16 v[56:57], v251 offset:49760\n"
    "ds_read_b64_tr_b16 v[58:59], v251 offset:49792\n"
    "ds_read_b64_tr_b16 v[60:61], v251 offset:49824\n"
    "ds_read_b64_tr_b16 v[62:63], v251 offset:49856\n"
    "ds_read_b64 a[0:1], v229 offset:0\n"
    "ds_read_b64 a[2:3], v229 offset:512\n"
    "ds_read_b64_tr_b16 a[32:33], v230 offset:0\n"
    "ds_read_b64_tr_b16 a[34:35], v230 offset:3584\n"
    "ds_read_b64_tr_b16 a[36:37], v230 offset:32\n"
    "ds_read_b64_tr_b16 a[38:39], v230 offset:3616\n"
    "ds_read_b64_tr_b16 a[40:41], v230 offset:64\n"
    "ds_read_b64_tr_b16 a[42:43], v230 offset:3648\n"
    "ds_read_b64_tr_b16 a[44:45], v230 offset:7184\n"
    "ds_read_b64_tr_b16 a[46:47], v230 offset:10768\n"
    "ds_read_b64_tr_b16 a[48:49], v230 offset:7216\n"
    "ds_read_b64_tr_b16 a[50:51], v230 offset:10800\n"
    "ds_read_b64_tr_b16 a[52:53], v230 offset:7248\n"
    "ds_read_b64_tr_b16 a[54:55], v230 offset:10832\n"
    "s_waitcnt lgkmcnt(14)\n"
    "ds_read_b64_tr_b16 a[56:57], v231 offset:0\n"
    "ds_read_b64_tr_b16 a[58:59], v231 offset:3584\n"
    "ds_read_b64 a[4:5], v229 offset:1024\n"
    "ds_read_b64 a[6:7], v229 offset:1536\n"
    "ds_read_b64 a[8:9], v229 offset:2048\n"
    "ds_read_b64 a[10:11], v229 offset:2560\n"
    "ds_read_b64 a[12:13], v229 offset:3072\n"
    "ds_read_b64 a[14:15], v229 offset:3584\n"
    "ds_read_b64 a[16:17], v229 offset:4096\n"
    "ds_read_b64 a[18:19], v229 offset:4608\n"
    "ds_read_b64 a[20:21], v229 offset:5120\n"
    "ds_read_b64 a[22:23], v229 offset:5632\n"
    "ds_read_b64 a[24:25], v229 offset:6144\n"
    "ds_read_b64 a[26:27], v229 offset:6656\n"
    "ds_read_b64 a[28:29], v229 offset:7168\n"
    "ds_read_b64 a[30:31], v229 offset:7680\n"
    "v_dot2_f32_bf16 v0, v48, v6, v0\n"
    "v_dot2_f32_bf16 v1, v50, v6, v1\n"
    "v_dot2_f32_bf16 v2, v52, v6, v2\n"
    "v_dot2_f32_bf16 v3, v54, v6, v3\n"
    "v_dot2_f32_bf16 v0, v56, v6, v0\n"
    "v_dot2_f32_bf16 v1, v58, v6, v1\n"
    "v_dot2_f32_bf16 v2, v60, v6, v2\n"
    "v_dot2_f32_bf16 v3, v62, v6, v3\n"
    "v_dot2_f32_bf16 v0, v49, v6, v0\n"
    "v_dot2_f32_bf16 v1, v51, v6, v1\n"
    "v_dot2_f32_bf16 v2, v53, v6, v2\n"
    "v_dot2_f32_bf16 v3, v55, v6, v3\n"
    "v_dot2_f32_bf16 v0, v57, v6, v0\n"
    "v_dot2_f32_bf16 v1, v59, v6, v1\n"
    "v_dot2_f32_bf16 v2, v61, v6, v2\n"
    "v_dot2_f32_bf16 v3, v63, v6, v3\n"
    "s_nop 1\n"
    "v_permlane32_swap_b32 v0, v2\n"
    "v_permlane32_swap_b32 v1, v3\n"
    "v_add_f32 v0, v0, v2\n"
    "v_add_f32 v1, v1, v3\n"
    "s_nop 1\n"
    "v_permlane16_swap_b32 v0, v1\n"
    "v_add_f32 v4, v0, v1\n"
    "v_mov_b32 v5, 1\n"
    "buffer_store_dwordx2 v[4:5], v156, s[8:11], 0 offen sc1\n"
    "s_waitcnt lgkmcnt(0)\n"
    "s_barrier\n"
    "v_mfma_f32_16x16x32_bf16 v[0:3], a[32:35], a[0:3], 0\n"
    "v_mfma_f32_16x16x32_bf16 v[4:7], a[36:39], a[0:3], 0\n"
    "s_add_u32 m0, s28, 0\n"
    "v_mfma_f32_16x16x32_bf16 v[8:11], a[40:43], a[0:3], 0\n"
    "buffer_load_dwordx4 v224, s[8:11], s33 offen offset:0 sc1 lds\n"
    "v_mfma_f32_16x16x32_bf16 v[12:15], a[44:47], a[0:3], 0\n"
    "v_mfma_f32_16x16x32_bf16 v[16:19], a[48:51], a[0:3], 0\n"
    "v_mfma_f32_16x16x32_bf16 v[20:23], a[52:55], a[0:3], 0\n"
    "v_mfma_f32_16x16x32_bf16 v[24:27], a[56:59], a[0:3], 0\n"
    "s_add_u32 m0, s29, 0\n"
    "v_mfma_f32_16x16x32_bf16 v[52:55], a[56:59], a[4:7], 0\n"
    "buffer_load_dwordx4 v225, s[12:15], s34 offen lds\n"
    "v_mfma_f32_16x16x32_bf16 v[48:51], a[52:55], a[4:7], 0\n"
    "v_mfma_f32_16x16x32_bf16 v[44:47], a[48:51], a[4:7], 0\n"
    "v_mfma_f32_16x16x32_bf16 v[40:43], a[44:47], a[4:7], 0\n"
    "v_mfma_f32_16x16x32_bf16 v[36:39], a[40:43], a[4:7], 0\n"
    "s_add_u32 m0, s28, 896\n"
    "v_mfma_f32_16x16x32_bf16 v[32:35], a[36:39], a[4:7], 0\n"
    "buffer_load_dwordx4 v224, s[8:11], s33 offen offset:128 sc1 lds\n"
    "v_mfma_f32_16x16x32_bf16 v[28:31], a[32:35], a[4:7], 0\n"
    "v_mfma_f32_16x16x32_bf16 v[56:59], a[32:35], a[8:11], 0\n"
    "v_mfma_f32_16x16x32_bf16 v[60:63], a[36:39], a[8:11], 0\n"
    "v_mfma_f32_16x16x32_bf16 v[64:67], a[40:43], a[8:11], 0\n"
    "v_mfma_f32_16x16x32_bf16 v[68:71], a[44:47], a[8:11], 0\n"
    "v_mfma_f32_16x16x32_bf16 v[72:75], a[48:51], a[8:11], 0\n"
    "v_mfma_f32_16x16x32_bf16 v[76:79], a[52:55], a[8:11], 0\n"
    "v_mfma_f32_16x16x32_bf16 v[80:83], a[56:59], a[8:11], 0\n"
    "s_waitcnt vmcnt(12) lgkmcnt(0)\n"
    "s_barrier\n"
    "v_mfma_f32_16x16x32_bf16 v[108:111], a[56:59], a[12:15], 0\n"
    "ds_read_b64 a[60:61], v229 offset:16384\n"
    "ds_read_b64 a[62:63], v229 offset:16896\n"
    "v_mfma_f32_16x16x32_bf16 v[104:107], a[52:55], a[12:15], 0\n"
    "ds_read_b64_tr_b16 a[92:93], v230 offset:15392\n"
    "ds_read_b64_tr_b16 a[94:95], v230 offset:18976\n"
    "v_mfma_f32_16x16x32_bf16 v[100:103], a[48:51], a[12:15], 0\n"
    "ds_read_b64_tr_b16 a[96:97], v230 offset:15424\n"
    "ds_read_b64_tr_b16 a[98:99], v230 offset:19008\n"
    "v_mfma_f32_16x16x32_bf16 v[96:99], a[44:47], a[12:15], 0\n"
    "ds_read_b64_tr_b16 a[100:101], v230 offset:15456\n"
    "ds_read_b64_tr_b16 a[102:103], v230 offset:19040\n"
    "v_mfma_f32_16x16x32_bf16 v[92:95], a[40:43], a[12:15], 0\n"
    "ds_read_b64_tr_b16 a[104:105], v230 offset:22576\n"
    "ds_read_b64_tr_b16 a[106:107], v230 offset:26160\n"
    "v_mfma_f32_16x16x32_bf16 v[88:91], a[36:39], a[12:15], 0\n"
    "s_add_u32 m0, s30, 0\n"
    "ds_read_b64_tr_b16 a[108:109], v230 offset:22608\n"
    "v_mfma_f32_16x16x32_bf16 v[84:87], a[32:35], a[12:15], 0\n"
    "buffer_load_dwordx4 v226, s[16:19], s34 offen lds\n"
    "ds_read_b64_tr_b16 a[110:111], v230 offset:26192\n"
    "v_mfma_f32_16x16x32_bf16 v[112:115], a[32:35], a[16:19], 0\n"
    "ds_read_b64_tr_b16 a[112:113], v230 offset:22640\n"
    "ds_read_b64_tr_b16 a[114:115], v230 offset:26224\n"
    "v_mfma_f32_16x16x32_bf16 v[116:119], a[36:39], a[16:19], 0\n"
    "ds_read_b64_tr_b16 a[116:117], v231 offset:15392\n"
    "ds_read_b64_tr_b16 a[118:119], v231 offset:18976\n"
    "v_mfma_f32_16x16x32_bf16 v[120:123], a[40:43], a[16:19], 0\n"
    "s_mov_b32 s36, 10485760\n"
    "ds_read_b64 a[64:65], v229 offset:17408\n"
    "v_mfma_f32_16x16x32_bf16 v[124:127], a[44:47], a[16:19], 0\n"
    "s_add_u32 m0, s28, 1792\n"
    "buffer_load_dwordx4 v[252:255], v240, s[8:11], s36 offen sc1\n"
    "v_mfma_f32_16x16x32_bf16 v[128:131], a[48:51], a[16:19], 0\n"
    "buffer_load_dwordx4 v224, s[8:11], s33 offen offset:256 sc1 lds\n"
    "ds_read_b64 a[66:67], v229 offset:17920\n"
    "v_mfma_f32_16x16x32_bf16 v[132:135], a[52:55], a[16:19], 0\n"
    "ds_read_b64 a[68:69], v229 offset:18432\n"
    "ds_read_b64 a[70:71], v229 offset:18944\n"
    "v_mfma_f32_16x16x32_bf16 v[136:139], a[56:59], a[16:19], 0\n"
    "ds_read_b64 a[72:73], v229 offset:19456\n"
    "ds_read_b64 a[74:75], v229 offset:19968\n"
    "v_mfma_f32_16x16x32_bf16 v[164:167], a[56:59], a[20:23], 0\n"
    "s_mov_b32 s36, 10747904\n"
    "ds_read_b64 a[76:77], v229 offset:20480\n"
    "v_mfma_f32_16x16x32_bf16 v[160:163], a[52:55], a[20:23], 0\n"
    "s_add_u32 m0, s31, 0\n"
    "buffer_load_dwordx4 a[244:247], v240, s[8:11], s36 offen sc1\n"
    "v_mfma_f32_16x16x32_bf16 v[156:159], a[48:51], a[20:23], 0\n"
    "buffer_load_dwordx4 v227, s[20:23], s34 offen lds\n"
    "ds_read_b64 a[78:79], v229 offset:20992\n"
    "v_mfma_f32_16x16x32_bf16 v[152:155], a[44:47], a[20:23], 0\n"
    "ds_read_b64 a[80:81], v229 offset:21504\n"
    "ds_read_b64 a[82:83], v229 offset:22016\n"
    "v_mfma_f32_16x16x32_bf16 v[148:151], a[40:43], a[20:23], 0\n"
    "buffer_load_dword v233, v232, s[48:51], 0 offen\n"
    "ds_read_b64 a[84:85], v229 offset:22528\n"
    "v_mfma_f32_16x16x32_bf16 v[144:147], a[36:39], a[20:23], 0\n"
    "v_add_u32 v232, s63, v232\n"
    "s_mov_b32 s36, 11010048\n"
    "v_mfma_f32_16x16x32_bf16 v[140:143], a[32:35], a[20:23], 0\n"
    "buffer_load_dwordx4 a[248:251], v240, s[8:11], s36 offen sc1\n"
    "ds_read_b64 a[86:87], v229 offset:23040\n"
    "v_mfma_f32_16x16x32_bf16 v[168:171], a[32:35], a[24:27], 0\n"
    "ds_read_b64 a[88:89], v229 offset:23552\n"
    "ds_read_b64 a[90:91], v229 offset:24064\n"
    "v_mfma_f32_16x16x32_bf16 v[172:175], a[36:39], a[24:27], 0\n"
    "s_add_u32 m0, s28, 2688\n"
    "v_mfma_f32_16x16x32_bf16 v[176:179], a[40:43], a[24:27], 0\n"
    "buffer_load_dwordx4 v224, s[8:11], s33 offen offset:384 sc1 lds\n"
    "v_mfma_f32_16x16x32_bf16 v[180:183], a[44:47], a[24:27], 0\n"
    "s_mov_b32 s36, 11272192\n"
    "v_mfma_f32_16x16x32_bf16 v[184:187], a[48:51], a[24:27], 0\n"
    "buffer_load_dwordx4 a[252:255], v240, s[8:11], s36 offen sc1\n"
    "v_mfma_f32_16x16x32_bf16 v[188:191], a[52:55], a[24:27], 0\n"
    "v_mfma_f32_16x16x32_bf16 v[192:195], a[56:59], a[24:27], 0\n"
    "v_mfma_f32_16x16x32_bf16 v[220:223], a[56:59], a[28:31], 0\n"
    "s_add_u32 m0, s32, 0\n"
    "v_mfma_f32_16x16x32_bf16 v[216:219], a[52:55], a[28:31], 0\n"
    "buffer_load_dwordx4 v228, s[24:27], s34 offen lds\n"
    "v_mfma_f32_16x16x32_bf16 v[212:215], a[48:51], a[28:31], 0\n"
    "s_cmp_eq_u32 s33, s53\n"
    "s_cselect_b32 s59, 655360, s56\n"
    "v_mfma_f32_16x16x32_bf16 v[208:211], a[44:47], a[28:31], 0\n"
    "s_cselect_b32 s62, 1146880, s57\n"
    "s_add_u32 s33, s33, s59\n"
    "v_mfma_f32_16x16x32_bf16 v[204:207], a[40:43], a[28:31], 0\n"
    "s_add_u32 s34, s34, s62\n"
    "v_mfma_f32_16x16x32_bf16 v[200:203], a[36:39], a[28:31], 0\n"
    "v_mfma_f32_16x16x32_bf16 v[196:199], a[32:35], a[28:31], 0\n"
    "s_waitcnt vmcnt(14) lgkmcnt(0)\n"
    "s_barrier\n"
    "v_mfma_f32_16x16x32_bf16 v[0:3], a[92:95], a[60:63], v[0:3]\n"
    "ds_read_b64 a[120:121], v229 offset:32768\n"
    "ds_read_b64 a[122:123], v229 offset:33280\n"
    "v_mfma_f32_16x16x32_bf16 v[4:7], a[96:99], a[60:63], v[4:7]\n"
    "s_add_u32 m0, s28, 16384\n"
    "ds_read_b64_tr_b16 a[152:153], v230 offset:30784\n"
    "v_mfma_f32_16x16x32_bf16 v[8:11], a[100:103], a[60:63], v[8:11]\n"
    "buffer_load_dwordx4 v224, s[8:11], s33 offen offset:0 sc1 lds\n"
    "ds_read_b64_tr_b16 a[154:155], v230 offset:34368\n"
    "v_mfma_f32_16x16x32_bf16 v[12:15], a[104:107], a[60:63], v[12:15]\n"
    "ds_read_b64_tr_b16 a[156:157], v230 offset:30816\n"
    "ds_read_b64_tr_b16 a[158:159], v230 offset:34400\n"
    "v_mfma_f32_16x16x32_bf16 v[16:19], a[108:111], a[60:63], v[16:19]\n"
    "ds_read_b64_tr_b16 a[160:161], v230 offset:30848\n"
    "ds_read_b64_tr_b16 a[162:163], v230 offset:34432\n"
    "v_mfma_f32_16x16x32_bf16 v[20:23], a[112:115], a[60:63], v[20:23]\n"
    "ds_read_b64_tr_b16 a[164:165], v230 offset:37968\n"
    "ds_read_b64_tr_b16 a[166:167], v230 offset:41552\n"
    "v_mfma_f32_16x16x32_bf16 v[24:27], a[116:119], a[60:63], v[24:27]\n"
    "s_add_u32 m0, s29, 15392\n"
    "ds_read_b64_tr_b16 a[168:169], v230 offset:38000\n"
    "v_mfma_f32_16x16x32_bf16 v[52:55], a[116:119], a[64:67], v[52:55]\n"
    "buffer_load_dwordx4 v225, s[12:15], s34 offen lds\n"
    "ds_read_b64_tr_b16 a[170:171], v230 offset:41584\n"
    "v_mfma_f32_16x16x32_bf16 v[48:51], a[112:115], a[64:67], v[48:51]\n"
    "ds_read_b64_tr_b16 a[172:173], v230 offset:38032\n"
    "ds_read_b64_tr_b16 a[174:175], v230 offset:41616\n"
    "v_mfma_f32_16x16x32_bf16 v[44:47], a[108:111], a[64:67], v[44:47]\n"
    "ds_read_b64_tr_b16 a[176:177], v231 offset:30784\n"
    "ds_read_b64_tr_b16 a[178:179], v231 offset:34368\n"
    "v_mfma_f32_16x16x32_bf16 v[40:43], a[104:107], a[64:67], v[40:43]\n"
    "ds_read_b64 a[124:125], v229 offset:33792\n"
    "ds_read_b64 a[126:127], v229 offset:34304\n"
    "v_mfma_f32_16x16x32_bf16 v[36:39], a[100:103], a[64:67], v[36:39]\n"
    "s_add_u32 m0, s28, 17280\n"
    "ds_read_b64 a[128:129], v229 offset:34816\n"
    "v_mfma_f32_16x16x32_bf16 v[32:35], a[96:99], a[64:67], v[32:35]\n"
    "buffer_load_dwordx4 v224, s[8:11], s33 offen offset:128 sc1 lds\n"
    "ds_read_b64 a[130:131], v229 offset:35328\n"
    "v_mfma_f32_16x16x32_bf16 v[28:31], a[92:95], a[64:67], v[28:31]\n"
    "ds_read_b64 a[132:133], v229 offset:35840\n"
    "ds_read_b64 a[134:135], v229 offset:36352\n"
    "v_mfma_f32_16x16x32_bf16 v[56:59], a[92:95], a[68:71], v[56:59]\n"
    "ds_read_b64 a[136:137], v229 offset:36864\n"
    "ds_read_b64 a[138:139], v229 offset:37376\n"
    "v_mfma_f32_16x16x32_bf16 v[60:63], a[96:99], a[68:71], v[60:63]\n"
    "ds_read_b64 a[140:141], v229 offset:37888\n"
    "ds_read_b64 a[142:143], v229 offset:38400\n"
    "v_mfma_f32_16x16x32_bf16 v[64:67], a[100:103], a[68:71], v[64:67]\n"
    "ds_read_b64 a[144:145], v229 offset:38912\n"
    "ds_read_b64 a[146:147], v229 offset:39424\n"
    "v_mfma_f32_16x16x32_bf16 v[68:71], a[104:107], a[68:71], v[68:71]\n"
    "ds_read_b64 a[148:149], v229 offset:39936\n"
    "ds_read_b64 a[150:151], v229 offset:40448\n"
    "v_mfma_f32_16x16x32_bf16 v[72:75], a[108:111], a[68:71], v[72:75]\n"
    "ds_read_b64 a[180:181], v229 offset:49152\n"
    "ds_read_b64 a[182:183], v229 offset:49664\n"
    "v_mfma_f32_16x16x32_bf16 v[76:79], a[112:115], a[68:71], v[76:79]\n"
    "ds_read_b64_tr_b16 a[212:213], v230 offset:46176\n"
    "ds_read_b64_tr_b16 a[214:215], v230 offset:49760\n"
    "v_mfma_f32_16x16x32_bf16 v[80:83], a[116:119], a[68:71], v[80:83]\n"
    "ds_read_b64_tr_b16 a[216:217], v230 offset:46208\n"
    "ds_read_b64_tr_b16 a[218:219], v230 offset:49792\n"
    "v_mfma_f32_16x16x32_bf16 v[108:111], a[116:119], a[72:75], v[108:111]\n"
    "ds_read_b64_tr_b16 a[220:221], v230 offset:46240\n"
    "ds_read_b64_tr_b16 a[222:223], v230 offset:49824\n"
    "v_mfma_f32_16x16x32_bf16 v[104:107], a[112:115], a[72:75], v[104:107]\n"
    "ds_read_b64_tr_b16 a[224:225], v230 offset:53360\n"
    "ds_read_b64_tr_b16 a[226:227], v230 offset:56944\n"
    "v_mfma_f32_16x16x32_bf16 v[100:103], a[108:111], a[72:75], v[100:103]\n"
    "ds_read_b64_tr_b16 a[228:229], v230 offset:53392\n"
    "ds_read_b64_tr_b16 a[230:231], v230 offset:56976\n"
    "v_mfma_f32_16x16x32_bf16 v[96:99], a[104:107], a[72:75], v[96:99]\n"
    "ds_read_b64_tr_b16 a[232:233], v230 offset:53424\n"
    "ds_read_b64_tr_b16 a[234:235], v230 offset:57008\n"
    "v_mfma_f32_16x16x32_bf16 v[92:95], a[100:103], a[72:75], v[92:95]\n"
    "ds_read_b64_tr_b16 a[236:237], v231 offset:46176\n"
    "ds_read_b64_tr_b16 a[238:239], v231 offset:49760\n"
    "v_mfma_f32_16x16x32_bf16 v[88:91], a[96:99], a[72:75], v[88:91]\n"
    "s_add_u32 m0, s30, 15392\n"
    "ds_read_b64 a[184:185], v229 offset:50176\n"
    "v_mfma_f32_16x16x32_bf16 v[84:87], a[92:95], a[72:75], v[84:87]\n"
    "buffer_load_dwordx4 v226, s[16:19], s34 offen lds\n"
    "ds_read_b64 a[186:187], v229 offset:50688\n"
    "v_mfma_f32_16x16x32_bf16 v[112:115], a[92:95], a[76:79], v[112:115]\n"
    "ds_read_b64 a[188:189], v229 offset:51200\n"
    "ds_read_b64 a[190:191], v229 offset:51712\n"
    "v_mfma_f32_16x16x32_bf16 v[116:119], a[96:99], a[76:79], v[116:119]\n"
    "ds_read_b64 a[192:193], v229 offset:52224\n"
    "ds_read_b64 a[194:195], v229 offset:52736\n"
    "v_mfma_f32_16x16x32_bf16 v[120:123], a[100:103], a[76:79], v[120:123]\n"
    "ds_read_b64 a[196:197], v229 offset:53248\n"
    "ds_read_b64 a[198:199], v229 offset:53760\n"
    "v_mfma_f32_16x16x32_bf16 v[124:127], a[104:107], a[76:79], v[124:127]\n"
    "s_add_u32 m0, s28, 18176\n"
    "ds_read_b64 a[200:201], v229 offset:54272\n"
    "v_mfma_f32_16x16x32_bf16 v[128:131], a[108:111], a[76:79], v[128:131]\n"
    "buffer_load_dwordx4 v224, s[8:11], s33 offen offset:256 sc1 lds\n"
    "ds_read_b64 a[202:203], v229 offset:54784\n"
    "v_mfma_f32_16x16x32_bf16 v[132:135], a[112:115], a[76:79], v[132:135]\n"
    "ds_read_b64 a[204:205], v229 offset:55296\n"
    "ds_read_b64 a[206:207], v229 offset:55808\n"
    "v_mfma_f32_16x16x32_bf16 v[136:139], a[116:119], a[76:79], v[136:139]\n"
    "ds_read_b64 a[208:209], v229 offset:56320\n"
    "ds_read_b64 a[210:211], v229 offset:56832\n"
    "v_mfma_f32_16x16x32_bf16 v[164:167], a[116:119], a[80:83], v[164:167]\n"
    "v_mfma_f32_16x16x32_bf16 v[160:163], a[112:115], a[80:83], v[160:163]\n"
    "s_add_u32 m0, s31, 15392\n"
    "v_mfma_f32_16x16x32_bf16 v[156:159], a[108:111], a[80:83], v[156:159]\n"
    "buffer_load_dwordx4 v227, s[20:23], s34 offen lds\n"
    "v_mfma_f32_16x16x32_bf16 v[152:155], a[104:107], a[80:83], v[152:155]\n"
    "v_mfma_f32_16x16x32_bf16 v[148:151], a[100:103], a[80:83], v[148:151]\n"
    "buffer_load_dword v233, v232, s[48:51], 0 offen\n"
    "v_mfma_f32_16x16x32_bf16 v[144:147], a[96:99], a[80:83], v[144:147]\n"
    "v_add_u32 v232, s63, v232\n"
    "v_mfma_f32_16x16x32_bf16 v[140:143], a[92:95], a[80:83], v[140:143]\n"
    "v_mfma_f32_16x16x32_bf16 v[168:171], a[92:95], a[84:87], v[168:171]\n"
    "v_mfma_f32_16x16x32_bf16 v[172:175], a[96:99], a[84:87], v[172:175]\n"
    "s_add_u32 m0, s28, 19072\n"
    "v_mfma_f32_16x16x32_bf16 v[176:179], a[100:103], a[84:87], v[176:179]\n"
    "buffer_load_dwordx4 v224, s[8:11], s33 offen offset:384 sc1 lds\n"
    "v_mfma_f32_16x16x32_bf16 v[180:183], a[104:107], a[84:87], v[180:183]\n"
    "v_mfma_f32_16x16x32_bf16 v[184:187], a[108:111], a[84:87], v[184:187]\n"
    "v_mfma_f32_16x16x32_bf16 v[188:191], a[112:115], a[84:87], v[188:191]\n"
    "v_mfma_f32_16x16x32_bf16 v[192:195], a[116:119], a[84:87], v[192:195]\n"
    "v_mfma_f32_16x16x32_bf16 v[220:223], a[116:119], a[88:91], v[220:223]\n"
    "s_add_u32 m0, s32, 15392\n"
    "v_mfma_f32_16x16x32_bf16 v[216:219], a[112:115], a[88:91], v[216:219]\n"
    "S2_H0%=:\n"
    "s_cbranch_execz S2_H3%=\n"
    "buffer_load_dwordx4 v228, s[24:27], s34 offen lds\n"
    "v_mfma_f32_16x16x32_bf16 v[212:215], a[108:111], a[88:91], v[212:215]\n"
    "s_cmp_eq_u32 s33, s53\n"
    "s_cselect_b32 s59, 655360, s56\n"
    "v_mfma_f32_16x16x32_bf16 v[208:211], a[104:107], a[88:91], v[208:211]\n"
    "s_cselect_b32 s62, 1146880, s57\n"
    "s_add_u32 s33, s33, s59\n"
    "v_mfma_f32_16x16x32_bf16 v[204:207], a[100:103], a[88:91], v[204:207]\n"
    "s_add_u32 s34, s34, s62\n"
    "v_mfma_f32_16x16x32_bf16 v[200:203], a[96:99], a[88:91], v[200:203]\n"
    "v_mfma_f32_16x16x32_bf16 v[196:199], a[92:95], a[88:91], v[196:199]\n"
    "s_waitcnt vmcnt(9) lgkmcnt(0)\n"
    "s_barrier\n"
    "v_mfma_f32_16x16x32_bf16 v[0:3], a[152:155], a[120:123], v[0:3]\n"
    "ds_read_b64 a[0:1], v229 offset:0\n"
    "ds_read_b64 a[2:3], v229 offset:512\n"
    "v_mfma_f32_16x16x32_bf16 v[0:3], a[212:215], a[180:183], v[0:3]\n"
    "s_add_u32 m0, s28, 32768\n"
    "ds_read_b64_tr_b16 a[32:33], v230 offset:0\n"
    "v_mfma_f32_16x16x32_bf16 v[4:7], a[216:219], a[180:183], v[4:7]\n"
    "buffer_load_dwordx4 v224, s[8:11], s33 offen offset:0 sc1 lds\n"
    "ds_read_b64_tr_b16 a[34:35], v230 offset:3584\n"
    "v_mfma_f32_16x16x32_bf16 v[4:7], a[156:159], a[120:123], v[4:7]\n"
    "ds_read_b64_tr_b16 a[36:37], v230 offset:32\n"
    "ds_read_b64_tr_b16 a[38:39], v230 offset:3616\n"
    "v_mfma_f32_16x16x32_bf16 v[8:11], a[160:163], a[120:123], v[8:11]\n"
    "s_add_u32 m0, s29, 30784\n"
    "ds_read_b64_tr_b16 a[40:41], v230 offset:64\n"
    "v_mfma_f32_16x16x32_bf16 v[8:11], a[220:223], a[180:183], v[8:11]\n"
    "buffer_load_dwordx4 v225, s[12:15], s34 offen lds\n"
    "ds_read_b64_tr_b16 a[42:43], v230 offset:3648\n"
    "v_mfma_f32_16x16x32_bf16 v[12:15], a[224:227], a[180:183], v[12:15]\n"
    "ds_read_b64_tr_b16 a[44:45], v230 offset:7184\n"
    "ds_read_b64_tr_b16 a[46:47], v230 offset:10768\n"
    "v_mfma_f32_16x16x32_bf16 v[12:15], a[164:167], a[120:123], v[12:15]\n"
    "s_add_u32 m0, s28, 33664\n"
    "ds_read_b64_tr_b16 a[48:49], v230 offset:7216\n"
    "v_mfma_f32_16x16x32_bf16 v[16:19], a[168:171], a[120:123], v[16:19]\n"
    "buffer_load_dwordx4 v224, s[8:11], s33 offen offset:128 sc1 lds\n"
    "ds_read_b64_tr_b16 a[50:51], v230 offset:10800\n"
    "v_mfma_f32_16x16x32_bf16 v[16:19], a[228:231], a[180:183], v[16:19]\n"
    "ds_read_b64_tr_b16 a[52:53], v230 offset:7248\n"
    "ds_read_b64_tr_b16 a[54:55], v230 offset:10832\n"
    "v_mfma_f32_16x16x32_bf16 v[20:23], a[232:235], a[180:183], v[20:23]\n"
    "s_add_u32 m0, s30, 30784\n"
    "ds_read_b64_tr_b16 a[56:57], v231 offset:0\n"
    "v_mfma_f32_16x16x32_bf16 v[20:23], a[172:175], a[120:123], v[20:23]\n"
    "buffer_load_dwordx4 v226, s[16:19], s34 offen lds\n"
    "ds_read_b64_tr_b16 a[58:59], v231 offset:3584\n"
    "v_mfma_f32_16x16x32_bf16 v[24:27], a[176:179], a[120:123], v[24:27]\n"
    "ds_read_b64 a[4:5], v229 offset:1024\n"
    "ds_read_b64 a[6:7], v229 offset:1536\n"
    "v_mfma_f32_16x16x32_bf16 v[24:27], a[236:239], a[180:183], v[24:27]\n"
    "s_add_u32 m0, s28, 34560\n"
    "ds_read_b64 a[8:9], v229 offset:2048\n"
    "v_mfma_f32_16x16x32_bf16 v[52:55], a[236:239], a[184:187], v[52:55]\n"
    "buffer_load_dwordx4 v224, s[8:11], s33 offen offset:256 sc1 lds\n"
    "ds_read_b64 a[10:11], v229 offset:2560\n"
    "v_mfma_f32_16x16x32_bf16 v[52:55], a[176:179], a[124:127], v[52:55]\n"
    "ds_read_b64 a[12:13], v229 offset:3072\n"
    "ds_read_b64 a[14:15], v229 offset:3584\n"
    "v_mfma_f32_16x16x32_bf16 v[48:51], a[172:175], a[124:127], v[48:51]\n"
    "s_add_u32 m0, s31, 30784\n"
    "ds_read_b64 a[16:17], v229 offset:4096\n"
    "v_mfma_f32_16x16x32_bf16 v[48:51], a[232:235], a[184:187], v[48:51]\n"
    "buffer_load_dwordx4 v227, s[20:23], s34 offen lds\n"
    "ds_read_b64 a[18:19], v229 offset:4608\n"
    "v_mfma_f32_16x16x32_bf16 v[44:47], a[228:231], a[184:187], v[44:47]\n"
    "ds_read_b64 a[20:21], v229 offset:5120\n"
    "ds_read_b64 a[22:23], v229 offset:5632\n"
    "v_mfma_f32_16x16x32_bf16 v[44:47], a[168:171], a[124:127], v[44:47]\n"
    "s_add_u32 m0, s28, 35456\n"
    "ds_read_b64 a[24:25], v229 offset:6144\n"
    "v_mfma_f32_16x16x32_bf16 v[40:43], a[164:167], a[124:127], v[40:43]\n"
    "buffer_load_dwordx4 v224, s[8:11], s33 offen offset:384 sc1 lds\n"
    "ds_read_b64 a[26:27], v229 offset:6656\n"
    "v_mfma_f32_16x16x32_bf16 v[40:43], a[224:227], a[184:187], v[40:43]\n"
    "ds_read_b64 a[28:29], v229 offset:7168\n"
    "ds_read_b64 a[30:31], v229 offset:7680\n"
    "v_mfma_f32_16x16x32_bf16 v[36:39], a[220:223], a[184:187], v[36:39]\n"
    "s_add_u32 m0, s32, 30784\n"
    "v_mfma_f32_16x16x32_bf16 v[36:39], a[160:163], a[124:127], v[36:39]\n"
    "buffer_load_dwordx4 v228, s[24:27], s34 offen lds\n"
    "v_mfma_f32_16x16x32_bf16 v[32:35], a[156:159], a[124:127], v[32:35]\n"
    "s_cmp_eq_u32 s33, s53\n"
    "s_cselect_b32 s59, 655360, s56\n"
    "v_mfma_f32_16x16x32_bf16 v[32:35], a[216:219], a[184:187], v[32:35]\n"
    "s_cselect_b32 s62, 1146880, s57\n"
    "s_add_u32 s33, s33, s59\n"
    "v_mfma_f32_16x16x32_bf16 v[28:31], a[212:215], a[184:187], v[28:31]\n"
    "s_add_u32 s34, s34, s62\n"
    "v_mfma_f32_16x16x32_bf16 v[28:31], a[152:155], a[124:127], v[28:31]\n"
    "s_add_u32 m0, s28, 49152\n"
    "v_mfma_f32_16x16x32_bf16 v[56:59], a[152:155], a[128:131], v[56:59]\n"
    "buffer_load_dwordx4 v224, s[8:11], s33 offen offset:0 sc1 lds\n"
    "v_mfma_f32_16x16x32_bf16 v[56:59], a[212:215], a[188:191], v[56:59]\n"
    "v_mfma_f32_16x16x32_bf16 v[60:63], a[216:219], a[188:191], v[60:63]\n"
    "s_add_u32 m0, s29, 46176\n"
    "v_mfma_f32_16x16x32_bf16 v[60:63], a[156:159], a[128:131], v[60:63]\n"
    "buffer_load_dwordx4 v225, s[12:15], s34 offen lds\n"
    "v_mfma_f32_16x16x32_bf16 v[64:67], a[160:163], a[128:131], v[64:67]\n"
    "v_mfma_f32_16x16x32_bf16 v[64:67], a[220:223], a[188:191], v[64:67]\n"
    "s_add_u32 m0, s28, 50048\n"
    "v_mfma_f32_16x16x32_bf16 v[68:71], a[224:227], a[188:191], v[68:71]\n"
    "buffer_load_dwordx4 v224, s[8:11], s33 offen offset:128 sc1 lds\n"
    "v_mfma_f32_16x16x32_bf16 v[68:71], a[164:167], a[128:131], v[68:71]\n"
    "v_mfma_f32_16x16x32_bf16 v[72:75], a[168:171], a[128:131], v[72:75]\n"
    "s_add_u32 m0, s30, 46176\n"
    "v_mfma_f32_16x16x32_bf16 v[72:75], a[228:231], a[188:191], v[72:75]\n"
    "buffer_load_dwordx4 v226, s[16:19], s34 offen lds\n"
    "v_mfma_f32_16x16x32_bf16 v[76:79], a[232:235], a[188:191], v[76:79]\n"
    "v_mfma_f32_16x16x32_bf16 v[76:79], a[172:175], a[128:131], v[76:79]\n"
    "s_add_u32 m0, s28, 50944\n"
    "buffer_load_dword v233, v232, s[48:51], 0 offen\n"
    "v_mfma_f32_16x16x32_bf16 v[80:83], a[176:179], a[128:131], v[80:83]\n"
    "buffer_load_dwordx4 v224, s[8:11], s33 offen offset:256 sc1 lds\n"
    "v_add_u32 v232, s63, v232\n"
    "v_mfma_f32_16x16x32_bf16 v[80:83], a[236:239], a[188:191], v[80:83]\n"
    "v_mfma_f32_16x16x32_bf16 v[108:111], a[236:239], a[192:195], v[108:111]\n"
    "s_add_u32 m0, s31, 46176\n"
    "v_mfma_f32_16x16x32_bf16 v[108:111], a[176:179], a[132:135], v[108:111]\n"
    "buffer_load_dwordx4 v227, s[20:23], s34 offen lds\n"
    "v_mfma_f32_16x16x32_bf16 v[104:107], a[172:175], a[132:135], v[104:107]\n"
    "v_mfma_f32_16x16x32_bf16 v[104:107], a[232:235], a[192:195], v[104:107]\n"
    "s_add_u32 m0, s28, 51840\n"
    "v_mfma_f32_16x16x32_bf16 v[100:103], a[228:231], a[192:195], v[100:103]\n"
    "buffer_load_dwordx4 v224, s[8:11], s33 offen offset:384 sc1 lds\n"
    "v_mfma_f32_16x16x32_bf16 v[100:103], a[168:171], a[132:135], v[100:103]\n"
    "v_mfma_f32_16x16x32_bf16 v[96:99], a[164:167], a[132:135], v[96:99]\n"
    "s_add_u32 m0, s32, 46176\n"
    "v_mfma_f32_16x16x32_bf16 v[96:99], a[224:227], a[192:195], v[96:99]\n"
    "buffer_load_dwordx4 v228, s[24:27], s34 offen lds\n"
    "v_mfma_f32_16x16x32_bf16 v[92:95], a[220:223], a[192:195], v[92:95]\n"
    "s_cmp_eq_u32 s33, s53\n"
    "s_cselect_b32 s59, 655360, s56\n"
    "v_mfma_f32_16x16x32_bf16 v[92:95], a[160:163], a[132:135], v[92:95]\n"
    "s_cselect_b32 s62, 1146880, s57\n"
    "s_add_u32 s33, s33, s59\n"
    "v_mfma_f32_16x16x32_bf16 v[88:91], a[156:159], a[132:135], v[88:91]\n"
    "s_add_u32 s34, s34, s62\n"
    "v_mfma_f32_16x16x32_bf16 v[88:91], a[216:219], a[192:195], v[88:91]\n"
    "v_mfma_f32_16x16x32_bf16 v[84:87], a[212:215], a[192:195], v[84:87]\n"
    "v_mfma_f32_16x16x32_bf16 v[84:87], a[152:155], a[132:135], v[84:87]\n"
    "s_waitcnt vmcnt(17) lgkmcnt(0)\n"
    "s_barrier\n"
    "v_mfma_f32_16x16x32_bf16 v[112:115], a[152:155], a[136:139], v[112:115]\n"
    "ds_read_b64 a[60:61], v229 offset:16384\n"
    "v_lshlrev_b32 v236, 16, v252\n"
    "v_mfma_f32_16x16x32_bf16 v[112:115], a[212:215], a[196:199], v[112:115]\n"
    "s_add_u32 m0, s28, 0\n"
    "v_and_b32 v237, 0xffff0000, v252\n"
    "v_mfma_f32_16x16x32_bf16 v[116:119], a[216:219], a[196:199], v[116:119]\n"
    "buffer_load_dwordx4 v224, s[8:11], s33 offen offset:0 sc1 lds\n"
    "v_mfma_f32_16x16x32_bf16 v[116:119], a[156:159], a[136:139], v[116:119]\n"
    "ds_read_b64 a[62:63], v229 offset:16896\n"
    "v_mul_f32 v246, v236, v236\n"
    "v_mfma_f32_16x16x32_bf16 v[120:123], a[160:163], a[136:139], v[120:123]\n"
    "ds_read_b64_tr_b16 a[92:93], v230 offset:15392\n"
    "v_mul_f32 v247, v237, v237\n"
    "v_mfma_f32_16x16x32_bf16 v[120:123], a[220:223], a[196:199], v[120:123]\n"
    "ds_read_b64_tr_b16 a[94:95], v230 offset:18976\n"
    "v_mfma_f32_16x16x32_bf16 v[124:127], a[224:227], a[196:199], v[124:127]\n"
    "s_add_u32 m0, s29, 0\n"
    "v_lshlrev_b32 v234, 16, v253\n"
    "v_mfma_f32_16x16x32_bf16 v[124:127], a[164:167], a[136:139], v[124:127]\n"
    "buffer_load_dwordx4 v225, s[12:15], s34 offen lds\n"
    "v_and_b32 v235, 0xffff0000, v253\n"
    "v_mfma_f32_16x16x32_bf16 v[128:131], a[168:171], a[136:139], v[128:131]\n"
    "ds_read_b64_tr_b16 a[96:97], v230 offset:15424\n"
    "v_mfma_f32_16x16x32_bf16 v[128:131], a[228:231], a[196:199], v[128:131]\n"
    "ds_read_b64_tr_b16 a[98:99], v230 offset:19008\n"
    "v_add_f32 v236, v236, v234\n"
    "v_mfma_f32_16x16x32_bf16 v[132:135], a[232:235], a[196:199], v[132:135]\n"
    "ds_read_b64_tr_b16 a[100:101], v230 offset:15456\n"
    "v_mfma_f32_16x16x32_bf16 v[132:135], a[172:175], a[136:139], v[132:135]\n"
    "s_add_u32 m0, s28, 896\n"
    "v_fmac_f32 v246, v234, v234\n"
    "v_mfma_f32_16x16x32_bf16 v[136:139], a[176:179], a[136:139], v[136:139]\n"
    "buffer_load_dwordx4 v224, s[8:11], s33 offen offset:128 sc1 lds\n"
    "v_add_f32 v237, v237, v235\n"
    "v_mfma_f32_16x16x32_bf16 v[136:139], a[236:239], a[196:199], v[136:139]\n"
    "ds_read_b64_tr_b16 a[102:103], v230 offset:19040\n"
    "v_mfma_f32_16x16x32_bf16 v[164:167], a[236:239], a[200:203], v[164:167]\n"
    "ds_read_b64_tr_b16 a[104:105], v230 offset:22576\n"
    "v_fmac_f32 v247, v235, v235\n"
    "v_mfma_f32_16x16x32_bf16 v[164:167], a[176:179], a[140:143], v[164:167]\n"
    "ds_read_b64_tr_b16 a[106:107], v230 offset:26160\n"
    "v_lshlrev_b32 v238, 16, v254\n"
    "v_mfma_f32_16x16x32_bf16 v[160:163], a[172:175], a[140:143], v[160:163]\n"
    "ds_read_b64_tr_b16 a[108:109], v230 offset:22608\n"
    "v_mfma_f32_16x16x32_bf16 v[160:163], a[232:235], a[200:203], v[160:163]\n"
    "ds_read_b64_tr_b16 a[110:111], v230 offset:26192\n"
    "v_and_b32 v239, 0xffff0000, v254\n"
    "v_mfma_f32_16x16x32_bf16 v[156:159], a[228:231], a[200:203], v[156:159]\n"
    "ds_read_b64_tr_b16 a[112:113], v230 offset:22640\n"
    "v_mfma_f32_16x16x32_bf16 v[156:159], a[168:171], a[140:143], v[156:159]\n"
    "ds_read_b64_tr_b16 a[114:115], v230 offset:26224\n"
    "v_mul_f32 v248, v238, v238\n"
    "v_mfma_f32_16x16x32_bf16 v[152:155], a[164:167], a[140:143], v[152:155]\n"
    "ds_read_b64_tr_b16 a[116:117], v231 offset:15392\n"
    "v_mul_f32 v249, v239, v239\n"
    "v_mfma_f32_16x16x32_bf16 v[152:155], a[224:227], a[200:203], v[152:155]\n"
    "ds_read_b64_tr_b16 a[118:119], v231 offset:18976\n"
    "v_mfma_f32_16x16x32_bf16 v[148:151], a[220:223], a[200:203], v[148:151]\n"
    "ds_read_b64 a[64:65], v229 offset:17408\n"
    "v_lshlrev_b32 v234, 16, v255\n"
    "v_mfma_f32_16x16x32_bf16 v[148:151], a[160:163], a[140:143], v[148:151]\n"
    "ds_read_b64 a[66:67], v229 offset:17920\n"
    "v_and_b32 v235, 0xffff0000, v255\n"
    "v_mfma_f32_16x16x32_bf16 v[144:147], a[156:159], a[140:143], v[144:147]\n"
    "ds_read_b64 a[68:69], v229 offset:18432\n"
    "v_mfma_f32_16x16x32_bf16 v[144:147], a[216:219], a[200:203], v[144:147]\n"
    "ds_read_b64 a[70:71], v229 offset:18944\n"
    "v_add_f32 v238, v238, v234\n"
    "v_mfma_f32_16x16x32_bf16 v[140:143], a[212:215], a[200:203], v[140:143]\n"
    "s_add_u32 m0, s30, 0\n"
    "v_fmac_f32 v248, v234, v234\n"
    "v_mfma_f32_16x16x32_bf16 v[140:143], a[152:155], a[140:143], v[140:143]\n"
    "buffer_load_dwordx4 v226, s[16:19], s34 offen lds\n"
    "v_mfma_f32_16x16x32_bf16 v[168:171], a[152:155], a[144:147], v[168:171]\n"
    "ds_read_b64 a[72:73], v229 offset:19456\n"
    "v_add_f32 v239, v239, v235\n"
    "v_mfma_f32_16x16x32_bf16 v[168:171], a[212:215], a[204:207], v[168:171]\n"
    "ds_read_b64 a[74:75], v229 offset:19968\n"
    "v_mfma_f32_16x16x32_bf16 v[172:175], a[216:219], a[204:207], v[172:175]\n"
    "ds_read_b64 a[76:77], v229 offset:20480\n"
    "v_fmac_f32 v249, v235, v235\n"
    "v_mfma_f32_16x16x32_bf16 v[172:175], a[156:159], a[144:147], v[172:175]\n"
    "s_add_u32 m0, s28, 1792\n"
    "v_accvgpr_read_b32 v240, a244\n"
    "v_mfma_f32_16x16x32_bf16 v[176:179], a[160:163], a[144:147], v[176:179]\n"
    "buffer_load_dwordx4 v224, s[8:11], s33 offen offset:256 sc1 lds\n"
    "v_mfma_f32_16x16x32_bf16 v[176:179], a[220:223], a[204:207], v[176:179]\n"
    "ds_read_b64 a[78:79], v229 offset:20992\n"
    "v_lshlrev_b32 v234, 16, v240\n"
    "v_mfma_f32_16x16x32_bf16 v[180:183], a[224:227], a[204:207], v[180:183]\n"
    "ds_read_b64 a[80:81], v229 offset:21504\n"
    "v_and_b32 v235, 0xffff0000, v240\n"
    "v_mfma_f32_16x16x32_bf16 v[180:183], a[164:167], a[144:147], v[180:183]\n"
    "ds_read_b64 a[82:83], v229 offset:22016\n"
    "v_mfma_f32_16x16x32_bf16 v[184:187], a[168:171], a[144:147], v[184:187]\n"
    "s_add_u32 m0, s31, 0\n"
    "v_add_f32 v236, v236, v234\n"
    "v_mfma_f32_16x16x32_bf16 v[184:187], a[228:231], a[204:207], v[184:187]\n"
    "buffer_load_dwordx4 v227, s[20:23], s34 offen lds\n"
    "v_mfma_f32_16x16x32_bf16 v[188:191], a[232:235], a[204:207], v[188:191]\n"
    "ds_read_b64 a[84:85], v229 offset:22528\n"
    "v_fmac_f32 v246, v234, v234\n"
    "v_mfma_f32_16x16x32_bf16 v[188:191], a[172:175], a[144:147], v[188:191]\n"
    "buffer_load_dword v233, v232, s[48:51], 0 offen\n"
    "v_add_f32 v237, v237, v235\n"
    "v_mfma_f32_16x16x32_bf16 v[192:195], a[176:179], a[144:147], v[192:195]\n"
    "v_add_u32 v232, s63, v232\n"
    "v_mfma_f32_16x16x32_bf16 v[192:195], a[236:239], a[204:207], v[192:195]\n"
    "ds_read_b64 a[86:87], v229 offset:23040\n"
    "v_fmac_f32 v247, v235, v235\n"
    "v_mfma_f32_16x16x32_bf16 v[220:223], a[236:239], a[208:211], v[220:223]\n"
    "ds_read_b64 a[88:89], v229 offset:23552\n"
    "v_accvgpr_read_b32 v240, a245\n"
    "v_mfma_f32_16x16x32_bf16 v[220:223], a[176:179], a[148:151], v[220:223]\n"
    "s_add_u32 m0, s28, 2688\n"
    "v_mfma_f32_16x16x32_bf16 v[216:219], a[172:175], a[148:151], v[216:219]\n"
    "buffer_load_dwordx4 v224, s[8:11], s33 offen offset:384 sc1 lds\n"
    "v_lshlrev_b32 v234, 16, v240\n"
    "v_mfma_f32_16x16x32_bf16 v[216:219], a[232:235], a[208:211], v[216:219]\n"
    "ds_read_b64 a[90:91], v229 offset:24064\n"
    "v_and_b32 v235, 0xffff0000, v240\n"
    "v_mfma_f32_16x16x32_bf16 v[212:215], a[228:231], a[208:211], v[212:215]\n"
    "v_add_f32 v236, v236, v234\n"
    "v_mfma_f32_16x16x32_bf16 v[212:215], a[168:171], a[148:151], v[212:215]\n"
    "v_fmac_f32 v246, v234, v234\n"
    "v_mfma_f32_16x16x32_bf16 v[208:211], a[164:167], a[148:151], v[208:211]\n"
    "v_add_f32 v237, v237, v235\n"
    "v_mfma_f32_16x16x32_bf16 v[208:211], a[224:227], a[208:211], v[208:211]\n"
    "s_add_u32 m0, s32, 0\n"
    "v_fmac_f32 v247, v235, v235\n"
    "v_mfma_f32_16x16x32_bf16 v[204:207], a[220:223], a[208:211], v[204:207]\n"
    "buffer_load_dwordx4 v228, s[24:27], s34 offen lds\n"
    "v_accvgpr_read_b32 v240, a246\n"
    "v_mfma_f32_16x16x32_bf16 v[204:207], a[160:163], a[148:151], v[204:207]\n"
    "s_cmp_eq_u32 s33, s53\n"
    "s_cselect_b32 s59, 655360, s56\n"
    "v_mfma_f32_16x16x32_bf16 v[200:203], a[156:159], a[148:151], v[200:203]\n"
    "s_cselect_b32 s62, 1146880, s57\n"
    "s_add_u32 s33, s33, s59\n"
    "v_mfma_f32_16x16x32_bf16 v[200:203], a[216:219], a[208:211], v[200:203]\n"
    "s_add_u32 s34, s34, s62\n"
    "v_mfma_f32_16x16x32_bf16 v[196:199], a[212:215], a[208:211], v[196:199]\n"
    "v_lshlrev_b32 v234, 16, v240\n"
    "v_mfma_f32_16x16x32_bf16 v[196:199], a[152:155], a[148:151], v[196:199]\n"
    "s_waitcnt vmcnt(18) lgkmcnt(0)\n"
    "s_barrier\n"
    "v_mfma_f32_16x16x32_bf16 v[0:3], a[32:35], a[0:3], v[0:3]\n"
    "ds_read_b64 a[120:121], v229 offset:32768\n"
    "v_and_b32 v235, 0xffff0000, v240\n"
    "v_mfma_f32_16x16x32_bf16 v[0:3], a[92:95], a[60:63], v[0:3]\n"
    "s_add_u32 m0, s28, 16384\n"
    "v_add_f32 v238, v238, v234\n"
    "v_mfma_f32_16x16x32_bf16 v[4:7], a[96:99], a[60:63], v[4:7]\n"
    "buffer_load_dwordx4 v224, s[8:11], s33 offen offset:0 sc1 lds\n"
    "v_mfma_f32_16x16x32_bf16 v[4:7], a[36:39], a[0:3], v[4:7]\n"
    "ds_read_b64 a[122:123], v229 offset:33280\n"
    "v_fmac_f32 v248, v234, v234\n"
    "v_mfma_f32_16x16x32_bf16 v[8:11], a[40:43], a[0:3], v[8:11]\n"
    "ds_read_b64_tr_b16 a[152:153], v230 offset:30784\n"
    "v_add_f32 v239, v239, v235\n"
    "v_mfma_f32_16x16x32_bf16 v[8:11], a[100:103], a[60:63], v[8:11]\n"
    "ds_read_b64_tr_b16 a[154:155], v230 offset:34368\n"
    "v_mfma_f32_16x16x32_bf16 v[12:15], a[104:107], a[60:63], v[12:15]\n"
    "s_add_u32 m0, s29, 15392\n"
    "v_fmac_f32 v249, v235, v235\n"
    "v_mfma_f32_16x16x32_bf16 v[12:15], a[44:47], a[0:3], v[12:15]\n"
    "buffer_load_dwordx4 v225, s[12:15], s34 offen lds\n"
    "v_accvgpr_read_b32 v240, a247\n"
    "v_mfma_f32_16x16x32_bf16 v[16:19], a[48:51], a[0:3], v[16:19]\n"
    "ds_read_b64_tr_b16 a[156:157], v230 offset:30816\n"
    "v_mfma_f32_16x16x32_bf16 v[16:19], a[108:111], a[60:63], v[16:19]\n"
    "ds_read_b64_tr_b16 a[158:159], v230 offset:34400\n"
    "v_lshlrev_b32 v234, 16, v240\n"
    "v_mfma_f32_16x16x32_bf16 v[20:23], a[112:115], a[60:63], v[20:23]\n"
    "ds_read_b64_tr_b16 a[160:161], v230 offset:30848\n"
    "v_mfma_f32_16x16x32_bf16 v[20:23], a[52:55], a[0:3], v[20:23]\n"
    "s_add_u32 m0, s28, 17280\n"
    "v_and_b32 v235, 0xffff0000, v240\n"
    "v_mfma_f32_16x16x32_bf16 v[24:27], a[56:59], a[0:3], v[24:27]\n"
    "buffer_load_dwordx4 v224, s[8:11], s33 offen offset:128 sc1 lds\n"
    "v_add_f32 v238, v238, v234\n"
    "v_mfma_f32_16x16x32_bf16 v[24:27], a[116:119], a[60:63], v[24:27]\n"
    "ds_read_b64_tr_b16 a[162:163], v230 offset:34432\n"
    "v_mfma_f32_16x16x32_bf16 v[52:55], a[116:119], a[64:67], v[52:55]\n"
    "ds_read_b64_tr_b16 a[164:165], v230 offset:37968\n"
    "v_fmac_f32 v248, v234, v234\n"
    "v_mfma_f32_16x16x32_bf16 v[52:55], a[56:59], a[4:7], v[52:55]\n"
    "ds_read_b64_tr_b16 a[166:167], v230 offset:41552\n"
    "v_add_f32 v239, v239, v235\n"
    "v_mfma_f32_16x16x32_bf16 v[48:51], a[52:55], a[4:7], v[48:51]\n"
    "ds_read_b64_tr_b16 a[168:169], v230 offset:38000\n"
    "v_mfma_f32_16x16x32_bf16 v[48:51], a[112:115], a[64:67], v[48:51]\n"
    "ds_read_b64_tr_b16 a[170:171], v230 offset:41584\n"
    "v_fmac_f32 v249, v235, v235\n"
    "v_mfma_f32_16x16x32_bf16 v[44:47], a[108:111], a[64:67], v[44:47]\n"
    "ds_read_b64_tr_b16 a[172:173], v230 offset:38032\n"
    "v_mfma_f32_16x16x32_bf16 v[44:47], a[48:51], a[4:7], v[44:47]\n"
    "ds_read_b64_tr_b16 a[174:175], v230 offset:41616\n"
    "v_accvgpr_read_b32 v240, a248\n"
    "v_mfma_f32_16x16x32_bf16 v[40:43], a[44:47], a[4:7], v[40:43]\n"
    "ds_read_b64_tr_b16 a[176:177], v231 offset:30784\n"
    "v_lshlrev_b32 v234, 16, v240\n"
    "v_mfma_f32_16x16x32_bf16 v[40:43], a[104:107], a[64:67], v[40:43]\n"
    "ds_read_b64_tr_b16 a[178:179], v231 offset:34368\n"
    "v_mfma_f32_16x16x32_bf16 v[36:39], a[100:103], a[64:67], v[36:39]\n"
    "ds_read_b64 a[124:125], v229 offset:33792\n"
    "v_and_b32 v235, 0xffff0000, v240\n"
    "v_mfma_f32_16x16x32_bf16 v[36:39], a[40:43], a[4:7], v[36:39]\n"
    "ds_read_b64 a[126:127], v229 offset:34304\n"
    "v_add_f32 v236, v236, v234\n"
    "v_mfma_f32_16x16x32_bf16 v[32:35], a[36:39], a[4:7], v[32:35]\n"
    "ds_read_b64 a[128:129], v229 offset:34816\n"
    "v_mfma_f32_16x16x32_bf16 v[32:35], a[96:99], a[64:67], v[32:35]\n"
    "ds_read_b64 a[130:131], v229 offset:35328\n"
    "v_fmac_f32 v246, v234, v234\n"
    "v_mfma_f32_16x16x32_bf16 v[28:31], a[92:95], a[64:67], v[28:31]\n"
    "s_add_u32 m0, s30, 15392\n"
    "v_add_f32 v237, v237, v235\n"
    "v_mfma_f32_16x16x32_bf16 v[28:31], a[32:35], a[4:7], v[28:31]\n"
    "buffer_load_dwordx4 v226, s[16:19], s34 offen lds\n"
    "v_mfma_f32_16x16x32_bf16 v[56:59], a[32:35], a[8:11], v[56:59]\n"
    "ds_read_b64 a[132:133], v229 offset:35840\n"
    "v_fmac_f32 v247, v235, v235\n"
    "v_mfma_f32_16x16x32_bf16 v[56:59], a[92:95], a[68:71], v[56:59]\n"
    "ds_read_b64 a[134:135], v229 offset:36352\n"
    "v_mfma_f32_16x16x32_bf16 v[60:63], a[96:99], a[68:71], v[60:63]\n"
    "ds_read_b64 a[136:137], v229 offset:36864\n"
    "v_accvgpr_read_b32 v240, a249\n"
    "v_mfma_f32_16x16x32_bf16 v[60:63], a[36:39], a[8:11], v[60:63]\n"
    "s_add_u32 m0, s28, 18176\n"
    "v_lshlrev_b32 v234, 16, v240\n"
    "v_mfma_f32_16x16x32_bf16 v[64:67], a[40:43], a[8:11], v[64:67]\n"
    "buffer_load_dwordx4 v224, s[8:11], s33 offen offset:256 sc1 lds\n"
    "v_mfma_f32_16x16x32_bf16 v[64:67], a[100:103], a[68:71], v[64:67]\n"
    "ds_read_b64 a[138:139], v229 offset:37376\n"
    "v_and_b32 v235, 0xffff0000, v240\n"
    "v_mfma_f32_16x16x32_bf16 v[68:71], a[104:107], a[68:71], v[68:71]\n"
    "ds_read_b64 a[140:141], v229 offset:37888\n"
    "v_add_f32 v236, v236, v234\n"
    "v_mfma_f32_16x16x32_bf16 v[68:71], a[44:47], a[8:11], v[68:71]\n"
    "ds_read_b64 a[142:143], v229 offset:38400\n"
    "v_mfma_f32_16x16x32_bf16 v[72:75], a[48:51], a[8:11], v[72:75]\n"
    "s_add_u32 m0, s31, 15392\n"
    "v_fmac_f32 v246, v234, v234\n"
    "v_mfma_f32_16x16x32_bf16 v[72:75], a[108:111], a[68:71], v[72:75]\n"
    "buffer_load_dwordx4 v227, s[20:23], s34 offen lds\n"
    "v_mfma_f32_16x16x32_bf16 v[76:79], a[112:115], a[68:71], v[76:79]\n"
    "ds_read_b64 a[144:145], v229 offset:38912\n"
    "v_add_f32 v237, v237, v235\n"
    "v_mfma_f32_16x16x32_bf16 v[76:79], a[52:55], a[8:11], v[76:79]\n"
    "buffer_load_dword v233, v232, s[48:51], 0 offen\n"
    "v_fmac_f32 v247, v235, v235\n"
    "v_mfma_f32_16x16x32_bf16 v[80:83], a[56:59], a[8:11], v[80:83]\n"
    "v_add_u32 v232, s63, v232\n"
    "v_mfma_f32_16x16x32_bf16 v[80:83], a[116:119], a[68:71], v[80:83]\n"
    "ds_read_b64 a[146:147], v229 offset:39424\n"
    "v_accvgpr_read_b32 v240, a250\n"
    "v_mfma_f32_16x16x32_bf16 v[108:111], a[116:119], a[72:75], v[108:111]\n"
    "ds_read_b64 a[148:149], v229 offset:39936\n"
    "v_lshlrev_b32 v234, 16, v240\n"
    "v_mfma_f32_16x16x32_bf16 v[108:111], a[56:59], a[12:15], v[108:111]\n"
    "s_add_u32 m0, s28, 19072\n"
    "v_mfma_f32_16x16x32_bf16 v[104:107], a[52:55], a[12:15], v[104:107]\n"
    "buffer_load_dwordx4 v224, s[8:11], s33 offen offset:384 sc1 lds\n"
    "v_and_b32 v235, 0xffff0000, v240\n"
    "v_mfma_f32_16x16x32_bf16 v[104:107], a[112:115], a[72:75], v[104:107]\n"
    "ds_read_b64 a[150:151], v229 offset:40448\n"
    "v_add_f32 v238, v238, v234\n"
    "v_mfma_f32_16x16x32_bf16 v[100:103], a[108:111], a[72:75], v[100:103]\n"
    "v_fmac_f32 v248, v234, v234\n"
    "v_mfma_f32_16x16x32_bf16 v[100:103], a[48:51], a[12:15], v[100:103]\n"
    "v_add_f32 v239, v239, v235\n"
    "v_mfma_f32_16x16x32_bf16 v[96:99], a[44:47], a[12:15], v[96:99]\n"
    "v_fmac_f32 v249, v235, v235\n"
    "v_mfma_f32_16x16x32_bf16 v[96:99], a[104:107], a[72:75], v[96:99]\n"
    "s_add_u32 m0, s32, 15392\n"
    "v_accvgpr_read_b32 v240, a251\n"
    "v_mfma_f32_16x16x32_bf16 v[92:95], a[100:103], a[72:75], v[92:95]\n"
    "buffer_load_dwordx4 v228, s[24:27], s34 offen lds\n"
    "v_lshlrev_b32 v234, 16, v240\n"
    "v_mfma_f32_16x16x32_bf16 v[92:95], a[40:43], a[12:15], v[92:95]\n"
    "s_cmp_eq_u32 s33, s53\n"
    "s_cselect_b32 s59, 655360, s56\n"
    "v_mfma_f32_16x16x32_bf16 v[88:91], a[36:39], a[12:15], v[88:91]\n"
    "s_cselect_b32 s62, 1146880, s57\n"
    "s_add_u32 s33, s33, s59\n"
    "v_mfma_f32_16x16x32_bf16 v[88:91], a[96:99], a[72:75], v[88:91]\n"
    "s_add_u32 s34, s34, s62\n"
    "v_mfma_f32_16x16x32_bf16 v[84:87], a[92:95], a[72:75], v[84:87]\n"
    "v_and_b32 v235, 0xffff0000, v240\n"
    "v_mfma_f32_16x16x32_bf16 v[84:87], a[32:35], a[12:15], v[84:87]\n"
    "s_waitcnt vmcnt(18) lgkmcnt(0)\n"
    "s_barrier\n"
    "v_mfma_f32_16x16x32_bf16 v[112:115], a[32:35], a[16:19], v[112:115]\n"
    "ds_read_b64 a[180:181], v229 offset:49152\n"
    "v_add_f32 v238, v238, v234\n"
    "v_mfma_f32_16x16x32_bf16 v[112:115], a[92:95], a[76:79], v[112:115]\n"
    "s_add_u32 m0, s28, 32768\n"
    "v_fmac_f32 v248, v234, v234\n"
    "v_mfma_f32_16x16x32_bf16 v[116:119], a[96:99], a[76:79], v[116:119]\n"
    "buffer_load_dwordx4 v224, s[8:11], s33 offen offset:0 sc1 lds\n"
    "v_mfma_f32_16x16x32_bf16 v[116:119], a[36:39], a[16:19], v[116:119]\n"
    "ds_read_b64 a[182:183], v229 offset:49664\n"
    "v_add_f32 v239, v239, v235\n"
    "v_mfma_f32_16x16x32_bf16 v[120:123], a[40:43], a[16:19], v[120:123]\n"
    "ds_read_b64_tr_b16 a[212:213], v230 offset:46176\n"
    "v_mfma_f32_16x16x32_bf16 v[120:123], a[100:103], a[76:79], v[120:123]\n"
    "ds_read_b64_tr_b16 a[214:215], v230 offset:49760\n"
    "v_fmac_f32 v249, v235, v235\n"
    "v_mfma_f32_16x16x32_bf16 v[124:127], a[104:107], a[76:79], v[124:127]\n"
    "s_add_u32 m0, s29, 30784\n"
    "v_mfma_f32_16x16x32_bf16 v[124:127], a[44:47], a[16:19], v[124:127]\n"
    "buffer_load_dwordx4 v225, s[12:15], s34 offen lds\n"
    "v_accvgpr_read_b32 v240, a252\n"
    "v_mfma_f32_16x16x32_bf16 v[128:131], a[48:51], a[16:19], v[128:131]\n"
    "ds_read_b64_tr_b16 a[216:217], v230 offset:46208\n"
    "v_lshlrev_b32 v234, 16, v240\n"
    "v_mfma_f32_16x16x32_bf16 v[128:131], a[108:111], a[76:79], v[128:131]\n"
    "ds_read_b64_tr_b16 a[218:219], v230 offset:49792\n"
    "v_mfma_f32_16x16x32_bf16 v[132:135], a[112:115], a[76:79], v[132:135]\n"
    "ds_read_b64_tr_b16 a[220:221], v230 offset:46240\n"
    "v_and_b32 v235, 0xffff0000, v240\n"
    "v_mfma_f32_16x16x32_bf16 v[132:135], a[52:55], a[16:19], v[132:135]\n"
    "s_add_u32 m0, s28, 33664\n"
    "v_mfma_f32_16x16x32_bf16 v[136:139], a[56:59], a[16:19], v[136:139]\n"
    "buffer_load_dwordx4 v224, s[8:11], s33 offen offset:128 sc1 lds\n"
    "v_add_f32 v236, v236, v234\n"
    "v_mfma_f32_16x16x32_bf16 v[136:139], a[116:119], a[76:79], v[136:139]\n"
    "ds_read_b64_tr_b16 a[222:223], v230 offset:49824\n"
    "v_mfma_f32_16x16x32_bf16 v[164:167], a[116:119], a[80:83], v[164:167]\n"
    "ds_read_b64_tr_b16 a[224:225], v230 offset:53360\n"
    "v_fmac_f32 v246, v234, v234\n"
    "v_mfma_f32_16x16x32_bf16 v[164:167], a[56:59], a[20:23], v[164:167]\n"
    "ds_read_b64_tr_b16 a[226:227], v230 offset:56944\n"
    "v_mfma_f32_16x16x32_bf16 v[160:163], a[52:55], a[20:23], v[160:163]\n"
    "ds_read_b64_tr_b16 a[228:229], v230 offset:53392\n"
    "v_add_f32 v237, v237, v235\n"
    "v_mfma_f32_16x16x32_bf16 v[160:163], a[112:115], a[80:83], v[160:163]\n"
    "ds_read_b64_tr_b16 a[230:231], v230 offset:56976\n"
    "v_fmac_f32 v247, v235, v235\n"
    "v_mfma_f32_16x16x32_bf16 v[156:159], a[108:111], a[80:83], v[156:159]\n"
    "ds_read_b64_tr_b16 a[232:233], v230 offset:53424\n"
    "v_mfma_f32_16x16x32_bf16 v[156:159], a[48:51], a[20:23], v[156:159]\n"
    "ds_read_b64_tr_b16 a[234:235], v230 offset:57008\n"
    "v_accvgpr_read_b32 v240, a253\n"
    "v_mfma_f32_16x16x32_bf16 v[152:155], a[44:47], a[20:23], v[152:155]\n"
    "ds_read_b64_tr_b16 a[236:237], v231 offset:46176\n"
    "v_mfma_f32_16x16x32_bf16 v[152:155], a[104:107], a[80:83], v[152:155]\n"
    "ds_read_b64_tr_b16 a[238:239], v231 offset:49760\n"
    "v_lshlrev_b32 v234, 16, v240\n"
    "v_mfma_f32_16x16x32_bf16 v[148:151], a[100:103], a[80:83], v[148:151]\n"
    "ds_read_b64 a[184:185], v229 offset:50176\n"
    "v_mfma_f32_16x16x32_bf16 v[148:151], a[40:43], a[20:23], v[148:151]\n"
    "ds_read_b64 a[186:187], v229 offset:50688\n"
    "v_and_b32 v235, 0xffff0000, v240\n"
    "v_mfma_f32_16x16x32_bf16 v[144:147], a[36:39], a[20:23], v[144:147]\n"
    "ds_read_b64 a[188:189], v229 offset:51200\n"
    "v_add_f32 v236, v236, v234\n"
    "v_mfma_f32_16x16x32_bf16 v[144:147], a[96:99], a[80:83], v[144:147]\n"
    "ds_read_b64 a[190:191], v229 offset:51712\n"
    "v_mfma_f32_16x16x32_bf16 v[140:143], a[92:95], a[80:83], v[140:143]\n"
    "s_add_u32 m0, s30, 30784\n"
    "v_fmac_f32 v246, v234, v234\n"
    "v_mfma_f32_16x16x32_bf16 v[140:143], a[32:35], a[20:23], v[140:143]\n"
    "buffer_load_dwordx4 v226, s[16:19], s34 offen lds\n"
    "v_mfma_f32_16x16x32_bf16 v[168:171], a[32:35], a[24:27], v[168:171]\n"
    "ds_read_b64 a[192:193], v229 offset:52224\n"
    "v_add_f32 v237, v237, v235\n"
    "v_mfma_f32_16x16x32_bf16 v[168:171], a[92:95], a[84:87], v[168:171]\n"
    "ds_read_b64 a[194:195], v229 offset:52736\n"
    "v_mfma_f32_16x16x32_bf16 v[172:175], a[96:99], a[84:87], v[172:175]\n"
    "ds_read_b64 a[196:197], v229 offset:53248\n"
    "v_fmac_f32 v247, v235, v235\n"
    "v_mfma_f32_16x16x32_bf16 v[172:175], a[36:39], a[24:27], v[172:175]\n"
    "s_add_u32 m0, s28, 34560\n"
    "v_mfma_f32_16x16x32_bf16 v[176:179], a[40:43], a[24:27], v[176:179]\n"
    "buffer_load_dwordx4 v224, s[8:11], s33 offen offset:256 sc1 lds\n"
    "v_accvgpr_read_b32 v240, a254\n"
    "v_mfma_f32_16x16x32_bf16 v[176:179], a[100:103], a[84:87], v[176:179]\n"
    "ds_read_b64 a[198:199], v229 offset:53760\n"
    "v_lshlrev_b32 v234, 16, v240\n"
    "v_mfma_f32_16x16x32_bf16 v[180:183], a[104:107], a[84:87], v[180:183]\n"
    "ds_read_b64 a[200:201], v229 offset:54272\n"
    "v_mfma_f32_16x16x32_bf16 v[180:183], a[44:47], a[24:27], v[180:183]\n"
    "ds_read_b64 a[202:203], v229 offset:54784\n"
    "v_and_b32 v235, 0xffff0000, v240\n"
    "v_mfma_f32_16x16x32_bf16 v[184:187], a[48:51], a[24:27], v[184:187]\n"
    "s_add_u32 m0, s31, 30784\n"
    "v_mfma_f32_16x16x32_bf16 v[184:187], a[108:111], a[84:87], v[184:187]\n"
    "buffer_load_dwordx4 v227, s[20:23], s34 offen lds\n"
    "v_add_f32 v238, v238, v234\n"
    "v_mfma_f32_16x16x32_bf16 v[188:191], a[112:115], a[84:87], v[188:191]\n"
    "ds_read_b64 a[204:205], v229 offset:55296\n"
    "v_mfma_f32_16x16x32_bf16 v[188:191], a[52:55], a[24:27], v[188:191]\n"
    "buffer_load_dword v233, v232, s[48:51], 0 offen\n"
    "v_fmac_f32 v248, v234, v234\n"
    "v_mfma_f32_16x16x32_bf16 v[192:195], a[56:59], a[24:27], v[192:195]\n"
    "v_add_u32 v232, s63, v232\n"
    "v_add_f32 v239, v239, v235\n"
    "v_mfma_f32_16x16x32_bf16 v[192:195], a[116:119], a[84:87], v[192:195]\n"
    "ds_read_b64 a[206:207], v229 offset:55808\n"
    "v_mfma_f32_16x16x32_bf16 v[220:223], a[116:119], a[88:91], v[220:223]\n"
    "ds_read_b64 a[208:209], v229 offset:56320\n"
    "v_fmac_f32 v249, v235, v235\n"
    "v_mfma_f32_16x16x32_bf16 v[220:223], a[56:59], a[28:31], v[220:223]\n"
    "s_add_u32 m0, s28, 35456\n"
    "v_mfma_f32_16x16x32_bf16 v[216:219], a[52:55], a[28:31], v[216:219]\n"
    "buffer_load_dwordx4 v224, s[8:11], s33 offen offset:384 sc1 lds\n"
    "v_accvgpr_read_b32 v240, a255\n"
    "v_mfma_f32_16x16x32_bf16 v[216:219], a[112:115], a[88:91], v[216:219]\n"
    "ds_read_b64 a[210:211], v229 offset:56832\n"
    "v_mfma_f32_16x16x32_bf16 v[212:215], a[108:111], a[88:91], v[212:215]\n"
    "v_lshlrev_b32 v234, 16, v240\n"
    "v_mfma_f32_16x16x32_bf16 v[212:215], a[48:51], a[28:31], v[212:215]\n"
    "v_and_b32 v235, 0xffff0000, v240\n"
    "v_add_f32 v238, v238, v234\n"
    "v_mfma_f32_16x16x32_bf16 v[208:211], a[44:47], a[28:31], v[208:211]\n"
    "v_fmac_f32 v248, v234, v234\n"
    "S2_H1%=:\n"
    "s_cbranch_execz S2_H4%=\n"
    "v_mfma_f32_16x16x32_bf16 v[208:211], a[104:107], a[88:91], v[208:211]\n"
    "s_add_u32 m0, s32, 30784\n"
    "v_mfma_f32_16x16x32_bf16 v[204:207], a[100:103], a[88:91], v[204:207]\n"
    "buffer_load_dwordx4 v228, s[24:27], s34 offen lds\n"
    "v_add_f32 v239, v239, v235\n"
    "v_mfma_f32_16x16x32_bf16 v[204:207], a[40:43], a[28:31], v[204:207]\n"
    "s_cmp_eq_u32 s33, s53\n"
    "s_cselect_b32 s59, 655360, s56\n"
    "v_mfma_f32_16x16x32_bf16 v[200:203], a[36:39], a[28:31], v[200:203]\n"
    "s_cselect_b32 s62, 1146880, s57\n"
    "s_add_u32 s33, s33, s59\n"
    "v_mfma_f32_16x16x32_bf16 v[200:203], a[96:99], a[88:91], v[200:203]\n"
    "s_add_u32 s34, s34, s62\n"
    "v_mfma_f32_16x16x32_bf16 v[196:199], a[92:95], a[88:91], v[196:199]\n"
    "v_fmac_f32 v249, v235, v235\n"
    "v_mfma_f32_16x16x32_bf16 v[196:199], a[32:35], a[28:31], v[196:199]\n"
    "s_waitcnt vmcnt(18) lgkmcnt(0)\n"
    "s_barrier\n"
    "v_mfma_f32_16x16x32_bf16 v[0:3], a[152:155], a[120:123], v[0:3]\n"
    "ds_read_b64 a[0:1], v229 offset:0\n"
    "ds_read_b64 a[2:3], v229 offset:512\n"
    "v_mfma_f32_16x16x32_bf16 v[0:3], a[212:215], a[180:183], v[0:3]\n"
    "s_add_u32 m0, s28, 49152\n"
    "ds_read_b64_tr_b16 a[32:33], v230 offset:0\n"
    "v_mfma_f32_16x16x32_bf16 v[4:7], a[216:219], a[180:183], v[4:7]\n"
    "buffer_load_dwordx4 v224, s[8:11], s33 offen offset:0 sc1 lds\n"
    "v_add_f32 v236, v236, v237\n"
    "v_mfma_f32_16x16x32_bf16 v[4:7], a[156:159], a[120:123], v[4:7]\n"
    "ds_read_b64_tr_b16 a[34:35], v230 offset:3584\n"
    "ds_read_b64_tr_b16 a[36:37], v230 offset:32\n"
    "v_mfma_f32_16x16x32_bf16 v[8:11], a[160:163], a[120:123], v[8:11]\n"
    "ds_read_b64_tr_b16 a[38:39], v230 offset:3616\n"
    "ds_read_b64_tr_b16 a[40:41], v230 offset:64\n"
    "v_mfma_f32_16x16x32_bf16 v[8:11], a[220:223], a[180:183], v[8:11]\n"
    "v_add_f32 v238, v238, v239\n"
    "ds_read_b64_tr_b16 a[42:43], v230 offset:3648\n"
    "v_mfma_f32_16x16x32_bf16 v[12:15], a[224:227], a[180:183], v[12:15]\n"
    "s_add_u32 m0, s29, 46176\n"
    "ds_read_b64_tr_b16 a[44:45], v230 offset:7184\n"
    "v_mfma_f32_16x16x32_bf16 v[12:15], a[164:167], a[120:123], v[12:15]\n"
    "buffer_load_dwordx4 v225, s[12:15], s34 offen lds\n"
    "ds_read_b64_tr_b16 a[46:47], v230 offset:10768\n"
    "v_mfma_f32_16x16x32_bf16 v[16:19], a[168:171], a[120:123], v[16:19]\n"
    "v_add_f32 v246, v246, v247\n"
    "ds_read_b64_tr_b16 a[48:49], v230 offset:7216\n"
    "v_mfma_f32_16x16x32_bf16 v[16:19], a[228:231], a[180:183], v[16:19]\n"
    "ds_read_b64_tr_b16 a[50:51], v230 offset:10800\n"
    "ds_read_b64_tr_b16 a[52:53], v230 offset:7248\n"
    "v_mfma_f32_16x16x32_bf16 v[20:23], a[232:235], a[180:183], v[20:23]\n"
    "ds_read_b64_tr_b16 a[54:55], v230 offset:10832\n"
    "ds_read_b64_tr_b16 a[56:57], v231 offset:0\n"
    "v_mfma_f32_16x16x32_bf16 v[20:23], a[172:175], a[120:123], v[20:23]\n"
    "s_add_u32 m0, s28, 50048\n"
    "v_add_f32 v248, v248, v249\n"
    "v_mfma_f32_16x16x32_bf16 v[24:27], a[176:179], a[120:123], v[24:27]\n"
    "buffer_load_dwordx4 v224, s[8:11], s33 offen offset:128 sc1 lds\n"
    "ds_read_b64_tr_b16 a[58:59], v231 offset:3584\n"
    "v_mfma_f32_16x16x32_bf16 v[24:27], a[236:239], a[180:183], v[24:27]\n"
    "ds_read_b64 a[4:5], v229 offset:1024\n"
    "ds_read_b64 a[6:7], v229 offset:1536\n"
    "v_mfma_f32_16x16x32_bf16 v[52:55], a[236:239], a[184:187], v[52:55]\n"
    "v_permlane32_swap_b32 v236, v246\n"
    "ds_read_b64 a[8:9], v229 offset:2048\n"
    "v_mfma_f32_16x16x32_bf16 v[52:55], a[176:179], a[124:127], v[52:55]\n"
    "ds_read_b64 a[10:11], v229 offset:2560\n"
    "ds_read_b64 a[12:13], v229 offset:3072\n"
    "v_mfma_f32_16x16x32_bf16 v[48:51], a[172:175], a[124:127], v[48:51]\n"
    "ds_read_b64 a[14:15], v229 offset:3584\n"
    "ds_read_b64 a[16:17], v229 offset:4096\n"
    "v_mfma_f32_16x16x32_bf16 v[48:51], a[232:235], a[184:187], v[48:51]\n"
    "v_permlane32_swap_b32 v238, v248\n"
    "ds_read_b64 a[18:19], v229 offset:4608\n"
    "v_mfma_f32_16x16x32_bf16 v[44:47], a[228:231], a[184:187], v[44:47]\n"
    "ds_read_b64 a[20:21], v229 offset:5120\n"
    "ds_read_b64 a[22:23], v229 offset:5632\n"
    "v_mfma_f32_16x16x32_bf16 v[44:47], a[168:171], a[124:127], v[44:47]\n"
    "ds_read_b64 a[24:25], v229 offset:6144\n"
    "ds_read_b64 a[26:27], v229 offset:6656\n"
    "v_mfma_f32_16x16x32_bf16 v[40:43], a[164:167], a[124:127], v[40:43]\n"
    "v_add_f32 v236, v236, v246\n"
    "ds_read_b64 a[28:29], v229 offset:7168\n"
    "v_mfma_f32_16x16x32_bf16 v[40:43], a[224:227], a[184:187], v[40:43]\n"
    "ds_read_b64 a[30:31], v229 offset:7680\n"
    "v_mfma_f32_16x16x32_bf16 v[36:39], a[220:223], a[184:187], v[36:39]\n"
    "v_mfma_f32_16x16x32_bf16 v[36:39], a[160:163], a[124:127], v[36:39]\n"
    "v_add_f32 v238, v238, v248\n"
    "v_mfma_f32_16x16x32_bf16 v[32:35], a[156:159], a[124:127], v[32:35]\n"
    "v_mfma_f32_16x16x32_bf16 v[32:35], a[216:219], a[184:187], v[32:35]\n"
    "v_mfma_f32_16x16x32_bf16 v[28:31], a[212:215], a[184:187], v[28:31]\n"
    "s_add_u32 m0, s30, 46176\n"
    "v_permlane16_swap_b32 v236, v238\n"
    "v_mfma_f32_16x16x32_bf16 v[28:31], a[152:155], a[124:127], v[28:31]\n"
    "buffer_load_dwordx4 v226, s[16:19], s34 offen lds\n"
    "v_mfma_f32_16x16x32_bf16 v[56:59], a[152:155], a[128:131], v[56:59]\n"
    "v_mfma_f32_16x16x32_bf16 v[56:59], a[212:215], a[188:191], v[56:59]\n"
    "v_add_f32 v236, v236, v238\n"
    "v_mfma_f32_16x16x32_bf16 v[60:63], a[216:219], a[188:191], v[60:63]\n"
    "v_mfma_f32_16x16x32_bf16 v[60:63], a[156:159], a[128:131], v[60:63]\n"
    "s_add_u32 m0, s28, 50944\n"
    "v_mfma_f32_16x16x32_bf16 v[64:67], a[160:163], a[128:131], v[64:67]\n"
    "buffer_load_dwordx4 v224, s[8:11], s33 offen offset:256 sc1 lds\n"
    "v_add_f32_dpp v236, v236, v236 row_ror:8 row_mask:0xf bank_mask:0xf\n"
    "v_mfma_f32_16x16x32_bf16 v[64:67], a[220:223], a[188:191], v[64:67]\n"
    "v_mfma_f32_16x16x32_bf16 v[68:71], a[224:227], a[188:191], v[68:71]\n"
    "v_mfma_f32_16x16x32_bf16 v[68:71], a[164:167], a[128:131], v[68:71]\n"
    "v_add_f32_dpp v236, v236, v236 row_ror:4 row_mask:0xf bank_mask:0xf\n"
    "v_mfma_f32_16x16x32_bf16 v[72:75], a[168:171], a[128:131], v[72:75]\n"
    "s_add_u32 m0, s31, 46176\n"
    "v_mfma_f32_16x16x32_bf16 v[72:75], a[228:231], a[188:191], v[72:75]\n"
    "buffer_load_dwordx4 v227, s[20:23], s34 offen lds\n"
    "v_mfma_f32_16x16x32_bf16 v[76:79], a[232:235], a[188:191], v[76:79]\n"
    "ds_write_b32 v245, v236\n"
    "v_mfma_f32_16x16x32_bf16 v[76:79], a[172:175], a[128:131], v[76:79]\n"
    "buffer_load_dword v233, v232, s[48:51], 0 offen\n"
    "v_mfma_f32_16x16x32_bf16 v[80:83], a[176:179], a[128:131], v[80:83]\n"
    "v_add_u32 v232, s63, v232\n"
    "v_mfma_f32_16x16x32_bf16 v[80:83], a[236:239], a[188:191], v[80:83]\n"
    "v_mfma_f32_16x16x32_bf16 v[108:111], a[236:239], a[192:195], v[108:111]\n"
    "v_mfma_f32_16x16x32_bf16 v[108:111], a[176:179], a[132:135], v[108:111]\n"
    "s_add_u32 m0, s28, 51840\n"
    "v_mfma_f32_16x16x32_bf16 v[104:107], a[172:175], a[132:135], v[104:107]\n"
    "buffer_load_dwordx4 v224, s[8:11], s33 offen offset:384 sc1 lds\n"
    "v_mfma_f32_16x16x32_bf16 v[104:107], a[232:235], a[192:195], v[104:107]\n"
    "v_mfma_f32_16x16x32_bf16 v[100:103], a[228:231], a[192:195], v[100:103]\n"
    "v_mfma_f32_16x16x32_bf16 v[100:103], a[168:171], a[132:135], v[100:103]\n"
    "v_mfma_f32_16x16x32_bf16 v[96:99], a[164:167], a[132:135], v[96:99]\n"
    "v_mfma_f32_16x16x32_bf16 v[96:99], a[224:227], a[192:195], v[96:99]\n"
    "s_add_u32 m0, s32, 46176\n"
    "v_mfma_f32_16x16x32_bf16 v[92:95], a[220:223], a[192:195], v[92:95]\n"
    "buffer_load_dwordx4 v228, s[24:27], s34 offen lds\n"
    "v_mfma_f32_16x16x32_bf16 v[92:95], a[160:163], a[132:135], v[92:95]\n"
    "s_cmp_eq_u32 s33, s53\n"
    "s_cselect_b32 s59, 655360, s56\n"
    "v_mfma_f32_16x16x32_bf16 v[88:91], a[156:159], a[132:135], v[88:91]\n"
    "s_cselect_b32 s62, 1146880, s57\n"
    "s_add_u32 s33, s33, s59\n"
    "v_mfma_f32_16x16x32_bf16 v[88:91], a[216:219], a[192:195], v[88:91]\n"
    "s_add_u32 s34, s34, s62\n"
    "v_mfma_f32_16x16x32_bf16 v[84:87], a[212:215], a[192:195], v[84:87]\n"
    "v_mfma_f32_16x16x32_bf16 v[84:87], a[152:155], a[132:135], v[84:87]\n"
    "s_waitcnt vmcnt(18) lgkmcnt(0)\n"
    "s_barrier\n"
    "v_mfma_f32_16x16x32_bf16 v[112:115], a[152:155], a[136:139], v[112:115]\n"
    "ds_read_b64 a[60:61], v229 offset:16384\n"
    "v_mfma_f32_16x16x32_bf16 v[112:115], a[212:215], a[196:199], v[112:115]\n"
    "s_add_u32 m0, s28, 0\n"
    "ds_read_b128 v[252:255], v250\n"
    "v_mfma_f32_16x16x32_bf16 v[116:119], a[216:219], a[196:199], v[116:119]\n"
    "buffer_load_dwordx4 v224, s[8:11], s33 offen offset:0 sc1 lds\n"
    "v_mfma_f32_16x16x32_bf16 v[116:119], a[156:159], a[136:139], v[116:119]\n"
    "ds_read_b64 a[62:63], v229 offset:16896\n"
    "v_mfma_f32_16x16x32_bf16 v[120:123], a[160:163], a[136:139], v[120:123]\n"
    "ds_read_b128 v[236:239], v250 offset:128\n"
    "v_mfma_f32_16x16x32_bf16 v[120:123], a[220:223], a[196:199], v[120:123]\n"
    "ds_read_b64_tr_b16 a[92:93], v230 offset:15392\n"
    "v_mfma_f32_16x16x32_bf16 v[124:127], a[224:227], a[196:199], v[124:127]\n"
    "s_add_u32 m0, s29, 0\n"
    "v_mfma_f32_16x16x32_bf16 v[124:127], a[164:167], a[136:139], v[124:127]\n"
    "buffer_load_dwordx4 v225, s[12:15], s34 offen lds\n"
    "v_mfma_f32_16x16x32_bf16 v[128:131], a[168:171], a[136:139], v[128:131]\n"
    "ds_read_b64_tr_b16 a[94:95], v230 offset:18976\n"
    "v_mfma_f32_16x16x32_bf16 v[128:131], a[228:231], a[196:199], v[128:131]\n"
    "ds_read_b64_tr_b16 a[96:97], v230 offset:15424\n"
    "v_mfma_f32_16x16x32_bf16 v[132:135], a[232:235], a[196:199], v[132:135]\n"
    "ds_read_b64_tr_b16 a[98:99], v230 offset:19008\n"
    "v_mfma_f32_16x16x32_bf16 v[132:135], a[172:175], a[136:139], v[132:135]\n"
    "s_add_u32 m0, s28, 896\n"
    "v_mfma_f32_16x16x32_bf16 v[136:139], a[176:179], a[136:139], v[136:139]\n"
    "buffer_load_dwordx4 v224, s[8:11], s33 offen offset:128 sc1 lds\n"
    "v_mfma_f32_16x16x32_bf16 v[136:139], a[236:239], a[196:199], v[136:139]\n"
    "ds_read_b64_tr_b16 a[100:101], v230 offset:15456\n"
    "v_mfma_f32_16x16x32_bf16 v[164:167], a[236:239], a[200:203], v[164:167]\n"
    "ds_read_b64_tr_b16 a[102:103], v230 offset:19040\n"
    "v_mfma_f32_16x16x32_bf16 v[164:167], a[176:179], a[140:143], v[164:167]\n"
    "ds_read_b64_tr_b16 a[104:105], v230 offset:22576\n"
    "v_mfma_f32_16x16x32_bf16 v[160:163], a[172:175], a[140:143], v[160:163]\n"
    "ds_read_b64_tr_b16 a[106:107], v230 offset:26160\n"
    "v_mfma_f32_16x16x32_bf16 v[160:163], a[232:235], a[200:203], v[160:163]\n"
    "ds_read_b64_tr_b16 a[108:109], v230 offset:22608\n"
    "v_mfma_f32_16x16x32_bf16 v[156:159], a[228:231], a[200:203], v[156:159]\n"
    "ds_read_b64_tr_b16 a[110:111], v230 offset:26192\n"
    "v_mfma_f32_16x16x32_bf16 v[156:159], a[168:171], a[140:143], v[156:159]\n"
    "ds_read_b64_tr_b16 a[112:113], v230 offset:22640\n"
    "v_mfma_f32_16x16x32_bf16 v[152:155], a[164:167], a[140:143], v[152:155]\n"
    "ds_read_b64_tr_b16 a[114:115], v230 offset:26224\n"
    "v_mfma_f32_16x16x32_bf16 v[152:155], a[224:227], a[200:203], v[152:155]\n"
    "ds_read_b64_tr_b16 a[116:117], v231 offset:15392\n"
    "v_mfma_f32_16x16x32_bf16 v[148:151], a[220:223], a[200:203], v[148:151]\n"
    "ds_read_b64_tr_b16 a[118:119], v231 offset:18976\n"
    "v_mfma_f32_16x16x32_bf16 v[148:151], a[160:163], a[140:143], v[148:151]\n"
    "ds_read_b64 a[64:65], v229 offset:17408\n"
    "v_mfma_f32_16x16x32_bf16 v[144:147], a[156:159], a[140:143], v[144:147]\n"
    "ds_read_b64 a[66:67], v229 offset:17920\n"
    "v_mfma_f32_16x16x32_bf16 v[144:147], a[216:219], a[200:203], v[144:147]\n"
    "ds_read_b64 a[68:69], v229 offset:18432\n"
    "v_mfma_f32_16x16x32_bf16 v[140:143], a[212:215], a[200:203], v[140:143]\n"
    "s_add_u32 m0, s30, 0\n"
    "v_mfma_f32_16x16x32_bf16 v[140:143], a[152:155], a[140:143], v[140:143]\n"
    "buffer_load_dwordx4 v226, s[16:19], s34 offen lds\n"
    "v_mfma_f32_16x16x32_bf16 v[168:171], a[152:155], a[144:147], v[168:171]\n"
    "ds_read_b64 a[70:71], v229 offset:18944\n"
    "v_mfma_f32_16x16x32_bf16 v[168:171], a[212:215], a[204:207], v[168:171]\n"
    "ds_read_b64 a[72:73], v229 offset:19456\n"
    "v_mfma_f32_16x16x32_bf16 v[172:175], a[216:219], a[204:207], v[172:175]\n"
    "ds_read_b64 a[74:75], v229 offset:19968\n"
    "v_mfma_f32_16x16x32_bf16 v[172:175], a[156:159], a[144:147], v[172:175]\n"
    "s_add_u32 m0, s28, 1792\n"
    "v_mfma_f32_16x16x32_bf16 v[176:179], a[160:163], a[144:147], v[176:179]\n"
    "buffer_load_dwordx4 v224, s[8:11], s33 offen offset:256 sc1 lds\n"
    "v_mfma_f32_16x16x32_bf16 v[176:179], a[220:223], a[204:207], v[176:179]\n"
    "ds_read_b64 a[76:77], v229 offset:20480\n"
    "v_mfma_f32_16x16x32_bf16 v[180:183], a[224:227], a[204:207], v[180:183]\n"
    "ds_read_b64 a[78:79], v229 offset:20992\n"
    "v_mfma_f32_16x16x32_bf16 v[180:183], a[164:167], a[144:147], v[180:183]\n"
    "ds_read_b64 a[80:81], v229 offset:21504\n"
    "v_mfma_f32_16x16x32_bf16 v[184:187], a[168:171], a[144:147], v[184:187]\n"
    "s_add_u32 m0, s31, 0\n"
    "v_mfma_f32_16x16x32_bf16 v[184:187], a[228:231], a[204:207], v[184:187]\n"
    "buffer_load_dwordx4 v227, s[20:23], s34 offen lds\n"
    "v_mfma_f32_16x16x32_bf16 v[188:191], a[232:235], a[204:207], v[188:191]\n"
    "ds_read_b64 a[82:83], v229 offset:22016\n"
    "v_mfma_f32_16x16x32_bf16 v[188:191], a[172:175], a[144:147], v[188:191]\n"
    "buffer_load_dword v233, v232, s[48:51], 0 offen\n"
    "v_mfma_f32_16x16x32_bf16 v[192:195], a[176:179], a[144:147], v[192:195]\n"
    "v_add_u32 v232, s63, v232\n"
    "v_mfma_f32_16x16x32_bf16 v[192:195], a[236:239], a[204:207], v[192:195]\n"
    "ds_read_b64 a[84:85], v229 offset:22528\n"
    "v_mfma_f32_16x16x32_bf16 v[220:223], a[236:239], a[208:211], v[220:223]\n"
    "ds_read_b64 a[86:87], v229 offset:23040\n"
    "v_mfma_f32_16x16x32_bf16 v[220:223], a[176:179], a[148:151], v[220:223]\n"
    "s_add_u32 m0, s28, 2688\n"
    "v_mfma_f32_16x16x32_bf16 v[216:219], a[172:175], a[148:151], v[216:219]\n"
    "buffer_load_dwordx4 v224, s[8:11], s33 offen offset:384 sc1 lds\n"
    "v_mfma_f32_16x16x32_bf16 v[216:219], a[232:235], a[208:211], v[216:219]\n"
    "ds_read_b64 a[88:89], v229 offset:23552\n"
    "v_mfma_f32_16x16x32_bf16 v[212:215], a[228:231], a[208:211], v[212:215]\n"
    "ds_read_b64 a[90:91], v229 offset:24064\n"
    "v_mfma_f32_16x16x32_bf16 v[212:215], a[168:171], a[148:151], v[212:215]\n"
    "v_mfma_f32_16x16x32_bf16 v[208:211], a[164:167], a[148:151], v[208:211]\n"
    "v_mfma_f32_16x16x32_bf16 v[208:211], a[224:227], a[208:211], v[208:211]\n"
    "s_add_u32 m0, s32, 0\n"
    "v_mfma_f32_16x16x32_bf16 v[204:207], a[220:223], a[208:211], v[204:207]\n"
    "buffer_load_dwordx4 v228, s[24:27], s34 offen lds\n"
    "v_mfma_f32_16x16x32_bf16 v[204:207], a[160:163], a[148:151], v[204:207]\n"
    "s_cmp_eq_u32 s33, s53\n"
    "s_cselect_b32 s59, 655360, s56\n"
    "v_mfma_f32_16x16x32_bf16 v[200:203], a[156:159], a[148:151], v[200:203]\n"
    "s_cselect_b32 s62, 1146880, s57\n"
    "s_add_u32 s33, s33, s59\n"
    "v_mfma_f32_16x16x32_bf16 v[200:203], a[216:219], a[208:211], v[200:203]\n"
    "s_add_u32 s34, s34, s62\n"
    "v_mfma_f32_16x16x32_bf16 v[196:199], a[212:215], a[208:211], v[196:199]\n"
    "v_mfma_f32_16x16x32_bf16 v[196:199], a[152:155], a[148:151], v[196:199]\n"
    "s_waitcnt vmcnt(18) lgkmcnt(0)\n"
    "s_barrier\n"
    "v_mfma_f32_16x16x32_bf16 v[0:3], a[32:35], a[0:3], v[0:3]\n"
    "ds_read_b64 a[120:121], v229 offset:32768\n"
    "ds_read_b64 a[122:123], v229 offset:33280\n"
    "v_mfma_f32_16x16x32_bf16 v[0:3], a[92:95], a[60:63], v[0:3]\n"
    "s_add_u32 m0, s28, 16384\n"
    "ds_read_b64_tr_b16 a[152:153], v230 offset:30784\n"
    "v_mfma_f32_16x16x32_bf16 v[4:7], a[96:99], a[60:63], v[4:7]\n"
    "buffer_load_dwordx4 v224, s[8:11], s33 offen offset:0 sc1 lds\n"
    "v_add_f32 v252, v252, v254\n"
    "v_mfma_f32_16x16x32_bf16 v[4:7], a[36:39], a[0:3], v[4:7]\n"
    "ds_read_b64_tr_b16 a[154:155], v230 offset:34368\n"
    "ds_read_b64_tr_b16 a[156:157], v230 offset:30816\n"
    "v_mfma_f32_16x16x32_bf16 v[8:11], a[40:43], a[0:3], v[8:11]\n"
    "v_add_f32 v253, v253, v255\n"
    "ds_read_b64_tr_b16 a[158:159], v230 offset:34400\n"
    "v_mfma_f32_16x16x32_bf16 v[8:11], a[100:103], a[60:63], v[8:11]\n"
    "ds_read_b64_tr_b16 a[160:161], v230 offset:30848\n"
    "ds_read_b64_tr_b16 a[162:163], v230 offset:34432\n"
    "v_mfma_f32_16x16x32_bf16 v[12:15], a[104:107], a[60:63], v[12:15]\n"
    "s_add_u32 m0, s29, 15392\n"
    "v_add_f32 v236, v236, v238\n"
    "v_mfma_f32_16x16x32_bf16 v[12:15], a[44:47], a[0:3], v[12:15]\n"
    "buffer_load_dwordx4 v225, s[12:15], s34 offen lds\n"
    "ds_read_b64_tr_b16 a[164:165], v230 offset:37968\n"
    "v_mfma_f32_16x16x32_bf16 v[16:19], a[48:51], a[0:3], v[16:19]\n"
    "ds_read_b64_tr_b16 a[166:167], v230 offset:41552\n"
    "ds_read_b64_tr_b16 a[168:169], v230 offset:38000\n"
    "v_mfma_f32_16x16x32_bf16 v[16:19], a[108:111], a[60:63], v[16:19]\n"
    "v_add_f32 v237, v237, v239\n"
    "ds_read_b64_tr_b16 a[170:171], v230 offset:41584\n"
    "v_mfma_f32_16x16x32_bf16 v[20:23], a[112:115], a[60:63], v[20:23]\n"
    "ds_read_b64_tr_b16 a[172:173], v230 offset:38032\n"
    "ds_read_b64_tr_b16 a[174:175], v230 offset:41616\n"
    "v_mfma_f32_16x16x32_bf16 v[20:23], a[52:55], a[0:3], v[20:23]\n"
    "s_add_u32 m0, s28, 17280\n"
    "v_add_f32 v252, v252, v253\n"
    "v_mfma_f32_16x16x32_bf16 v[24:27], a[56:59], a[0:3], v[24:27]\n"
    "buffer_load_dwordx4 v224, s[8:11], s33 offen offset:128 sc1 lds\n"
    "ds_read_b64_tr_b16 a[176:177], v231 offset:30784\n"
    "v_mfma_f32_16x16x32_bf16 v[24:27], a[116:119], a[60:63], v[24:27]\n"
    "ds_read_b64_tr_b16 a[178:179], v231 offset:34368\n"
    "ds_read_b64 a[124:125], v229 offset:33792\n"
    "v_mfma_f32_16x16x32_bf16 v[52:55], a[116:119], a[64:67], v[52:55]\n"
    "v_add_f32 v236, v236, v237\n"
    "ds_read_b64 a[126:127], v229 offset:34304\n"
    "v_mfma_f32_16x16x32_bf16 v[52:55], a[56:59], a[4:7], v[52:55]\n"
    "ds_read_b64 a[128:129], v229 offset:34816\n"
    "ds_read_b64 a[130:131], v229 offset:35328\n"
    "v_mfma_f32_16x16x32_bf16 v[48:51], a[52:55], a[4:7], v[48:51]\n"
    "v_mul_f32 v238, 0x3a800000, v252\n"
    "ds_read_b64 a[132:133], v229 offset:35840\n"
    "v_mfma_f32_16x16x32_bf16 v[48:51], a[112:115], a[64:67], v[48:51]\n"
    "ds_read_b64 a[134:135], v229 offset:36352\n"
    "ds_read_b64 a[136:137], v229 offset:36864\n"
    "v_mfma_f32_16x16x32_bf16 v[44:47], a[108:111], a[64:67], v[44:47]\n"
    "v_mul_f32 v236, 0x3a800000, v236\n"
    "ds_read_b64 a[138:139], v229 offset:37376\n"
    "v_mfma_f32_16x16x32_bf16 v[44:47], a[48:51], a[4:7], v[44:47]\n"
    "ds_read_b64 a[140:141], v229 offset:37888\n"
    "ds_read_b64 a[142:143], v229 offset:38400\n"
    "v_mfma_f32_16x16x32_bf16 v[40:43], a[44:47], a[4:7], v[40:43]\n"
    "ds_read_b64 a[144:145], v229 offset:38912\n"
    "ds_read_b64 a[146:147], v229 offset:39424\n"
    "v_mfma_f32_16x16x32_bf16 v[40:43], a[104:107], a[64:67], v[40:43]\n"
    "v_fma_f32 v236, -v238, v238, v236\n"
    "ds_read_b64 a[148:149], v229 offset:39936\n"
    "v_mfma_f32_16x16x32_bf16 v[36:39], a[100:103], a[64:67], v[36:39]\n"
    "ds_read_b64 a[150:151], v229 offset:40448\n"
    "v_mfma_f32_16x16x32_bf16 v[36:39], a[40:43], a[4:7], v[36:39]\n"
    "v_max_f32 v236, 0, v236\n"
    "v_mfma_f32_16x16x32_bf16 v[32:35], a[36:39], a[4:7], v[32:35]\n"
    "v_mfma_f32_16x16x32_bf16 v[32:35], a[96:99], a[64:67], v[32:35]\n"
    "v_mfma_f32_16x16x32_bf16 v[28:31], a[92:95], a[64:67], v[28:31]\n"
    "s_add_u32 m0, s30, 15392\n"
    "v_add_f32 v239, 0x3727c5ac, v236\n"
    "v_mfma_f32_16x16x32_bf16 v[28:31], a[32:35], a[4:7], v[28:31]\n"
    "buffer_load_dwordx4 v226, s[16:19], s34 offen lds\n"
    "v_mfma_f32_16x16x32_bf16 v[56:59], a[32:35], a[8:11], v[56:59]\n"
    "buffer_store_dwordx2 v[238:239], v243, s[8:11], 0 offen sc1\n"
    "v_mfma_f32_16x16x32_bf16 v[56:59], a[92:95], a[68:71], v[56:59]\n"
    "v_mfma_f32_16x16x32_bf16 v[60:63], a[96:99], a[68:71], v[60:63]\n"
    "v_mfma_f32_16x16x32_bf16 v[60:63], a[36:39], a[8:11], v[60:63]\n"
    "s_add_u32 m0, s28, 18176\n"
    "v_mfma_f32_16x16x32_bf16 v[64:67], a[40:43], a[8:11], v[64:67]\n"
    "buffer_load_dwordx4 v224, s[8:11], s33 offen offset:256 sc1 lds\n"
    "v_mfma_f32_16x16x32_bf16 v[64:67], a[100:103], a[68:71], v[64:67]\n"
    "v_mfma_f32_16x16x32_bf16 v[68:71], a[104:107], a[68:71], v[68:71]\n"
    "v_mfma_f32_16x16x32_bf16 v[68:71], a[44:47], a[8:11], v[68:71]\n"
    "v_mfma_f32_16x16x32_bf16 v[72:75], a[48:51], a[8:11], v[72:75]\n"
    "s_add_u32 m0, s31, 15392\n"
    "v_mfma_f32_16x16x32_bf16 v[72:75], a[108:111], a[68:71], v[72:75]\n"
    "buffer_load_dwordx4 v227, s[20:23], s34 offen lds\n"
    "v_mfma_f32_16x16x32_bf16 v[76:79], a[112:115], a[68:71], v[76:79]\n"
    "v_mfma_f32_16x16x32_bf16 v[76:79], a[52:55], a[8:11], v[76:79]\n"
    "buffer_load_dword v233, v232, s[48:51], 0 offen\n"
    "v_mfma_f32_16x16x32_bf16 v[80:83], a[56:59], a[8:11], v[80:83]\n"
    "v_add_u32 v232, s63, v232\n"
    "v_mfma_f32_16x16x32_bf16 v[80:83], a[116:119], a[68:71], v[80:83]\n"
    "v_mfma_f32_16x16x32_bf16 v[108:111], a[116:119], a[72:75], v[108:111]\n"
    "v_mfma_f32_16x16x32_bf16 v[108:111], a[56:59], a[12:15], v[108:111]\n"
    "s_add_u32 m0, s28, 19072\n"
    "v_mfma_f32_16x16x32_bf16 v[104:107], a[52:55], a[12:15], v[104:107]\n"
    "buffer_load_dwordx4 v224, s[8:11], s33 offen offset:384 sc1 lds\n"
    "v_mfma_f32_16x16x32_bf16 v[104:107], a[112:115], a[72:75], v[104:107]\n"
    "v_mfma_f32_16x16x32_bf16 v[100:103], a[108:111], a[72:75], v[100:103]\n"
    "v_mfma_f32_16x16x32_bf16 v[100:103], a[48:51], a[12:15], v[100:103]\n"
    "v_mfma_f32_16x16x32_bf16 v[96:99], a[44:47], a[12:15], v[96:99]\n"
    "v_mfma_f32_16x16x32_bf16 v[96:99], a[104:107], a[72:75], v[96:99]\n"
    "s_add_u32 m0, s32, 15392\n"
    "v_mfma_f32_16x16x32_bf16 v[92:95], a[100:103], a[72:75], v[92:95]\n"
    "buffer_load_dwordx4 v228, s[24:27], s34 offen lds\n"
    "v_mfma_f32_16x16x32_bf16 v[92:95], a[40:43], a[12:15], v[92:95]\n"
    "s_cmp_eq_u32 s33, s53\n"
    "s_cselect_b32 s59, 655360, s56\n"
    "v_mfma_f32_16x16x32_bf16 v[88:91], a[36:39], a[12:15], v[88:91]\n"
    "s_cselect_b32 s62, 1146880, s57\n"
    "s_add_u32 s33, s33, s59\n"
    "v_mfma_f32_16x16x32_bf16 v[88:91], a[96:99], a[72:75], v[88:91]\n"
    "s_add_u32 s34, s34, s62\n"
    "v_mfma_f32_16x16x32_bf16 v[84:87], a[92:95], a[72:75], v[84:87]\n"
    "v_mfma_f32_16x16x32_bf16 v[84:87], a[32:35], a[12:15], v[84:87]\n"
    "s_mov_b32 s35, 4\n"
    "G1_LOOP%=:\n"
    "s_waitcnt vmcnt(18) lgkmcnt(0)\n"
    "s_barrier\n"
    "v_mfma_f32_16x16x32_bf16 v[112:115], a[32:35], a[16:19], v[112:115]\n"
    "ds_read_b64 a[180:181], v229 offset:49152\n"
    "v_mfma_f32_16x16x32_bf16 v[112:115], a[92:95], a[76:79], v[112:115]\n"
    "s_add_u32 m0, s28, 32768\n"
    "v_mfma_f32_16x16x32_bf16 v[116:119], a[96:99], a[76:79], v[116:119]\n"
    "buffer_load_dwordx4 v224, s[8:11], s33 offen offset:0 sc1 lds\n"
    "v_mfma_f32_16x16x32_bf16 v[116:119], a[36:39], a[16:19], v[116:119]\n"
    "ds_read_b64 a[182:183], v229 offset:49664\n"
    "v_mfma_f32_16x16x32_bf16 v[120:123], a[40:43], a[16:19], v[120:123]\n"
    "ds_read_b64_tr_b16 a[212:213], v230 offset:46176\n"
    "v_mfma_f32_16x16x32_bf16 v[120:123], a[100:103], a[76:79], v[120:123]\n"
    "ds_read_b64_tr_b16 a[214:215], v230 offset:49760\n"
    "v_mfma_f32_16x16x32_bf16 v[124:127], a[104:107], a[76:79], v[124:127]\n"
    "s_add_u32 m0, s29, 30784\n"
    "v_mfma_f32_16x16x32_bf16 v[124:127], a[44:47], a[16:19], v[124:127]\n"
    "buffer_load_dwordx4 v225, s[12:15], s34 offen lds\n"
    "v_mfma_f32_16x16x32_bf16 v[128:131], a[48:51], a[16:19], v[128:131]\n"
    "ds_read_b64_tr_b16 a[216:217], v230 offset:46208\n"
    "v_mfma_f32_16x16x32_bf16 v[128:131], a[108:111], a[76:79], v[128:131]\n"
    "ds_read_b64_tr_b16 a[218:219], v230 offset:49792\n"
    "v_mfma_f32_16x16x32_bf16 v[132:135], a[112:115], a[76:79], v[132:135]\n"
    "ds_read_b64_tr_b16 a[220:221], v230 offset:46240\n"
    "v_mfma_f32_16x16x32_bf16 v[132:135], a[52:55], a[16:19], v[132:135]\n"
    "s_add_u32 m0, s28, 33664\n"
    "v_mfma_f32_16x16x32_bf16 v[136:139], a[56:59], a[16:19], v[136:139]\n"
    "buffer_load_dwordx4 v224, s[8:11], s33 offen offset:128 sc1 lds\n"
    "v_mfma_f32_16x16x32_bf16 v[136:139], a[116:119], a[76:79], v[136:139]\n"
    "ds_read_b64_tr_b16 a[222:223], v230 offset:49824\n"
    "v_mfma_f32_16x16x32_bf16 v[164:167], a[116:119], a[80:83], v[164:167]\n"
    "ds_read_b64_tr_b16 a[224:225], v230 offset:53360\n"
    "v_mfma_f32_16x16x32_bf16 v[164:167], a[56:59], a[20:23], v[164:167]\n"
    "ds_read_b64_tr_b16 a[226:227], v230 offset:56944\n"
    "v_mfma_f32_16x16x32_bf16 v[160:163], a[52:55], a[20:23], v[160:163]\n"
    "ds_read_b64_tr_b16 a[228:229], v230 offset:53392\n"
    "v_mfma_f32_16x16x32_bf16 v[160:163], a[112:115], a[80:83], v[160:163]\n"
    "ds_read_b64_tr_b16 a[230:231], v230 offset:56976\n"
    "v_mfma_f32_16x16x32_bf16 v[156:159], a[108:111], a[80:83], v[156:159]\n"
    "ds_read_b64_tr_b16 a[232:233], v230 offset:53424\n"
    "v_mfma_f32_16x16x32_bf16 v[156:159], a[48:51], a[20:23], v[156:159]\n"
    "ds_read_b64_tr_b16 a[234:235], v230 offset:57008\n"
    "v_mfma_f32_16x16x32_bf16 v[152:155], a[44:47], a[20:23], v[152:155]\n"
    "ds_read_b64_tr_b16 a[236:237], v231 offset:46176\n"
    "v_mfma_f32_16x16x32_bf16 v[152:155], a[104:107], a[80:83], v[152:155]\n"
    "ds_read_b64_tr_b16 a[238:239], v231 offset:49760\n"
    "v_mfma_f32_16x16x32_bf16 v[148:151], a[100:103], a[80:83], v[148:151]\n"
    "ds_read_b64 a[184:185], v229 offset:50176\n"
    "v_mfma_f32_16x16x32_bf16 v[148:151], a[40:43], a[20:23], v[148:151]\n"
    "ds_read_b64 a[186:187], v229 offset:50688\n"
    "v_mfma_f32_16x16x32_bf16 v[144:147], a[36:39], a[20:23], v[144:147]\n"
    "ds_read_b64 a[188:189], v229 offset:51200\n"
    "v_mfma_f32_16x16x32_bf16 v[144:147], a[96:99], a[80:83], v[144:147]\n"
    "ds_read_b64 a[190:191], v229 offset:51712\n"
    "v_mfma_f32_16x16x32_bf16 v[140:143], a[92:95], a[80:83], v[140:143]\n"
    "s_add_u32 m0, s30, 30784\n"
    "v_mfma_f32_16x16x32_bf16 v[140:143], a[32:35], a[20:23], v[140:143]\n"
    "buffer_load_dwordx4 v226, s[16:19], s34 offen lds\n"
    "v_mfma_f32_16x16x32_bf16 v[168:171], a[32:35], a[24:27], v[168:171]\n"
    "ds_read_b64 a[192:193], v229 offset:52224\n"
    "v_mfma_f32_16x16x32_bf16 v[168:171], a[92:95], a[84:87], v[168:171]\n"
    "ds_read_b64 a[194:195], v229 offset:52736\n"
    "v_mfma_f32_16x16x32_bf16 v[172:175], a[96:99], a[84:87], v[172:175]\n"
    "ds_read_b64 a[196:197], v229 offset:53248\n"
    "v_mfma_f32_16x16x32_bf16 v[172:175], a[36:39], a[24:27], v[172:175]\n"
    "s_add_u32 m0, s28, 34560\n"
    "v_mfma_f32_16x16x32_bf16 v[176:179], a[40:43], a[24:27], v[176:179]\n"
    "buffer_load_dwordx4 v224, s[8:11], s33 offen offset:256 sc1 lds\n"
    "v_mfma_f32_16x16x32_bf16 v[176:179], a[100:103], a[84:87], v[176:179]\n"
    "ds_read_b64 a[198:199], v229 offset:53760\n"
    "v_mfma_f32_16x16x32_bf16 v[180:183], a[104:107], a[84:87], v[180:183]\n"
    "ds_read_b64 a[200:201], v229 offset:54272\n"
    "v_mfma_f32_16x16x32_bf16 v[180:183], a[44:47], a[24:27], v[180:183]\n"
    "ds_read_b64 a[202:203], v229 offset:54784\n"
    "v_mfma_f32_16x16x32_bf16 v[184:187], a[48:51], a[24:27], v[184:187]\n"
    "s_add_u32 m0, s31, 30784\n"
    "v_mfma_f32_16x16x32_bf16 v[184:187], a[108:111], a[84:87], v[184:187]\n"
    "buffer_load_dwordx4 v227, s[20:23], s34 offen lds\n"
    "v_mfma_f32_16x16x32_bf16 v[188:191], a[112:115], a[84:87], v[188:191]\n"
    "ds_read_b64 a[204:205], v229 offset:55296\n"
    "v_mfma_f32_16x16x32_bf16 v[188:191], a[52:55], a[24:27], v[188:191]\n"
    "buffer_load_dword v233, v232, s[48:51], 0 offen\n"
    "v_mfma_f32_16x16x32_bf16 v[192:195], a[56:59], a[24:27], v[192:195]\n"
    "v_add_u32 v232, s63, v232\n"
    "v_mfma_f32_16x16x32_bf16 v[192:195], a[116:119], a[84:87], v[192:195]\n"
    "ds_read_b64 a[206:207], v229 offset:55808\n"
    "v_mfma_f32_16x16x32_bf16 v[220:223], a[116:119], a[88:91], v[220:223]\n"
    "ds_read_b64 a[208:209], v229 offset:56320\n"
    "v_mfma_f32_16x16x32_bf16 v[220:223], a[56:59], a[28:31], v[220:223]\n"
    "s_add_u32 m0, s28, 35456\n"
    "v_mfma_f32_16x16x32_bf16 v[216:219], a[52:55], a[28:31], v[216:219]\n"
    "buffer_load_dwordx4 v224, s[8:11], s33 offen offset:384 sc1 lds\n"
    "v_mfma_f32_16x16x32_bf16 v[216:219], a[112:115], a[88:91], v[216:219]\n"
    "ds_read_b64 a[210:211], v229 offset:56832\n"
    "v_mfma_f32_16x16x32_bf16 v[212:215], a[108:111], a[88:91], v[212:215]\n"
    "v_mfma_f32_16x16x32_bf16 v[212:215], a[48:51], a[28:31], v[212:215]\n"
    "v_mfma_f32_16x16x32_bf16 v[208:211], a[44:47], a[28:31], v[208:211]\n"
    "v_mfma_f32_16x16x32_bf16 v[208:211], a[104:107], a[88:91], v[208:211]\n"
    "s_add_u32 m0, s32, 30784\n"
    "v_mfma_f32_16x16x32_bf16 v[204:207], a[100:103], a[88:91], v[204:207]\n"
    "buffer_load_dwordx4 v228, s[24:27], s34 offen lds\n"
    "v_mfma_f32_16x16x32_bf16 v[204:207], a[40:43], a[28:31], v[204:207]\n"
    "s_cmp_eq_u32 s33, s53\n"
    "s_cselect_b32 s59, 655360, s56\n"
    "v_mfma_f32_16x16x32_bf16 v[200:203], a[36:39], a[28:31], v[200:203]\n"
    "s_cselect_b32 s62, 1146880, s57\n"
    "s_add_u32 s33, s33, s59\n"
    "v_mfma_f32_16x16x32_bf16 v[200:203], a[96:99], a[88:91], v[200:203]\n"
    "s_add_u32 s34, s34, s62\n"
    "v_mfma_f32_16x16x32_bf16 v[196:199], a[92:95], a[88:91], v[196:199]\n"
    "v_mfma_f32_16x16x32_bf16 v[196:199], a[32:35], a[28:31], v[196:199]\n"
    "s_waitcnt vmcnt(18) lgkmcnt(0)\n"
    "s_barrier\n"
    "v_mfma_f32_16x16x32_bf16 v[0:3], a[152:155], a[120:123], v[0:3]\n"
    "ds_read_b64 a[0:1], v229 offset:0\n"
    "v_mfma_f32_16x16x32_bf16 v[0:3], a[212:215], a[180:183], v[0:3]\n"
    "s_add_u32 m0, s28, 49152\n"
    "v_mfma_f32_16x16x32_bf16 v[4:7], a[216:219], a[180:183], v[4:7]\n"
    "buffer_load_dwordx4 v224, s[8:11], s33 offen offset:0 sc1 lds\n"
    "v_mfma_f32_16x16x32_bf16 v[4:7], a[156:159], a[120:123], v[4:7]\n"
    "ds_read_b64 a[2:3], v229 offset:512\n"
    "v_mfma_f32_16x16x32_bf16 v[8:11], a[160:163], a[120:123], v[8:11]\n"
    "ds_read_b64_tr_b16 a[32:33], v230 offset:0\n"
    "v_mfma_f32_16x16x32_bf16 v[8:11], a[220:223], a[180:183], v[8:11]\n"
    "ds_read_b64_tr_b16 a[34:35], v230 offset:3584\n"
    "v_mfma_f32_16x16x32_bf16 v[12:15], a[224:227], a[180:183], v[12:15]\n"
    "s_add_u32 m0, s29, 46176\n"
    "v_mfma_f32_16x16x32_bf16 v[12:15], a[164:167], a[120:123], v[12:15]\n"
    "buffer_load_dwordx4 v225, s[12:15], s34 offen lds\n"
    "v_mfma_f32_16x16x32_bf16 v[16:19], a[168:171], a[120:123], v[16:19]\n"
    "ds_read_b64_tr_b16 a[36:37], v230 offset:32\n"
    "v_mfma_f32_16x16x32_bf16 v[16:19], a[228:231], a[180:183], v[16:19]\n"
    "ds_read_b64_tr_b16 a[38:39], v230 offset:3616\n"
    "v_mfma_f32_16x16x32_bf16 v[20:23], a[232:235], a[180:183], v[20:23]\n"
    "ds_read_b64_tr_b16 a[40:41], v230 offset:64\n"
    "v_mfma_f32_16x16x32_bf16 v[20:23], a[172:175], a[120:123], v[20:23]\n"
    "s_add_u32 m0, s28, 50048\n"
    "v_mfma_f32_16x16x32_bf16 v[24:27], a[176:179], a[120:123], v[24:27]\n"
    "buffer_load_dwordx4 v224, s[8:11], s33 offen offset:128 sc1 lds\n"
    "v_mfma_f32_16x16x32_bf16 v[24:27], a[236:239], a[180:183], v[24:27]\n"
    "ds_read_b64_tr_b16 a[42:43], v230 offset:3648\n"
    "v_mfma_f32_16x16x32_bf16 v[52:55], a[236:239], a[184:187], v[52:55]\n"
    "ds_read_b64_tr_b16 a[44:45], v230 offset:7184\n"
    "v_mfma_f32_16x16x32_bf16 v[52:55], a[176:179], a[124:127], v[52:55]\n"
    "ds_read_b64_tr_b16 a[46:47], v230 offset:10768\n"
    "v_mfma_f32_16x16x32_bf16 v[48:51], a[172:175], a[124:127], v[48:51]\n"
    "ds_read_b64_tr_b16 a[48:49], v230 offset:7216\n"
    "v_mfma_f32_16x16x32_bf16 v[48:51], a[232:235], a[184:187], v[48:51]\n"
    "ds_read_b64_tr_b16 a[50:51], v230 offset:10800\n"
    "v_mfma_f32_16x16x32_bf16 v[44:47], a[228:231], a[184:187], v[44:47]\n"
    "ds_read_b64_tr_b16 a[52:53], v230 offset:7248\n"
    "v_mfma_f32_16x16x32_bf16 v[44:47], a[168:171], a[124:127], v[44:47]\n"
    "ds_read_b64_tr_b16 a[54:55], v230 offset:10832\n"
    "v_mfma_f32_16x16x32_bf16 v[40:43], a[164:167], a[124:127], v[40:43]\n"
    "ds_read_b64_tr_b16 a[56:57], v231 offset:0\n"
    "v_mfma_f32_16x16x32_bf16 v[40:43], a[224:227], a[184:187], v[40:43]\n"
    "ds_read_b64_tr_b16 a[58:59], v231 offset:3584\n"
    "v_mfma_f32_16x16x32_bf16 v[36:39], a[220:223], a[184:187], v[36:39]\n"
    "ds_read_b64 a[4:5], v229 offset:1024\n"
    "v_mfma_f32_16x16x32_bf16 v[36:39], a[160:163], a[124:127], v[36:39]\n"
    "ds_read_b64 a[6:7], v229 offset:1536\n"
    "v_mfma_f32_16x16x32_bf16 v[32:35], a[156:159], a[124:127], v[32:35]\n"
    "S2_H2%=:\n"
    "s_cbranch_execz S2_H5%=\n"
    "ds_read_b64 a[8:9], v229 offset:2048\n"
    "v_mfma_f32_16x16x32_bf16 v[32:35], a[216:219], a[184:187], v[32:35]\n"
    "ds_read_b64 a[10:11], v229 offset:2560\n"
    "v_mfma_f32_16x16x32_bf16 v[28:31], a[212:215], a[184:187], v[28:31]\n"
    "s_add_u32 m0, s30, 46176\n"
    "v_mfma_f32_16x16x32_bf16 v[28:31], a[152:155], a[124:127], v[28:31]\n"
    "buffer_load_dwordx4 v226, s[16:19], s34 offen lds\n"
    "v_mfma_f32_16x16x32_bf16 v[56:59], a[152:155], a[128:131], v[56:59]\n"
    "ds_read_b64 a[12:13], v229 offset:3072\n"
    "v_mfma_f32_16x16x32_bf16 v[56:59], a[212:215], a[188:191], v[56:59]\n"
    "ds_read_b64 a[14:15], v229 offset:3584\n"
    "v_mfma_f32_16x16x32_bf16 v[60:63], a[216:219], a[188:191], v[60:63]\n"
    "ds_read_b64 a[16:17], v229 offset:4096\n"
    "v_mfma_f32_16x16x32_bf16 v[60:63], a[156:159], a[128:131], v[60:63]\n"
    "s_add_u32 m0, s28, 50944\n"
    "v_mfma_f32_16x16x32_bf16 v[64:67], a[160:163], a[128:131], v[64:67]\n"
    "buffer_load_dwordx4 v224, s[8:11], s33 offen offset:256 sc1 lds\n"
    "v_mfma_f32_16x16x32_bf16 v[64:67], a[220:223], a[188:191], v[64:67]\n"
    "ds_read_b64 a[18:19], v229 offset:4608\n"
    "v_mfma_f32_16x16x32_bf16 v[68:71], a[224:227], a[188:191], v[68:71]\n"
    "ds_read_b64 a[20:21], v229 offset:5120\n"
    "v_mfma_f32_16x16x32_bf16 v[68:71], a[164:167], a[128:131], v[68:71]\n"
    "ds_read_b64 a[22:23], v229 offset:5632\n"
    "v_mfma_f32_16x16x32_bf16 v[72:75], a[168:171], a[128:131], v[72:75]\n"
    "s_add_u32 m0, s31, 46176\n"
    "v_mfma_f32_16x16x32_bf16 v[72:75], a[228:231], a[188:191], v[72:75]\n"
    "buffer_load_dwordx4 v227, s[20:23], s34 offen lds\n"
    "v_mfma_f32_16x16x32_bf16 v[76:79], a[232:235], a[188:191], v[76:79]\n"
    "ds_read_b64 a[24:25], v229 offset:6144\n"
    "v_mfma_f32_16x16x32_bf16 v[76:79], a[172:175], a[128:131], v[76:79]\n"
    "buffer_load_dword v233, v232, s[48:51], 0 offen\n"
    "v_mfma_f32_16x16x32_bf16 v[80:83], a[176:179], a[128:131], v[80:83]\n"
    "v_add_u32 v232, s63, v232\n"
    "v_mfma_f32_16x16x32_bf16 v[80:83], a[236:239], a[188:191], v[80:83]\n"
    "ds_read_b64 a[26:27], v229 offset:6656\n"
    "v_mfma_f32_16x16x32_bf16 v[108:111], a[236:239], a[192:195], v[108:111]\n"
    "ds_read_b64 a[28:29], v229 offset:7168\n"
    "v_mfma_f32_16x16x32_bf16 v[108:111], a[176:179], a[132:135], v[108:111]\n"
    "s_add_u32 m0, s28, 51840\n"
    "v_mfma_f32_16x16x32_bf16 v[104:107], a[172:175], a[132:135], v[104:107]\n"
    "buffer_load_dwordx4 v224, s[8:11], s33 offen offset:384 sc1 lds\n"
    "v_mfma_f32_16x16x32_bf16 v[104:107], a[232:235], a[192:195], v[104:107]\n"
    "ds_read_b64 a[30:31], v229 offset:7680\n"
    "v_mfma_f32_16x16x32_bf16 v[100:103], a[228:231], a[192:195], v[100:103]\n"
    "v_mfma_f32_16x16x32_bf16 v[100:103], a[168:171], a[132:135], v[100:103]\n"
    "v_mfma_f32_16x16x32_bf16 v[96:99], a[164:167], a[132:135], v[96:99]\n"
    "v_mfma_f32_16x16x32_bf16 v[96:99], a[224:227], a[192:195], v[96:99]\n"
    "s_add_u32 m0, s32, 46176\n"
    "v_mfma_f32_16x16x32_bf16 v[92:95], a[220:223], a[192:195], v[92:95]\n"
    "buffer_load_dwordx4 v228, s[24:27], s34 offen lds\n"
    "v_mfma_f32_16x16x32_bf16 v[92:95], a[160:163], a[132:135], v[92:95]\n"
    "s_cmp_eq_u32 s33, s53\n"
    "s_cselect_b32 s59, 655360, s56\n"
    "v_mfma_f32_16x16x32_bf16 v[88:91], a[156:159], a[132:135], v[88:91]\n"
    "s_cselect_b32 s62, 1146880, s57\n"
    "s_add_u32 s33, s33, s59\n"
    "v_mfma_f32_16x16x32_bf16 v[88:91], a[216:219], a[192:195], v[88:91]\n"
    "s_add_u32 s34, s34, s62\n"
    "v_mfma_f32_16x16x32_bf16 v[84:87], a[212:215], a[192:195], v[84:87]\n"
    "v_mfma_f32_16x16x32_bf16 v[84:87], a[152:155], a[132:135], v[84:87]\n"
    "s_waitcnt vmcnt(18) lgkmcnt(0)\n"
    "s_barrier\n"
    "v_mfma_f32_16x16x32_bf16 v[112:115], a[152:155], a[136:139], v[112:115]\n"
    "ds_read_b64 a[60:61], v229 offset:16384\n"
    "v_mfma_f32_16x16x32_bf16 v[112:115], a[212:215], a[196:199], v[112:115]\n"
    "s_add_u32 m0, s28, 0\n"
    "v_mfma_f32_16x16x32_bf16 v[116:119], a[216:219], a[196:199], v[116:119]\n"
    "buffer_load_dwordx4 v224, s[8:11], s33 offen offset:0 sc1 lds\n"
    "v_mfma_f32_16x16x32_bf16 v[116:119], a[156:159], a[136:139], v[116:119]\n"
    "ds_read_b64 a[62:63], v229 offset:16896\n"
    "v_mfma_f32_16x16x32_bf16 v[120:123], a[160:163], a[136:139], v[120:123]\n"
    "ds_read_b64_tr_b16 a[92:93], v230 offset:15392\n"
    "v_mfma_f32_16x16x32_bf16 v[120:123], a[220:223], a[196:199], v[120:123]\n"
    "ds_read_b64_tr_b16 a[94:95], v230 offset:18976\n"
    "v_mfma_f32_16x16x32_bf16 v[124:127], a[224:227], a[196:199], v[124:127]\n"
    "s_add_u32 m0, s29, 0\n"
    "v_mfma_f32_16x16x32_bf16 v[124:127], a[164:167], a[136:139], v[124:127]\n"
    "buffer_load_dwordx4 v225, s[12:15], s34 offen lds\n"
    "v_mfma_f32_16x16x32_bf16 v[128:131], a[168:171], a[136:139], v[128:131]\n"
    "ds_read_b64_tr_b16 a[96:97], v230 offset:15424\n"
    "v_mfma_f32_16x16x32_bf16 v[128:131], a[228:231], a[196:199], v[128:131]\n"
    "ds_read_b64_tr_b16 a[98:99], v230 offset:19008\n"
    "v_mfma_f32_16x16x32_bf16 v[132:135], a[232:235], a[196:199], v[132:135]\n"
    "ds_read_b64_tr_b16 a[100:101], v230 offset:15456\n"
    "v_mfma_f32_16x16x32_bf16 v[132:135], a[172:175], a[136:139], v[132:135]\n"
    "s_add_u32 m0, s28, 896\n"
    "v_mfma_f32_16x16x32_bf16 v[136:139], a[176:179], a[136:139], v[136:139]\n"
    "buffer_load_dwordx4 v224, s[8:11], s33 offen offset:128 sc1 lds\n"
    "v_mfma_f32_16x16x32_bf16 v[136:139], a[236:239], a[196:199], v[136:139]\n"
    "ds_read_b64_tr_b16 a[102:103], v230 offset:19040\n"
    "v_mfma_f32_16x16x32_bf16 v[164:167], a[236:239], a[200:203], v[164:167]\n"
    "ds_read_b64_tr_b16 a[104:105], v230 offset:22576\n"
    "v_mfma_f32_16x16x32_bf16 v[164:167], a[176:179], a[140:143], v[164:167]\n"
    "ds_read_b64_tr_b16 a[106:107], v230 offset:26160\n"
    "v_mfma_f32_16x16x32_bf16 v[160:163], a[172:175], a[140:143], v[160:163]\n"
    "ds_read_b64_tr_b16 a[108:109], v230 offset:22608\n"
    "v_mfma_f32_16x16x32_bf16 v[160:163], a[232:235], a[200:203], v[160:163]\n"
    "ds_read_b64_tr_b16 a[110:111], v230 offset:26192\n"
    "v_mfma_f32_16x16x32_bf16 v[156:159], a[228:231], a[200:203], v[156:159]\n"
    "ds_read_b64_tr_b16 a[112:113], v230 offset:22640\n"
    "v_mfma_f32_16x16x32_bf16 v[156:159], a[168:171], a[140:143], v[156:159]\n"
    "ds_read_b64_tr_b16 a[114:115], v230 offset:26224\n"
    "v_mfma_f32_16x16x32_bf16 v[152:155], a[164:167], a[140:143], v[152:155]\n"
    "ds_read_b64_tr_b16 a[116:117], v231 offset:15392\n"
    "v_mfma_f32_16x16x32_bf16 v[152:155], a[224:227], a[200:203], v[152:155]\n"
    "ds_read_b64_tr_b16 a[118:119], v231 offset:18976\n"
    "v_mfma_f32_16x16x32_bf16 v[148:151], a[220:223], a[200:203], v[148:151]\n"
    "ds_read_b64 a[64:65], v229 offset:17408\n"
    "v_mfma_f32_16x16x32_bf16 v[148:151], a[160:163], a[140:143], v[148:151]\n"
    "ds_read_b64 a[66:67], v229 offset:17920\n"
    "v_mfma_f32_16x16x32_bf16 v[144:147], a[156:159], a[140:143], v[144:147]\n"
    "ds_read_b64 a[68:69], v229 offset:18432\n"
    "v_mfma_f32_16x16x32_bf16 v[144:147], a[216:219], a[200:203], v[144:147]\n"
    "ds_read_b64 a[70:71], v229 offset:18944\n"
    "v_mfma_f32_16x16x32_bf16 v[140:143], a[212:215], a[200:203], v[140:143]\n"
    "s_add_u32 m0, s30, 0\n"
    "v_mfma_f32_16x16x32_bf16 v[140:143], a[152:155], a[140:143], v[140:143]\n"
    "buffer_load_dwordx4 v226, s[16:19], s34 offen lds\n"
    "v_mfma_f32_16x16x32_bf16 v[168:171], a[152:155], a[144:147], v[168:171]\n"
    "ds_read_b64 a[72:73], v229 offset:19456\n"
    "v_mfma_f32_16x16x32_bf16 v[168:171], a[212:215], a[204:207], v[168:171]\n"
    "ds_read_b64 a[74:75], v229 offset:19968\n"
    "v_mfma_f32_16x16x32_bf16 v[172:175], a[216:219], a[204:207], v[172:175]\n"
    "ds_read_b64 a[76:77], v229 offset:20480\n"
    "v_mfma_f32_16x16x32_bf16 v[172:175], a[156:159], a[144:147], v[172:175]\n"
    "s_add_u32 m0, s28, 1792\n"
    "v_mfma_f32_16x16x32_bf16 v[176:179], a[160:163], a[144:147], v[176:179]\n"
    "buffer_load_dwordx4 v224, s[8:11], s33 offen offset:256 sc1 lds\n"
    "v_mfma_f32_16x16x32_bf16 v[176:179], a[220:223], a[204:207], v[176:179]\n"
    "ds_read_b64 a[78:79], v229 offset:20992\n"
    "v_mfma_f32_16x16x32_bf16 v[180:183], a[224:227], a[204:207], v[180:183]\n"
    "ds_read_b64 a[80:81], v229 offset:21504\n"
    "v_mfma_f32_16x16x32_bf16 v[180:183], a[164:167], a[144:147], v[180:183]\n"
    "ds_read_b64 a[82:83], v229 offset:22016\n"
    "v_mfma_f32_16x16x32_bf16 v[184:187], a[168:171], a[144:147], v[184:187]\n"
    "s_add_u32 m0, s31, 0\n"
    "v_mfma_f32_16x16x32_bf16 v[184:187], a[228:231], a[204:207], v[184:187]\n"
    "buffer_load_dwordx4 v227, s[20:23], s34 offen lds\n"
    "v_mfma_f32_16x16x32_bf16 v[188:191], a[232:235], a[204:207], v[188:191]\n"
    "ds_read_b64 a[84:85], v229 offset:22528\n"
    "v_mfma_f32_16x16x32_bf16 v[188:191], a[172:175], a[144:147], v[188:191]\n"
    "buffer_load_dword v233, v232, s[48:51], 0 offen\n"
    "v_mfma_f32_16x16x32_bf16 v[192:195], a[176:179], a[144:147], v[192:195]\n"
    "v_add_u32 v232, s63, v232\n"
    "v_mfma_f32_16x16x32_bf16 v[192:195], a[236:239], a[204:207], v[192:195]\n"
    "ds_read_b64 a[86:87], v229 offset:23040\n"
    "v_mfma_f32_16x16x32_bf16 v[220:223], a[236:239], a[208:211], v[220:223]\n"
    "ds_read_b64 a[88:89], v229 offset:23552\n"
    "v_mfma_f32_16x16x32_bf16 v[220:223], a[176:179], a[148:151], v[220:223]\n"
    "s_add_u32 m0, s28, 2688\n"
    "v_mfma_f32_16x16x32_bf16 v[216:219], a[172:175], a[148:151], v[216:219]\n"
    "buffer_load_dwordx4 v224, s[8:11], s33 offen offset:384 sc1 lds\n"
    "v_mfma_f32_16x16x32_bf16 v[216:219], a[232:235], a[208:211], v[216:219]\n"
    "ds_read_b64 a[90:91], v229 offset:24064\n"
    "v_mfma_f32_16x16x32_bf16 v[212:215], a[228:231], a[208:211], v[212:215]\n"
    "v_mfma_f32_16x16x32_bf16 v[212:215], a[168:171], a[148:151], v[212:215]\n"
    "v_mfma_f32_16x16x32_bf16 v[208:211], a[164:167], a[148:151], v[208:211]\n"
    "v_mfma_f32_16x16x32_bf16 v[208:211], a[224:227], a[208:211], v[208:211]\n"
    "s_add_u32 m0, s32, 0\n"
    "v_mfma_f32_16x16x32_bf16 v[204:207], a[220:223], a[208:211], v[204:207]\n"
    "buffer_load_dwordx4 v228, s[24:27], s34 offen lds\n"
    "v_mfma_f32_16x16x32_bf16 v[204:207], a[160:163], a[148:151], v[204:207]\n"
    "s_cmp_eq_u32 s33, s53\n"
    "s_cselect_b32 s59, 655360, s56\n"
    "v_mfma_f32_16x16x32_bf16 v[200:203], a[156:159], a[148:151], v[200:203]\n"
    "s_cselect_b32 s62, 1146880, s57\n"
    "s_add_u32 s33, s33, s59\n"
    "v_mfma_f32_16x16x32_bf16 v[200:203], a[216:219], a[208:211], v[200:203]\n"
    "s_add_u32 s34, s34, s62\n"
    "v_mfma_f32_16x16x32_bf16 v[196:199], a[212:215], a[208:211], v[196:199]\n"
    "v_mfma_f32_16x16x32_bf16 v[196:199], a[152:155], a[148:151], v[196:199]\n"
    "s_waitcnt vmcnt(18) lgkmcnt(0)\n"
    "s_barrier\n"
    "v_mfma_f32_16x16x32_bf16 v[0:3], a[32:35], a[0:3], v[0:3]\n"
    "ds_read_b64 a[120:121], v229 offset:32768\n"
    "v_mfma_f32_16x16x32_bf16 v[0:3], a[92:95], a[60:63], v[0:3]\n"
    "s_add_u32 m0, s28, 16384\n"
    "v_mfma_f32_16x16x32_bf16 v[4:7], a[96:99], a[60:63], v[4:7]\n"
    "buffer_load_dwordx4 v224, s[8:11], s33 offen offset:0 sc1 lds\n"
    "v_mfma_f32_16x16x32_bf16 v[4:7], a[36:39], a[0:3], v[4:7]\n"
    "ds_read_b64 a[122:123], v229 offset:33280\n"
    "v_mfma_f32_16x16x32_bf16 v[8:11], a[40:43], a[0:3], v[8:11]\n"
    "ds_read_b64_tr_b16 a[152:153], v230 offset:30784\n"
    "v_mfma_f32_16x16x32_bf16 v[8:11], a[100:103], a[60:63], v[8:11]\n"
    "ds_read_b64_tr_b16 a[154:155], v230 offset:34368\n"
    "v_mfma_f32_16x16x32_bf16 v[12:15], a[104:107], a[60:63], v[12:15]\n"
    "s_add_u32 m0, s29, 15392\n"
    "v_mfma_f32_16x16x32_bf16 v[12:15], a[44:47], a[0:3], v[12:15]\n"
    "buffer_load_dwordx4 v225, s[12:15], s34 offen lds\n"
    "v_mfma_f32_16x16x32_bf16 v[16:19], a[48:51], a[0:3], v[16:19]\n"
    "ds_read_b64_tr_b16 a[156:157], v230 offset:30816\n"
    "v_mfma_f32_16x16x32_bf16 v[16:19], a[108:111], a[60:63], v[16:19]\n"
    "ds_read_b64_tr_b16 a[158:159], v230 offset:34400\n"
    "v_mfma_f32_16x16x32_bf16 v[20:23], a[112:115], a[60:63], v[20:23]\n"
    "ds_read_b64_tr_b16 a[160:161], v230 offset:30848\n"
    "v_mfma_f32_16x16x32_bf16 v[20:23], a[52:55], a[0:3], v[20:23]\n"
    "s_add_u32 m0, s28, 17280\n"
    "v_mfma_f32_16x16x32_bf16 v[24:27], a[56:59], a[0:3], v[24:27]\n"
    "buffer_load_dwordx4 v224, s[8:11], s33 offen offset:128 sc1 lds\n"
    "v_mfma_f32_16x16x32_bf16 v[24:27], a[116:119], a[60:63], v[24:27]\n"
    "ds_read_b64_tr_b16 a[162:163], v230 offset:34432\n"
    "v_mfma_f32_16x16x32_bf16 v[52:55], a[116:119], a[64:67], v[52:55]\n"
    "ds_read_b64_tr_b16 a[164:165], v230 offset:37968\n"
    "v_mfma_f32_16x16x32_bf16 v[52:55], a[56:59], a[4:7], v[52:55]\n"
    "ds_read_b64_tr_b16 a[166:167], v230 offset:41552\n"
    "v_mfma_f32_16x16x32_bf16 v[48:51], a[52:55], a[4:7], v[48:51]\n"
    "ds_read_b64_tr_b16 a[168:169], v230 offset:38000\n"
    "v_mfma_f32_16x16x32_bf16 v[48:51], a[112:115], a[64:67], v[48:51]\n"
    "ds_read_b64_tr_b16 a[170:171], v230 offset:41584\n"
    "v_mfma_f32_16x16x32_bf16 v[44:47], a[108:111], a[64:67], v[44:47]\n"
    "ds_read_b64_tr_b16 a[172:173], v230 offset:38032\n"
    "v_mfma_f32_16x16x32_bf16 v[44:47], a[48:51], a[4:7], v[44:47]\n"
    "ds_read_b64_tr_b16 a[174:175], v230 offset:41616\n"
    "v_mfma_f32_16x16x32_bf16 v[40:43], a[44:47], a[4:7], v[40:43]\n"
    "ds_read_b64_tr_b16 a[176:177], v231 offset:30784\n"
    "v_mfma_f32_16x16x32_bf16 v[40:43], a[104:107], a[64:67], v[40:43]\n"
    "ds_read_b64_tr_b16 a[178:179], v231 offset:34368\n"
    "v_mfma_f32_16x16x32_bf16 v[36:39], a[100:103], a[64:67], v[36:39]\n"
    "ds_read_b64 a[124:125], v229 offset:33792\n"
    "v_mfma_f32_16x16x32_bf16 v[36:39], a[40:43], a[4:7], v[36:39]\n"
    "ds_read_b64 a[126:127], v229 offset:34304\n"
    "v_mfma_f32_16x16x32_bf16 v[32:35], a[36:39], a[4:7], v[32:35]\n"
    "ds_read_b64 a[128:129], v229 offset:34816\n"
    "v_mfma_f32_16x16x32_bf16 v[32:35], a[96:99], a[64:67], v[32:35]\n"
    "ds_read_b64 a[130:131], v229 offset:35328\n"
    "v_mfma_f32_16x16x32_bf16 v[28:31], a[92:95], a[64:67], v[28:31]\n"
    "s_add_u32 m0, s30, 15392\n"
    "v_mfma_f32_16x16x32_bf16 v[28:31], a[32:35], a[4:7], v[28:31]\n"
    "buffer_load_dwordx4 v226, s[16:19], s34 offen lds\n"
    "v_mfma_f32_16x16x32_bf16 v[56:59], a[32:35], a[8:11], v[56:59]\n"
    "ds_read_b64 a[132:133], v229 offset:35840\n"
    "v_mfma_f32_16x16x32_bf16 v[56:59], a[92:95], a[68:71], v[56:59]\n"
    "ds_read_b64 a[134:135], v229 offset:36352\n"
    "v_mfma_f32_16x16x32_bf16 v[60:63], a[96:99], a[68:71], v[60:63]\n"
    "ds_read_b64 a[136:137], v229 offset:36864\n"
    "v_mfma_f32_16x16x32_bf16 v[60:63], a[36:39], a[8:11], v[60:63]\n"
    "s_add_u32 m0, s28, 18176\n"
    "v_mfma_f32_16x16x32_bf16 v[64:67], a[40:43], a[8:11], v[64:67]\n"
    "buffer_load_dwordx4 v224, s[8:11], s33 offen offset:256 sc1 lds\n"
    "v_mfma_f32_16x16x32_bf16 v[64:67], a[100:103], a[68:71], v[64:67]\n"
    "ds_read_b64 a[138:139], v229 offset:37376\n"
    "v_mfma_f32_16x16x32_bf16 v[68:71], a[104:107], a[68:71], v[68:71]\n"
    "ds_read_b64 a[140:141], v229 offset:37888\n"
    "v_mfma_f32_16x16x32_bf16 v[68:71], a[44:47], a[8:11], v[68:71]\n"
    "ds_read_b64 a[142:143], v229 offset:38400\n"
    "v_mfma_f32_16x16x32_bf16 v[72:75], a[48:51], a[8:11], v[72:75]\n"
    "s_add_u32 m0, s31, 15392\n"
    "v_mfma_f32_16x16x32_bf16 v[72:75], a[108:111], a[68:71], v[72:75]\n"
    "buffer_load_dwordx4 v227, s[20:23], s34 offen lds\n"
    "v_mfma_f32_16x16x32_bf16 v[76:79], a[112:115], a[68:71], v[76:79]\n"
    "ds_read_b64 a[144:145], v229 offset:38912\n"
    "v_mfma_f32_16x16x32_bf16 v[76:79], a[52:55], a[8:11], v[76:79]\n"
    "buffer_load_dword v233, v232, s[48:51], 0 offen\n"
    "v_mfma_f32_16x16x32_bf16 v[80:83], a[56:59], a[8:11], v[80:83]\n"
    "v_add_u32 v232, s63, v232\n"
    "v_mfma_f32_16x16x32_bf16 v[80:83], a[116:119], a[68:71], v[80:83]\n"
    "ds_read_b64 a[146:147], v229 offset:39424\n"
    "v_mfma_f32_16x16x32_bf16 v[108:111], a[116:119], a[72:75], v[108:111]\n"
    "ds_read_b64 a[148:149], v229 offset:39936\n"
    "v_mfma_f32_16x16x32_bf16 v[108:111], a[56:59], a[12:15], v[108:111]\n"
    "s_add_u32 m0, s28, 19072\n"
    "v_mfma_f32_16x16x32_bf16 v[104:107], a[52:55], a[12:15], v[104:107]\n"
    "buffer_load_dwordx4 v224, s[8:11], s33 offen offset:384 sc1 lds\n"
    "v_mfma_f32_16x16x32_bf16 v[104:107], a[112:115], a[72:75], v[104:107]\n"
    "ds_read_b64 a[150:151], v229 offset:40448\n"
    "v_mfma_f32_16x16x32_bf16 v[100:103], a[108:111], a[72:75], v[100:103]\n"
    "v_mfma_f32_16x16x32_bf16 v[100:103], a[48:51], a[12:15], v[100:103]\n"
    "v_mfma_f32_16x16x32_bf16 v[96:99], a[44:47], a[12:15], v[96:99]\n"
    "v_mfma_f32_16x16x32_bf16 v[96:99], a[104:107], a[72:75], v[96:99]\n"
    "s_add_u32 m0, s32, 15392\n"
    "v_mfma_f32_16x16x32_bf16 v[92:95], a[100:103], a[72:75], v[92:95]\n"
    "buffer_load_dwordx4 v228, s[24:27], s34 offen lds\n"
    "v_mfma_f32_16x16x32_bf16 v[92:95], a[40:43], a[12:15], v[92:95]\n"
    "s_cmp_eq_u32 s33, s53\n"
    "s_cselect_b32 s59, 655360, s56\n"
    "v_mfma_f32_16x16x32_bf16 v[88:91], a[36:39], a[12:15], v[88:91]\n"
    "s_cselect_b32 s62, 1146880, s57\n"
    "s_add_u32 s33, s33, s59\n"
    "v_mfma_f32_16x16x32_bf16 v[88:91], a[96:99], a[72:75], v[88:91]\n"
    "s_add_u32 s34, s34, s62\n"
    "s_sub_u32 s35, s35, 1\n"
    "v_mfma_f32_16x16x32_bf16 v[84:87], a[92:95], a[72:75], v[84:87]\n"
    "s_cmp_lg_u32 s35, 0\n"
    "v_mfma_f32_16x16x32_bf16 v[84:87], a[32:35], a[12:15], v[84:87]\n"
    "s_cbranch_scc1 G1_LOOP%=\n"
    "s_waitcnt vmcnt(18) lgkmcnt(0)\n"
    "s_barrier\n"
    "v_mfma_f32_16x16x32_bf16 v[112:115], a[32:35], a[16:19], v[112:115]\n"
    "ds_read_b64 a[180:181], v229 offset:49152\n"
    "v_mfma_f32_16x16x32_bf16 v[112:115], a[92:95], a[76:79], v[112:115]\n"
    "s_add_u32 m0, s28, 32768\n"
    "v_mfma_f32_16x16x32_bf16 v[116:119], a[96:99], a[76:79], v[116:119]\n"
    "buffer_load_dwordx4 v224, s[8:11], s33 offen offset:0 sc1 lds\n"
    "v_mfma_f32_16x16x32_bf16 v[116:119], a[36:39], a[16:19], v[116:119]\n"
    "ds_read_b64 a[182:183], v229 offset:49664\n"
    "v_mfma_f32_16x16x32_bf16 v[120:123], a[40:43], a[16:19], v[120:123]\n"
    "ds_read_b64_tr_b16 a[212:213], v230 offset:46176\n"
    "v_mfma_f32_16x16x32_bf16 v[120:123], a[100:103], a[76:79], v[120:123]\n"
    "ds_read_b64_tr_b16 a[214:215], v230 offset:49760\n"
    "v_mfma_f32_16x16x32_bf16 v[124:127], a[104:107], a[76:79], v[124:127]\n"
    "s_add_u32 m0, s29, 30784\n"
    "v_mfma_f32_16x16x32_bf16 v[124:127], a[44:47], a[16:19], v[124:127]\n"
    "buffer_load_dwordx4 v225, s[12:15], s34 offen lds\n"
    "v_mfma_f32_16x16x32_bf16 v[128:131], a[48:51], a[16:19], v[128:131]\n"
    "ds_read_b64_tr_b16 a[216:217], v230 offset:46208\n"
    "v_mfma_f32_16x16x32_bf16 v[128:131], a[108:111], a[76:79], v[128:131]\n"
    "ds_read_b64_tr_b16 a[218:219], v230 offset:49792\n"
    "v_mfma_f32_16x16x32_bf16 v[132:135], a[112:115], a[76:79], v[132:135]\n"
    "ds_read_b64_tr_b16 a[220:221], v230 offset:46240\n"
    "v_mfma_f32_16x16x32_bf16 v[132:135], a[52:55], a[16:19], v[132:135]\n"
    "s_add_u32 m0, s28, 33664\n"
    "v_mfma_f32_16x16x32_bf16 v[136:139], a[56:59], a[16:19], v[136:139]\n"
    "buffer_load_dwordx4 v224, s[8:11], s33 offen offset:128 sc1 lds\n"
    "v_mfma_f32_16x16x32_bf16 v[136:139], a[116:119], a[76:79], v[136:139]\n"
    "ds_read_b64_tr_b16 a[222:223], v230 offset:49824\n"
    "v_mfma_f32_16x16x32_bf16 v[164:167], a[116:119], a[80:83], v[164:167]\n"
    "ds_read_b64_tr_b16 a[224:225], v230 offset:53360\n"
    "v_mfma_f32_16x16x32_bf16 v[164:167], a[56:59], a[20:23], v[164:167]\n"
    "ds_read_b64_tr_b16 a[226:227], v230 offset:56944\n"
    "v_mfma_f32_16x16x32_bf16 v[160:163], a[52:55], a[20:23], v[160:163]\n"
    "ds_read_b64_tr_b16 a[228:229], v230 offset:53392\n"
    "v_mfma_f32_16x16x32_bf16 v[160:163], a[112:115], a[80:83], v[160:163]\n"
    "ds_read_b64_tr_b16 a[230:231], v230 offset:56976\n"
    "v_mfma_f32_16x16x32_bf16 v[156:159], a[108:111], a[80:83], v[156:159]\n"
    "ds_read_b64_tr_b16 a[232:233], v230 offset:53424\n"
    "v_mfma_f32_16x16x32_bf16 v[156:159], a[48:51], a[20:23], v[156:159]\n"
    "ds_read_b64_tr_b16 a[234:235], v230 offset:57008\n"
    "v_mfma_f32_16x16x32_bf16 v[152:155], a[44:47], a[20:23], v[152:155]\n"
    "ds_read_b64_tr_b16 a[236:237], v231 offset:46176\n"
    "v_mfma_f32_16x16x32_bf16 v[152:155], a[104:107], a[80:83], v[152:155]\n"
    "ds_read_b64_tr_b16 a[238:239], v231 offset:49760\n"
    "v_mfma_f32_16x16x32_bf16 v[148:151], a[100:103], a[80:83], v[148:151]\n"
    "ds_read_b64 a[184:185], v229 offset:50176\n"
    "v_mfma_f32_16x16x32_bf16 v[148:151], a[40:43], a[20:23], v[148:151]\n"
    "ds_read_b64 a[186:187], v229 offset:50688\n"
    "v_mfma_f32_16x16x32_bf16 v[144:147], a[36:39], a[20:23], v[144:147]\n"
    "ds_read_b64 a[188:189], v229 offset:51200\n"
    "v_mfma_f32_16x16x32_bf16 v[144:147], a[96:99], a[80:83], v[144:147]\n"
    "ds_read_b64 a[190:191], v229 offset:51712\n"
    "v_mfma_f32_16x16x32_bf16 v[140:143], a[92:95], a[80:83], v[140:143]\n"
    "s_add_u32 m0, s30, 30784\n"
    "v_mfma_f32_16x16x32_bf16 v[140:143], a[32:35], a[20:23], v[140:143]\n"
    "buffer_load_dwordx4 v226, s[16:19], s34 offen lds\n"
    "v_mfma_f32_16x16x32_bf16 v[168:171], a[32:35], a[24:27], v[168:171]\n"
    "ds_read_b64 a[192:193], v229 offset:52224\n"
    "v_mfma_f32_16x16x32_bf16 v[168:171], a[92:95], a[84:87], v[168:171]\n"
    "ds_read_b64 a[194:195], v229 offset:52736\n"
    "v_mfma_f32_16x16x32_bf16 v[172:175], a[96:99], a[84:87], v[172:175]\n"
    "ds_read_b64 a[196:197], v229 offset:53248\n"
    "v_mfma_f32_16x16x32_bf16 v[172:175], a[36:39], a[24:27], v[172:175]\n"
    "s_add_u32 m0, s28, 34560\n"
    "v_mfma_f32_16x16x32_bf16 v[176:179], a[40:43], a[24:27], v[176:179]\n"
    "buffer_load_dwordx4 v224, s[8:11], s33 offen offset:256 sc1 lds\n"
    "v_mfma_f32_16x16x32_bf16 v[176:179], a[100:103], a[84:87], v[176:179]\n"
    "ds_read_b64 a[198:199], v229 offset:53760\n"
    "v_mfma_f32_16x16x32_bf16 v[180:183], a[104:107], a[84:87], v[180:183]\n"
    "ds_read_b64 a[200:201], v229 offset:54272\n"
    "v_mfma_f32_16x16x32_bf16 v[180:183], a[44:47], a[24:27], v[180:183]\n"
    "ds_read_b64 a[202:203], v229 offset:54784\n"
    "v_mfma_f32_16x16x32_bf16 v[184:187], a[48:51], a[24:27], v[184:187]\n"
    "s_add_u32 m0, s31, 30784\n"
    "v_mfma_f32_16x16x32_bf16 v[184:187], a[108:111], a[84:87], v[184:187]\n"
    "buffer_load_dwordx4 v227, s[20:23], s34 offen lds\n"
    "v_mfma_f32_16x16x32_bf16 v[188:191], a[112:115], a[84:87], v[188:191]\n"
    "ds_read_b64 a[204:205], v229 offset:55296\n"
    "v_mfma_f32_16x16x32_bf16 v[188:191], a[52:55], a[24:27], v[188:191]\n"
    "buffer_load_dword v233, v232, s[48:51], 0 offen\n"
    "v_mfma_f32_16x16x32_bf16 v[192:195], a[56:59], a[24:27], v[192:195]\n"
    "v_add_u32 v232, s63, v232\n"
    "v_mfma_f32_16x16x32_bf16 v[192:195], a[116:119], a[84:87], v[192:195]\n"
    "ds_read_b64 a[206:207], v229 offset:55808\n"
    "v_mfma_f32_16x16x32_bf16 v[220:223], a[116:119], a[88:91], v[220:223]\n"
    "ds_read_b64 a[208:209], v229 offset:56320\n"
    "v_mfma_f32_16x16x32_bf16 v[220:223], a[56:59], a[28:31], v[220:223]\n"
    "s_add_u32 m0, s28, 35456\n"
    "v_mfma_f32_16x16x32_bf16 v[216:219], a[52:55], a[28:31], v[216:219]\n"
    "buffer_load_dwordx4 v224, s[8:11], s33 offen offset:384 sc1 lds\n"
    "v_mfma_f32_16x16x32_bf16 v[216:219], a[112:115], a[88:91], v[216:219]\n"
    "ds_read_b64 a[210:211], v229 offset:56832\n"
    "v_mfma_f32_16x16x32_bf16 v[212:215], a[108:111], a[88:91], v[212:215]\n"
    "v_mfma_f32_16x16x32_bf16 v[212:215], a[48:51], a[28:31], v[212:215]\n"
    "v_mfma_f32_16x16x32_bf16 v[208:211], a[44:47], a[28:31], v[208:211]\n"
    "v_mfma_f32_16x16x32_bf16 v[208:211], a[104:107], a[88:91], v[208:211]\n"
    "s_add_u32 m0, s32, 30784\n"
    "v_mfma_f32_16x16x32_bf16 v[204:207], a[100:103], a[88:91], v[204:207]\n"
    "buffer_load_dwordx4 v228, s[24:27], s34 offen lds\n"
    "v_mfma_f32_16x16x32_bf16 v[204:207], a[40:43], a[28:31], v[204:207]\n"
    "s_cmp_eq_u32 s33, s53\n"
    "s_cselect_b32 s59, 655360, s56\n"
    "v_mfma_f32_16x16x32_bf16 v[200:203], a[36:39], a[28:31], v[200:203]\n"
    "s_cselect_b32 s62, 1146880, s57\n"
    "s_add_u32 s33, s33, s59\n"
    "v_mfma_f32_16x16x32_bf16 v[200:203], a[96:99], a[88:91], v[200:203]\n"
    "s_add_u32 s34, s34, s62\n"
    "v_mfma_f32_16x16x32_bf16 v[196:199], a[92:95], a[88:91], v[196:199]\n"
    "v_mfma_f32_16x16x32_bf16 v[196:199], a[32:35], a[28:31], v[196:199]\n"
    "s_waitcnt vmcnt(18) lgkmcnt(0)\n"
    "s_barrier\n"
    "v_mfma_f32_16x16x32_bf16 v[0:3], a[152:155], a[120:123], v[0:3]\n"
    "ds_read_b64 a[0:1], v229 offset:0\n"
    "v_mfma_f32_16x16x32_bf16 v[0:3], a[212:215], a[180:183], v[0:3]\n"
    "s_add_u32 m0, s28, 49152\n"
    "v_mfma_f32_16x16x32_bf16 v[4:7], a[216:219], a[180:183], v[4:7]\n"
    "buffer_load_dwordx4 v224, s[8:11], s33 offen offset:0 sc1 lds\n"
    "v_mfma_f32_16x16x32_bf16 v[4:7], a[156:159], a[120:123], v[4:7]\n"
    "ds_read_b64 a[2:3], v229 offset:512\n"
    "v_mfma_f32_16x16x32_bf16 v[8:11], a[160:163], a[120:123], v[8:11]\n"
    "ds_read_b64_tr_b16 a[32:33], v230 offset:0\n"
    "v_mfma_f32_16x16x32_bf16 v[8:11], a[220:223], a[180:183], v[8:11]\n"
    "ds_read_b64_tr_b16 a[34:35], v230 offset:3584\n"
    "v_mfma_f32_16x16x32_bf16 v[12:15], a[224:227], a[180:183], v[12:15]\n"
    "s_add_u32 m0, s29, 46176\n"
    "v_mfma_f32_16x16x32_bf16 v[12:15], a[164:167], a[120:123], v[12:15]\n"
    "buffer_load_dwordx4 v225, s[12:15], s34 offen lds\n"
    "v_mfma_f32_16x16x32_bf16 v[16:19], a[168:171], a[120:123], v[16:19]\n"
    "ds_read_b64_tr_b16 a[36:37], v230 offset:32\n"
    "v_mfma_f32_16x16x32_bf16 v[16:19], a[228:231], a[180:183], v[16:19]\n"
    "ds_read_b64_tr_b16 a[38:39], v230 offset:3616\n"
    "v_mfma_f32_16x16x32_bf16 v[20:23], a[232:235], a[180:183], v[20:23]\n"
    "ds_read_b64_tr_b16 a[40:41], v230 offset:64\n"
    "v_mfma_f32_16x16x32_bf16 v[20:23], a[172:175], a[120:123], v[20:23]\n"
    "s_add_u32 m0, s28, 50048\n"
    "v_mfma_f32_16x16x32_bf16 v[24:27], a[176:179], a[120:123], v[24:27]\n"
    "buffer_load_dwordx4 v224, s[8:11], s33 offen offset:128 sc1 lds\n"
    "v_mfma_f32_16x16x32_bf16 v[24:27], a[236:239], a[180:183], v[24:27]\n"
    "ds_read_b64_tr_b16 a[42:43], v230 offset:3648\n"
    "v_mfma_f32_16x16x32_bf16 v[52:55], a[236:239], a[184:187], v[52:55]\n"
    "ds_read_b64_tr_b16 a[44:45], v230 offset:7184\n"
    "v_mfma_f32_16x16x32_bf16 v[52:55], a[176:179], a[124:127], v[52:55]\n"
    "ds_read_b64_tr_b16 a[46:47], v230 offset:10768\n"
    "v_mfma_f32_16x16x32_bf16 v[48:51], a[172:175], a[124:127], v[48:51]\n"
    "ds_read_b64_tr_b16 a[48:49], v230 offset:7216\n"
    "v_mfma_f32_16x16x32_bf16 v[48:51], a[232:235], a[184:187], v[48:51]\n"
    "ds_read_b64_tr_b16 a[50:51], v230 offset:10800\n"
    "v_mfma_f32_16x16x32_bf16 v[44:47], a[228:231], a[184:187], v[44:47]\n"
    "ds_read_b64_tr_b16 a[52:53], v230 offset:7248\n"
    "v_mfma_f32_16x16x32_bf16 v[44:47], a[168:171], a[124:127], v[44:47]\n"
    "ds_read_b64_tr_b16 a[54:55], v230 offset:10832\n"
    "v_mfma_f32_16x16x32_bf16 v[40:43], a[164:167], a[124:127], v[40:43]\n"
    "ds_read_b64_tr_b16 a[56:57], v231 offset:0\n"
    "v_mfma_f32_16x16x32_bf16 v[40:43], a[224:227], a[184:187], v[40:43]\n"
    "ds_read_b64_tr_b16 a[58:59], v231 offset:3584\n"
    "v_mfma_f32_16x16x32_bf16 v[36:39], a[220:223], a[184:187], v[36:39]\n"
    "ds_read_b64 a[4:5], v229 offset:1024\n"
    "v_mfma_f32_16x16x32_bf16 v[36:39], a[160:163], a[124:127], v[36:39]\n"
    "ds_read_b64 a[6:7], v229 offset:1536\n"
    "v_mfma_f32_16x16x32_bf16 v[32:35], a[156:159], a[124:127], v[32:35]\n"
    "ds_read_b64 a[8:9], v229 offset:2048\n"
    "v_mfma_f32_16x16x32_bf16 v[32:35], a[216:219], a[184:187], v[32:35]\n"
    "ds_read_b64 a[10:11], v229 offset:2560\n"
    "v_mfma_f32_16x16x32_bf16 v[28:31], a[212:215], a[184:187], v[28:31]\n"
    "s_add_u32 m0, s30, 46176\n"
    "v_mfma_f32_16x16x32_bf16 v[28:31], a[152:155], a[124:127], v[28:31]\n"
    "buffer_load_dwordx4 v226, s[16:19], s34 offen lds\n"
    "v_mfma_f32_16x16x32_bf16 v[56:59], a[152:155], a[128:131], v[56:59]\n"
    "ds_read_b64 a[12:13], v229 offset:3072\n"
    "v_mfma_f32_16x16x32_bf16 v[56:59], a[212:215], a[188:191], v[56:59]\n"
    "ds_read_b64 a[14:15], v229 offset:3584\n"
    "v_mfma_f32_16x16x32_bf16 v[60:63], a[216:219], a[188:191], v[60:63]\n"
    "ds_read_b64 a[16:17], v229 offset:4096\n"
    "v_mfma_f32_16x16x32_bf16 v[60:63], a[156:159], a[128:131], v[60:63]\n"
    "s_add_u32 m0, s28, 50944\n"
    "v_mfma_f32_16x16x32_bf16 v[64:67], a[160:163], a[128:131], v[64:67]\n"
    "buffer_load_dwordx4 v224, s[8:11], s33 offen offset:256 sc1 lds\n"
    "v_mfma_f32_16x16x32_bf16 v[64:67], a[220:223], a[188:191], v[64:67]\n"
    "ds_read_b64 a[18:19], v229 offset:4608\n"
    "v_mfma_f32_16x16x32_bf16 v[68:71], a[224:227], a[188:191], v[68:71]\n"
    "ds_read_b64 a[20:21], v229 offset:5120\n"
    "v_mfma_f32_16x16x32_bf16 v[68:71], a[164:167], a[128:131], v[68:71]\n"
    "ds_read_b64 a[22:23], v229 offset:5632\n"
    "v_mfma_f32_16x16x32_bf16 v[72:75], a[168:171], a[128:131], v[72:75]\n"
    "s_add_u32 m0, s31, 46176\n"
    "v_mfma_f32_16x16x32_bf16 v[72:75], a[228:231], a[188:191], v[72:75]\n"
    "buffer_load_dwordx4 v227, s[20:23], s34 offen lds\n"
    "v_mfma_f32_16x16x32_bf16 v[76:79], a[232:235], a[188:191], v[76:79]\n"
    "ds_read_b64 a[24:25], v229 offset:6144\n"
    "v_mfma_f32_16x16x32_bf16 v[76:79], a[172:175], a[128:131], v[76:79]\n"
    "buffer_load_dword v233, v232, s[48:51], 0 offen\n"
    "v_mfma_f32_16x16x32_bf16 v[80:83], a[176:179], a[128:131], v[80:83]\n"
    "v_add_u32 v232, s63, v232\n"
    "v_mfma_f32_16x16x32_bf16 v[80:83], a[236:239], a[188:191], v[80:83]\n"
    "ds_read_b64 a[26:27], v229 offset:6656\n"
    "v_mfma_f32_16x16x32_bf16 v[108:111], a[236:239], a[192:195], v[108:111]\n"
    "ds_read_b64 a[28:29], v229 offset:7168\n"
    "v_mfma_f32_16x16x32_bf16 v[108:111], a[176:179], a[132:135], v[108:111]\n"
    "s_add_u32 m0, s28, 51840\n"
    "v_mfma_f32_16x16x32_bf16 v[104:107], a[172:175], a[132:135], v[104:107]\n"
    "buffer_load_dwordx4 v224, s[8:11], s33 offen offset:384 sc1 lds\n"
    "v_mfma_f32_16x16x32_bf16 v[104:107], a[232:235], a[192:195], v[104:107]\n"
    "ds_read_b64 a[30:31], v229 offset:7680\n"
    "v_mfma_f32_16x16x32_bf16 v[100:103], a[228:231], a[192:195], v[100:103]\n"
    "v_mfma_f32_16x16x32_bf16 v[100:103], a[168:171], a[132:135], v[100:103]\n"
    "v_mfma_f32_16x16x32_bf16 v[96:99], a[164:167], a[132:135], v[96:99]\n"
    "v_mfma_f32_16x16x32_bf16 v[96:99], a[224:227], a[192:195], v[96:99]\n"
    "s_add_u32 m0, s32, 46176\n"
    "v_mfma_f32_16x16x32_bf16 v[92:95], a[220:223], a[192:195], v[92:95]\n"
    "buffer_load_dwordx4 v228, s[24:27], s34 offen lds\n"
    "v_mfma_f32_16x16x32_bf16 v[92:95], a[160:163], a[132:135], v[92:95]\n"
    "s_cmp_eq_u32 s33, s53\n"
    "s_cselect_b32 s59, 655360, s56\n"
    "v_mfma_f32_16x16x32_bf16 v[88:91], a[156:159], a[132:135], v[88:91]\n"
    "s_cselect_b32 s62, 1146880, s57\n"
    "s_add_u32 s33, s33, s59\n"
    "v_mfma_f32_16x16x32_bf16 v[88:91], a[216:219], a[192:195], v[88:91]\n"
    "s_add_u32 s34, s34, s62\n"
    "v_mfma_f32_16x16x32_bf16 v[84:87], a[212:215], a[192:195], v[84:87]\n"
    "v_mfma_f32_16x16x32_bf16 v[84:87], a[152:155], a[132:135], v[84:87]\n"
    "s_waitcnt vmcnt(18) lgkmcnt(0)\n"
    "s_barrier\n"
    "v_mfma_f32_16x16x32_bf16 v[112:115], a[152:155], a[136:139], v[112:115]\n"
    "v_mbcnt_lo_u32_b32 v236, -1, 0\n"
    "v_mbcnt_hi_u32_b32 v236, -1, v236\n"
    "v_mfma_f32_16x16x32_bf16 v[112:115], a[212:215], a[196:199], v[112:115]\n"
    "v_lshrrev_b32 v237, 4, v236\n"
    "ds_read_b64 a[60:61], v229 offset:16384\n"
    "v_mfma_f32_16x16x32_bf16 v[116:119], a[216:219], a[196:199], v[116:119]\n"
    "v_and_b32 v236, 15, v236\n"
    "ds_read_b64 a[62:63], v229 offset:16896\n"
    "v_mfma_f32_16x16x32_bf16 v[116:119], a[156:159], a[136:139], v[116:119]\n"
    "v_lshlrev_b32 v238, 1, v237\n"
    "ds_read_b64_tr_b16 a[92:93], v230 offset:15392\n"
    "v_mfma_f32_16x16x32_bf16 v[120:123], a[160:163], a[136:139], v[120:123]\n"
    "v_xor_b32 v236, v236, v238\n"
    "v_lshlrev_b32 v237, 11, v237\n"
    "v_mfma_f32_16x16x32_bf16 v[120:123], a[220:223], a[196:199], v[120:123]\n"
    "s_and_b32 s56, s5, 7\n"
    "ds_read_b64_tr_b16 a[94:95], v230 offset:18976\n"
    "v_mfma_f32_16x16x32_bf16 v[124:127], a[224:227], a[196:199], v[124:127]\n"
    "s_lshl_b32 s56, s56, 8\n"
    "ds_read_b64_tr_b16 a[96:97], v230 offset:15424\n"
    "v_mfma_f32_16x16x32_bf16 v[124:127], a[164:167], a[136:139], v[124:127]\n"
    "s_lshl_b32 s57, s37, 14\n"
    "ds_read_b64_tr_b16 a[98:99], v230 offset:19008\n"
    "v_mfma_f32_16x16x32_bf16 v[128:131], a[168:171], a[136:139], v[128:131]\n"
    "S2_H3%=:\n"
    "s_cbranch_execz S2_R%=\n"
    "s_add_u32 s57, s57, s56\n"
    "s_lshr_b32 s58, s37, 1\n"
    "v_mfma_f32_16x16x32_bf16 v[128:131], a[228:231], a[196:199], v[128:131]\n"
    "s_lshl_b32 s58, s58, 13\n"
    "ds_read_b64_tr_b16 a[100:101], v230 offset:15456\n"
    "v_mfma_f32_16x16x32_bf16 v[132:135], a[232:235], a[196:199], v[132:135]\n"
    "s_add_u32 s58, s58, s56\n"
    "ds_read_b64_tr_b16 a[102:103], v230 offset:19040\n"
    "v_mfma_f32_16x16x32_bf16 v[132:135], a[172:175], a[136:139], v[132:135]\n"
    "v_lshl_add_u32 v238, v236, 4, v237\n"
    "ds_read_b64_tr_b16 a[104:105], v230 offset:22576\n"
    "v_mfma_f32_16x16x32_bf16 v[136:139], a[176:179], a[136:139], v[136:139]\n"
    "v_add_u32 v224, s57, v238\n"
    "v_add_u32 v226, s58, v238\n"
    "v_mfma_f32_16x16x32_bf16 v[136:139], a[236:239], a[196:199], v[136:139]\n"
    "v_xor_b32 v236, 8, v236\n"
    "ds_read_b64_tr_b16 a[106:107], v230 offset:26160\n"
    "v_mfma_f32_16x16x32_bf16 v[164:167], a[236:239], a[200:203], v[164:167]\n"
    "s_add_u32 s57, s57, 8192\n"
    "ds_read_b64_tr_b16 a[108:109], v230 offset:22608\n"
    "v_mfma_f32_16x16x32_bf16 v[164:167], a[176:179], a[140:143], v[164:167]\n"
    "s_add_u32 s58, s58, 16384\n"
    "ds_read_b64_tr_b16 a[110:111], v230 offset:26192\n"
    "v_mfma_f32_16x16x32_bf16 v[160:163], a[172:175], a[140:143], v[160:163]\n"
    "v_lshl_add_u32 v238, v236, 4, v237\n"
    "v_add_u32 v225, s57, v238\n"
    "v_mfma_f32_16x16x32_bf16 v[160:163], a[232:235], a[200:203], v[160:163]\n"
    "v_add_u32 v227, s58, v238\n"
    "ds_read_b64_tr_b16 a[112:113], v230 offset:22640\n"
    "v_mfma_f32_16x16x32_bf16 v[156:159], a[228:231], a[200:203], v[156:159]\n"
    "ds_read_b64_tr_b16 a[114:115], v230 offset:26224\n"
    "ds_read_b64_tr_b16 a[116:117], v231 offset:15392\n"
    "v_mfma_f32_16x16x32_bf16 v[156:159], a[168:171], a[140:143], v[156:159]\n"
    "ds_read_b64_tr_b16 a[118:119], v231 offset:18976\n"
    "ds_read_b64 a[64:65], v229 offset:17408\n"
    "v_mfma_f32_16x16x32_bf16 v[152:155], a[164:167], a[140:143], v[152:155]\n"
    "v_mbcnt_lo_u32_b32 v240, -1, 0\n"
    "ds_read_b64 a[66:67], v229 offset:17920\n"
    "v_mfma_f32_16x16x32_bf16 v[152:155], a[224:227], a[200:203], v[152:155]\n"
    "v_mbcnt_hi_u32_b32 v240, -1, v240\n"
    "ds_read_b64 a[68:69], v229 offset:18432\n"
    "v_mfma_f32_16x16x32_bf16 v[148:151], a[220:223], a[200:203], v[148:151]\n"
    "s_and_b32 s56, s37, 1\n"
    "ds_read_b64 a[70:71], v229 offset:18944\n"
    "v_mfma_f32_16x16x32_bf16 v[148:151], a[160:163], a[140:143], v[148:151]\n"
    "s_lshl_b32 s56, s56, 6\n"
    "ds_read_b64 a[72:73], v229 offset:19456\n"
    "v_mfma_f32_16x16x32_bf16 v[144:147], a[156:159], a[140:143], v[144:147]\n"
    "v_add_u32 v234, s56, v240\n"
    "ds_read_b64 a[74:75], v229 offset:19968\n"
    "v_mfma_f32_16x16x32_bf16 v[144:147], a[216:219], a[200:203], v[144:147]\n"
    "ds_read_b64 a[76:77], v229 offset:20480\n"
    "ds_read_b64 a[78:79], v229 offset:20992\n"
    "v_mfma_f32_16x16x32_bf16 v[140:143], a[212:215], a[200:203], v[140:143]\n"
    "v_add_u32 v235, 16, v234\n"
    "ds_read_b64 a[80:81], v229 offset:21504\n"
    "v_mfma_f32_16x16x32_bf16 v[140:143], a[152:155], a[140:143], v[140:143]\n"
    "v_lshrrev_b32 v235, 7, v235\n"
    "ds_read_b64 a[82:83], v229 offset:22016\n"
    "v_mfma_f32_16x16x32_bf16 v[168:171], a[152:155], a[144:147], v[168:171]\n"
    "s_lshr_b32 s56, s37, 1\n"
    "ds_read_b64 a[84:85], v229 offset:22528\n"
    "v_mfma_f32_16x16x32_bf16 v[168:171], a[212:215], a[204:207], v[168:171]\n"
    "s_mul_i32 s56, s56, 3584\n"
    "ds_read_b64 a[86:87], v229 offset:23040\n"
    "v_mfma_f32_16x16x32_bf16 v[172:175], a[216:219], a[204:207], v[172:175]\n"
    "s_mul_i32 s57, s6, 7168\n"
    "ds_read_b64 a[88:89], v229 offset:23552\n"
    "v_mfma_f32_16x16x32_bf16 v[172:175], a[156:159], a[144:147], v[172:175]\n"
    "ds_read_b64 a[90:91], v229 offset:24064\n"
    "v_mfma_f32_16x16x32_bf16 v[176:179], a[160:163], a[144:147], v[176:179]\n"
    "s_add_u32 s56, s56, s57\n"
    "v_mfma_f32_16x16x32_bf16 v[176:179], a[220:223], a[204:207], v[176:179]\n"
    "s_mul_i32 s57, s7, 112\n"
    "v_mfma_f32_16x16x32_bf16 v[180:183], a[224:227], a[204:207], v[180:183]\n"
    "s_add_u32 s56, s56, s57\n"
    "s_add_u32 s50, s45, 0\n"
    "v_mfma_f32_16x16x32_bf16 v[180:183], a[164:167], a[144:147], v[180:183]\n"
    "s_add_u32 s56, s56, 67584\n"
    "s_add_u32 m0, s44, 0\n"
    "v_mfma_f32_16x16x32_bf16 v[184:187], a[168:171], a[144:147], v[184:187]\n"
    "v_add_lshl_u32 v234, v234, s56, 3\n"
    "buffer_load_dwordx4 v224, s[40:43], s50 offen lds\n"
    "v_mfma_f32_16x16x32_bf16 v[184:187], a[228:231], a[204:207], v[184:187]\n"
    "v_mfma_f32_16x16x32_bf16 v[188:191], a[232:235], a[204:207], v[188:191]\n"
    "v_lshl_or_b32 v243, v235, 31, v234\n"
    "v_mfma_f32_16x16x32_bf16 v[188:191], a[172:175], a[144:147], v[188:191]\n"
    "s_lshl_b32 s56, s37, 6\n"
    "v_mfma_f32_16x16x32_bf16 v[192:195], a[176:179], a[144:147], v[192:195]\n"
    "v_add_u32 v240, s56, v240\n"
    "v_mfma_f32_16x16x32_bf16 v[192:195], a[236:239], a[204:207], v[192:195]\n"
    "s_add_u32 s56, s3, 127104\n"
    "v_mfma_f32_16x16x32_bf16 v[220:223], a[236:239], a[208:211], v[220:223]\n"
    "v_lshl_add_u32 v250, v240, 2, s56\n"
    "v_mfma_f32_16x16x32_bf16 v[220:223], a[176:179], a[148:151], v[220:223]\n"
    "s_add_u32 m0, s44, 1024\n"
    "v_mfma_f32_16x16x32_bf16 v[216:219], a[172:175], a[148:151], v[216:219]\n"
    "v_min_u32 v240, 223, v240\n"
    "buffer_load_dwordx4 v225, s[40:43], s50 offen lds\n"
    "v_mfma_f32_16x16x32_bf16 v[216:219], a[232:235], a[208:211], v[216:219]\n"
    "v_add_u32 v234, 16, v240\n"
    "v_mfma_f32_16x16x32_bf16 v[212:215], a[228:231], a[208:211], v[212:215]\n"
    "v_lshrrev_b32 v234, 7, v234\n"
    "v_mfma_f32_16x16x32_bf16 v[212:215], a[168:171], a[148:151], v[212:215]\n"
    "v_mul_u32_u24 v234, 3472, v234\n"
    "v_mfma_f32_16x16x32_bf16 v[208:211], a[164:167], a[148:151], v[208:211]\n"
    "v_add3_u32 v240, v240, v234, s57\n"
    "v_mfma_f32_16x16x32_bf16 v[208:211], a[224:227], a[208:211], v[208:211]\n"
    "v_mfma_f32_16x16x32_bf16 v[204:207], a[220:223], a[208:211], v[204:207]\n"
    "v_lshlrev_b32 v245, 3, v240\n"
    "v_mfma_f32_16x16x32_bf16 v[204:207], a[160:163], a[148:151], v[204:207]\n"
    "v_mfma_f32_16x16x32_bf16 v[200:203], a[156:159], a[148:151], v[200:203]\n"
    "v_mfma_f32_16x16x32_bf16 v[200:203], a[216:219], a[208:211], v[200:203]\n"
    "v_mfma_f32_16x16x32_bf16 v[196:199], a[212:215], a[208:211], v[196:199]\n"
    "v_mfma_f32_16x16x32_bf16 v[196:199], a[152:155], a[148:151], v[196:199]\n"
    "s_waitcnt vmcnt(11) lgkmcnt(0)\n"
    "s_barrier\n"
    "v_mfma_f32_16x16x32_bf16 v[0:3], a[32:35], a[0:3], v[0:3]\n"
    "ds_read_b64 a[120:121], v229 offset:32768\n"
    "ds_read_b64 a[122:123], v229 offset:33280\n"
    "v_mfma_f32_16x16x32_bf16 v[0:3], a[92:95], a[60:63], v[0:3]\n"
    "s_add_u32 s50, s45, 131072\n"
    "ds_read_b64_tr_b16 a[152:153], v230 offset:30784\n"
    "v_mfma_f32_16x16x32_bf16 v[4:7], a[96:99], a[60:63], v[4:7]\n"
    "s_add_u32 m0, s44, 8192\n"
    "ds_read_b64_tr_b16 a[154:155], v230 offset:34368\n"
    "v_mfma_f32_16x16x32_bf16 v[4:7], a[36:39], a[0:3], v[4:7]\n"
    "buffer_load_dwordx4 v224, s[40:43], s50 offen lds\n"
    "ds_read_b64_tr_b16 a[156:157], v230 offset:30816\n"
    "v_mfma_f32_16x16x32_bf16 v[8:11], a[40:43], a[0:3], v[8:11]\n"
    "ds_read_b64_tr_b16 a[158:159], v230 offset:34400\n"
    "ds_read_b64_tr_b16 a[160:161], v230 offset:30848\n"
    "v_mfma_f32_16x16x32_bf16 v[8:11], a[100:103], a[60:63], v[8:11]\n"
    "ds_read_b64_tr_b16 a[162:163], v230 offset:34432\n"
    "ds_read_b64_tr_b16 a[164:165], v230 offset:37968\n"
    "v_mfma_f32_16x16x32_bf16 v[12:15], a[104:107], a[60:63], v[12:15]\n"
    "ds_read_b64_tr_b16 a[166:167], v230 offset:41552\n"
    "ds_read_b64_tr_b16 a[168:169], v230 offset:38000\n"
    "v_mfma_f32_16x16x32_bf16 v[12:15], a[44:47], a[0:3], v[12:15]\n"
    "ds_read_b64_tr_b16 a[170:171], v230 offset:41584\n"
    "ds_read_b64_tr_b16 a[172:173], v230 offset:38032\n"
    "v_mfma_f32_16x16x32_bf16 v[16:19], a[48:51], a[0:3], v[16:19]\n"
    "ds_read_b64_tr_b16 a[174:175], v230 offset:41616\n"
    "ds_read_b64_tr_b16 a[176:177], v231 offset:30784\n"
    "v_mfma_f32_16x16x32_bf16 v[16:19], a[108:111], a[60:63], v[16:19]\n"
    "s_add_u32 m0, s44, 9216\n"
    "ds_read_b64_tr_b16 a[178:179], v231 offset:34368\n"
    "v_mfma_f32_16x16x32_bf16 v[20:23], a[112:115], a[60:63], v[20:23]\n"
    "buffer_load_dwordx4 v225, s[40:43], s50 offen lds\n"
    "ds_read_b64 a[124:125], v229 offset:33792\n"
    "v_mfma_f32_16x16x32_bf16 v[20:23], a[52:55], a[0:3], v[20:23]\n"
    "ds_read_b64 a[126:127], v229 offset:34304\n"
    "ds_read_b64 a[128:129], v229 offset:34816\n"
    "v_mfma_f32_16x16x32_bf16 v[24:27], a[56:59], a[0:3], v[24:27]\n"
    "ds_read_b64 a[130:131], v229 offset:35328\n"
    "ds_read_b64 a[132:133], v229 offset:35840\n"
    "v_mfma_f32_16x16x32_bf16 v[24:27], a[116:119], a[60:63], v[24:27]\n"
    "ds_read_b64 a[134:135], v229 offset:36352\n"
    "ds_read_b64 a[136:137], v229 offset:36864\n"
    "v_mfma_f32_16x16x32_bf16 v[52:55], a[116:119], a[64:67], v[52:55]\n"
    "s_mov_b32 s36, 540672\n"
    "ds_read_b64 a[138:139], v229 offset:37376\n"
    "v_mfma_f32_16x16x32_bf16 v[52:55], a[56:59], a[4:7], v[52:55]\n"
    "buffer_load_dwordx2 a[244:245], v245, s[8:11], s36 offen sc1\n"
    "s_add_u32 s50, s60, 65536\n"
    "v_mfma_f32_16x16x32_bf16 v[48:51], a[52:55], a[4:7], v[48:51]\n"
    "s_add_u32 m0, s44, 16384\n"
    "ds_read_b64 a[140:141], v229 offset:37888\n"
    "v_mfma_f32_16x16x32_bf16 v[48:51], a[112:115], a[64:67], v[48:51]\n"
    "buffer_load_dwordx4 v226, s[40:43], s50 offen lds\n"
    "ds_read_b64 a[142:143], v229 offset:38400\n"
    "v_mfma_f32_16x16x32_bf16 v[44:47], a[108:111], a[64:67], v[44:47]\n"
    "ds_read_b64 a[144:145], v229 offset:38912\n"
    "ds_read_b64 a[146:147], v229 offset:39424\n"
    "v_mfma_f32_16x16x32_bf16 v[44:47], a[48:51], a[4:7], v[44:47]\n"
    "s_mov_b32 s36, 598016\n"
    "ds_read_b64 a[148:149], v229 offset:39936\n"
    "v_mfma_f32_16x16x32_bf16 v[40:43], a[44:47], a[4:7], v[40:43]\n"
    "buffer_load_dwordx2 a[246:247], v245, s[8:11], s36 offen sc1\n"
    "ds_read_b64 a[150:151], v229 offset:40448\n"
    "v_mfma_f32_16x16x32_bf16 v[40:43], a[104:107], a[64:67], v[40:43]\n"
    "v_mfma_f32_16x16x32_bf16 v[36:39], a[100:103], a[64:67], v[36:39]\n"
    "v_mfma_f32_16x16x32_bf16 v[36:39], a[40:43], a[4:7], v[36:39]\n"
    "s_add_u32 m0, s44, 17408\n"
    "v_mfma_f32_16x16x32_bf16 v[32:35], a[36:39], a[4:7], v[32:35]\n"
    "s_mov_b32 s36, 655360\n"
    "buffer_load_dwordx4 v227, s[40:43], s50 offen lds\n"
    "v_mfma_f32_16x16x32_bf16 v[32:35], a[96:99], a[64:67], v[32:35]\n"
    "buffer_load_dwordx2 a[248:249], v245, s[8:11], s36 offen sc1\n"
    "v_mfma_f32_16x16x32_bf16 v[28:31], a[92:95], a[64:67], v[28:31]\n"
    "v_mfma_f32_16x16x32_bf16 v[28:31], a[32:35], a[4:7], v[28:31]\n"
    "v_mfma_f32_16x16x32_bf16 v[56:59], a[32:35], a[8:11], v[56:59]\n"
    "v_mfma_f32_16x16x32_bf16 v[56:59], a[92:95], a[68:71], v[56:59]\n"
    "s_mov_b32 s36, 712704\n"
    "s_add_u32 s50, s61, 98304\n"
    "v_mfma_f32_16x16x32_bf16 v[60:63], a[96:99], a[68:71], v[60:63]\n"
    "buffer_load_dwordx2 a[250:251], v245, s[8:11], s36 offen sc1\n"
    "s_add_u32 m0, s44, 24576\n"
    "v_mfma_f32_16x16x32_bf16 v[60:63], a[36:39], a[8:11], v[60:63]\n"
    "buffer_load_dwordx4 v226, s[40:43], s50 offen lds\n"
    "v_mfma_f32_16x16x32_bf16 v[64:67], a[40:43], a[8:11], v[64:67]\n"
    "v_mfma_f32_16x16x32_bf16 v[64:67], a[100:103], a[68:71], v[64:67]\n"
    "v_mfma_f32_16x16x32_bf16 v[68:71], a[104:107], a[68:71], v[68:71]\n"
    "s_mov_b32 s36, 770048\n"
    "v_mfma_f32_16x16x32_bf16 v[68:71], a[44:47], a[8:11], v[68:71]\n"
    "buffer_load_dwordx2 a[252:253], v245, s[8:11], s36 offen sc1\n"
    "v_mfma_f32_16x16x32_bf16 v[72:75], a[48:51], a[8:11], v[72:75]\n"
    "v_mfma_f32_16x16x32_bf16 v[72:75], a[108:111], a[68:71], v[72:75]\n"
    "s_add_u32 m0, s44, 25600\n"
    "v_mfma_f32_16x16x32_bf16 v[76:79], a[112:115], a[68:71], v[76:79]\n"
    "buffer_load_dwordx4 v227, s[40:43], s50 offen lds\n"
    "v_mfma_f32_16x16x32_bf16 v[76:79], a[52:55], a[8:11], v[76:79]\n"
    "s_mov_b32 s36, 827392\n"
    "v_mfma_f32_16x16x32_bf16 v[80:83], a[56:59], a[8:11], v[80:83]\n"
    "buffer_load_dwordx2 a[254:255], v245, s[8:11], s36 offen sc1\n"
    "v_mfma_f32_16x16x32_bf16 v[80:83], a[116:119], a[68:71], v[80:83]\n"
    "v_mfma_f32_16x16x32_bf16 v[108:111], a[116:119], a[72:75], v[108:111]\n"
    "v_mfma_f32_16x16x32_bf16 v[108:111], a[56:59], a[12:15], v[108:111]\n"
    "v_mfma_f32_16x16x32_bf16 v[104:107], a[52:55], a[12:15], v[104:107]\n"
    "s_mov_b32 s36, 884736\n"
    "v_mfma_f32_16x16x32_bf16 v[104:107], a[112:115], a[72:75], v[104:107]\n"
    "buffer_load_dwordx2 v[252:253], v245, s[8:11], s36 offen sc1\n"
    "v_mfma_f32_16x16x32_bf16 v[100:103], a[108:111], a[72:75], v[100:103]\n"
    "v_mfma_f32_16x16x32_bf16 v[100:103], a[48:51], a[12:15], v[100:103]\n"
    "v_mfma_f32_16x16x32_bf16 v[96:99], a[44:47], a[12:15], v[96:99]\n"
    "v_mfma_f32_16x16x32_bf16 v[96:99], a[104:107], a[72:75], v[96:99]\n"
    "s_mov_b32 s36, 942080\n"
    "v_mfma_f32_16x16x32_bf16 v[92:95], a[100:103], a[72:75], v[92:95]\n"
    "buffer_load_dwordx2 v[254:255], v245, s[8:11], s36 offen sc1\n"
    "v_mfma_f32_16x16x32_bf16 v[92:95], a[40:43], a[12:15], v[92:95]\n"
    "v_mfma_f32_16x16x32_bf16 v[88:91], a[36:39], a[12:15], v[88:91]\n"
    "v_mfma_f32_16x16x32_bf16 v[88:91], a[96:99], a[72:75], v[88:91]\n"
    "v_mfma_f32_16x16x32_bf16 v[84:87], a[92:95], a[72:75], v[84:87]\n"
    "v_mfma_f32_16x16x32_bf16 v[84:87], a[32:35], a[12:15], v[84:87]\n"
    "s_waitcnt vmcnt(16) lgkmcnt(0)\n"
    "s_barrier\n"
    "v_mfma_f32_16x16x32_bf16 v[112:115], a[32:35], a[16:19], v[112:115]\n"
    "ds_read_b64 a[180:181], v229 offset:49152\n"
    "ds_read_b64 a[182:183], v229 offset:49664\n"
    "v_mfma_f32_16x16x32_bf16 v[112:115], a[92:95], a[76:79], v[112:115]\n"
    "s_add_u32 s50, s45, 229376\n"
    "ds_read_b64_tr_b16 a[212:213], v230 offset:46176\n"
    "v_mfma_f32_16x16x32_bf16 v[116:119], a[96:99], a[76:79], v[116:119]\n"
    "s_add_u32 m0, s44, 32768\n"
    "ds_read_b64_tr_b16 a[214:215], v230 offset:49760\n"
    "v_mfma_f32_16x16x32_bf16 v[116:119], a[36:39], a[16:19], v[116:119]\n"
    "buffer_load_dwordx4 v224, s[40:43], s50 offen lds\n"
    "ds_read_b64_tr_b16 a[216:217], v230 offset:46208\n"
    "v_mfma_f32_16x16x32_bf16 v[120:123], a[40:43], a[16:19], v[120:123]\n"
    "ds_read_b64_tr_b16 a[218:219], v230 offset:49792\n"
    "ds_read_b64_tr_b16 a[220:221], v230 offset:46240\n"
    "v_mfma_f32_16x16x32_bf16 v[120:123], a[100:103], a[76:79], v[120:123]\n"
    "ds_read_b64_tr_b16 a[222:223], v230 offset:49824\n"
    "ds_read_b64_tr_b16 a[224:225], v230 offset:53360\n"
    "v_mfma_f32_16x16x32_bf16 v[124:127], a[104:107], a[76:79], v[124:127]\n"
    "ds_read_b64_tr_b16 a[226:227], v230 offset:56944\n"
    "ds_read_b64_tr_b16 a[228:229], v230 offset:53392\n"
    "v_mfma_f32_16x16x32_bf16 v[124:127], a[44:47], a[16:19], v[124:127]\n"
    "ds_read_b64_tr_b16 a[230:231], v230 offset:56976\n"
    "ds_read_b64_tr_b16 a[232:233], v230 offset:53424\n"
    "v_mfma_f32_16x16x32_bf16 v[128:131], a[48:51], a[16:19], v[128:131]\n"
    "ds_read_b64_tr_b16 a[234:235], v230 offset:57008\n"
    "ds_read_b64_tr_b16 a[236:237], v231 offset:46176\n"
    "v_mfma_f32_16x16x32_bf16 v[128:131], a[108:111], a[76:79], v[128:131]\n"
    "s_add_u32 m0, s44, 33792\n"
    "ds_read_b64_tr_b16 a[238:239], v231 offset:49760\n"
    "v_mfma_f32_16x16x32_bf16 v[132:135], a[112:115], a[76:79], v[132:135]\n"
    "buffer_load_dwordx4 v225, s[40:43], s50 offen lds\n"
    "ds_read_b64 a[184:185], v229 offset:50176\n"
    "v_mfma_f32_16x16x32_bf16 v[132:135], a[52:55], a[16:19], v[132:135]\n"
    "ds_read_b64 a[186:187], v229 offset:50688\n"
    "ds_read_b64 a[188:189], v229 offset:51200\n"
    "v_mfma_f32_16x16x32_bf16 v[136:139], a[56:59], a[16:19], v[136:139]\n"
    "ds_read_b64 a[190:191], v229 offset:51712\n"
    "ds_read_b64 a[192:193], v229 offset:52224\n"
    "v_mfma_f32_16x16x32_bf16 v[136:139], a[116:119], a[76:79], v[136:139]\n"
    "ds_read_b64 a[194:195], v229 offset:52736\n"
    "ds_read_b64 a[196:197], v229 offset:53248\n"
    "v_mfma_f32_16x16x32_bf16 v[164:167], a[116:119], a[80:83], v[164:167]\n"
    "ds_read_b64 a[198:199], v229 offset:53760\n"
    "ds_read_b64 a[200:201], v229 offset:54272\n"
    "v_mfma_f32_16x16x32_bf16 v[164:167], a[56:59], a[20:23], v[164:167]\n"
    "s_add_u32 s50, s45, 360448\n"
    "ds_read_b64 a[202:203], v229 offset:54784\n"
    "v_mfma_f32_16x16x32_bf16 v[160:163], a[52:55], a[20:23], v[160:163]\n"
    "s_add_u32 m0, s44, 40960\n"
    "ds_read_b64 a[204:205], v229 offset:55296\n"
    "v_mfma_f32_16x16x32_bf16 v[160:163], a[112:115], a[80:83], v[160:163]\n"
    "buffer_load_dwordx4 v224, s[40:43], s50 offen lds\n"
    "ds_read_b64 a[206:207], v229 offset:55808\n"
    "v_mfma_f32_16x16x32_bf16 v[156:159], a[108:111], a[80:83], v[156:159]\n"
    "ds_read_b64 a[208:209], v229 offset:56320\n"
    "ds_read_b64 a[210:211], v229 offset:56832\n"
    "v_mfma_f32_16x16x32_bf16 v[156:159], a[48:51], a[20:23], v[156:159]\n"
    "v_mfma_f32_16x16x32_bf16 v[152:155], a[44:47], a[20:23], v[152:155]\n"
    "v_mfma_f32_16x16x32_bf16 v[152:155], a[104:107], a[80:83], v[152:155]\n"
    "v_mfma_f32_16x16x32_bf16 v[148:151], a[100:103], a[80:83], v[148:151]\n"
    "v_mfma_f32_16x16x32_bf16 v[148:151], a[40:43], a[20:23], v[148:151]\n"
    "s_add_u32 m0, s44, 41984\n"
    "v_mfma_f32_16x16x32_bf16 v[144:147], a[36:39], a[20:23], v[144:147]\n"
    "buffer_load_dwordx4 v225, s[40:43], s50 offen lds\n"
    "v_mfma_f32_16x16x32_bf16 v[144:147], a[96:99], a[80:83], v[144:147]\n"
    "v_mfma_f32_16x16x32_bf16 v[140:143], a[92:95], a[80:83], v[140:143]\n"
    "v_mfma_f32_16x16x32_bf16 v[140:143], a[32:35], a[20:23], v[140:143]\n"
    "v_mfma_f32_16x16x32_bf16 v[168:171], a[32:35], a[24:27], v[168:171]\n"
    "v_mfma_f32_16x16x32_bf16 v[168:171], a[92:95], a[84:87], v[168:171]\n"
    "v_mfma_f32_16x16x32_bf16 v[172:175], a[96:99], a[84:87], v[172:175]\n"
    "v_mfma_f32_16x16x32_bf16 v[172:175], a[36:39], a[24:27], v[172:175]\n"
    "v_mfma_f32_16x16x32_bf16 v[176:179], a[40:43], a[24:27], v[176:179]\n"
    "s_getpc_b64 s[16:17]\n"
    ".Lh3_pd0:\n"
    "v_mfma_f32_16x16x32_bf16 v[176:179], a[100:103], a[84:87], v[176:179]\n"
    "s_and_b32 s17, s17, 0xffff\n"
    "v_mfma_f32_16x16x32_bf16 v[180:183], a[104:107], a[84:87], v[180:183]\n"
    "s_mov_b32 s18, .Lh3_end0-.Lh3_pd0\n"
    "v_mfma_f32_16x16x32_bf16 v[180:183], a[44:47], a[24:27], v[180:183]\n"
    "v_mfma_f32_16x16x32_bf16 v[184:187], a[48:51], a[24:27], v[184:187]\n"
    "s_mov_b32 s19, 0x20000\n"
    "v_mfma_f32_16x16x32_bf16 v[184:187], a[108:111], a[84:87], v[184:187]\n"
    "s_lshl_b32 s20, s5, 2\n"
    "v_mfma_f32_16x16x32_bf16 v[188:191], a[112:115], a[84:87], v[188:191]\n"
    "v_mfma_f32_16x16x32_bf16 v[188:191], a[52:55], a[24:27], v[188:191]\n"
    "s_add_u32 s20, s20, s37\n"
    "v_mfma_f32_16x16x32_bf16 v[192:195], a[56:59], a[24:27], v[192:195]\n"
    "s_lshl_b32 s20, s20, 8\n"
    "v_mfma_f32_16x16x32_bf16 v[192:195], a[116:119], a[84:87], v[192:195]\n"
    "v_mfma_f32_16x16x32_bf16 v[220:223], a[116:119], a[88:91], v[220:223]\n"
    "v_mbcnt_lo_u32_b32 v234, -1, 0\n"
    "v_mfma_f32_16x16x32_bf16 v[220:223], a[56:59], a[28:31], v[220:223]\n"
    "v_mbcnt_hi_u32_b32 v234, -1, v234\n"
    "v_mfma_f32_16x16x32_bf16 v[216:219], a[52:55], a[28:31], v[216:219]\n"
    "v_mfma_f32_16x16x32_bf16 v[216:219], a[112:115], a[88:91], v[216:219]\n"
    "v_lshrrev_b32 v235, 1, v234\n"
    "v_mfma_f32_16x16x32_bf16 v[212:215], a[108:111], a[88:91], v[212:215]\n"
    "v_min_u32 v235, 1, v235\n"
    "v_mfma_f32_16x16x32_bf16 v[212:215], a[48:51], a[28:31], v[212:215]\n"
    "v_mfma_f32_16x16x32_bf16 v[208:211], a[44:47], a[28:31], v[208:211]\n"
    "v_lshl_add_u32 v234, v234, 7, s20\n"
    "v_mfma_f32_16x16x32_bf16 v[208:211], a[104:107], a[88:91], v[208:211]\n"
    "v_lshl_or_b32 v234, v235, 31, v234\n"
    "v_mfma_f32_16x16x32_bf16 v[204:207], a[100:103], a[88:91], v[204:207]\n"
    "v_mfma_f32_16x16x32_bf16 v[204:207], a[40:43], a[28:31], v[204:207]\n"
    "buffer_load_dword v233, v234, s[16:19], 0 offen\n"
    "v_mfma_f32_16x16x32_bf16 v[200:203], a[36:39], a[28:31], v[200:203]\n"
    "v_mfma_f32_16x16x32_bf16 v[200:203], a[96:99], a[88:91], v[200:203]\n"
    "v_mfma_f32_16x16x32_bf16 v[196:199], a[92:95], a[88:91], v[196:199]\n"
    "v_mfma_f32_16x16x32_bf16 v[196:199], a[32:35], a[28:31], v[196:199]\n"
    "s_waitcnt lgkmcnt(0)\n"
    "s_barrier\n"
    "v_mfma_f32_16x16x32_bf16 v[0:3], a[152:155], a[120:123], v[0:3]\n"
    "v_mfma_f32_16x16x32_bf16 v[0:3], a[212:215], a[180:183], v[0:3]\n"
    "s_add_u32 s50, s60, 294912\n"
    "v_mfma_f32_16x16x32_bf16 v[4:7], a[216:219], a[180:183], v[4:7]\n"
    "s_add_u32 m0, s44, 49152\n"
    "v_mfma_f32_16x16x32_bf16 v[4:7], a[156:159], a[120:123], v[4:7]\n"
    "buffer_load_dwordx4 v226, s[40:43], s50 offen lds\n"
    "v_mfma_f32_16x16x32_bf16 v[8:11], a[160:163], a[120:123], v[8:11]\n"
    "s_waitcnt vmcnt(6) lgkmcnt(0)\n"
    "v_mfma_f32_16x16x32_bf16 v[8:11], a[220:223], a[180:183], v[8:11]\n"
    "v_add_f32 v236, v252, v254\n"
    "v_mfma_f32_16x16x32_bf16 v[12:15], a[224:227], a[180:183], v[12:15]\n"
    "v_min_u32 v237, v253, v255\n"
    "v_mfma_f32_16x16x32_bf16 v[12:15], a[164:167], a[120:123], v[12:15]\n"
    "v_accvgpr_read_b32 v234, a244\n"
    "v_mfma_f32_16x16x32_bf16 v[16:19], a[168:171], a[120:123], v[16:19]\n"
    "v_accvgpr_read_b32 v235, a245\n"
    "v_mfma_f32_16x16x32_bf16 v[16:19], a[228:231], a[180:183], v[16:19]\n"
    "s_add_u32 m0, s44, 50176\n"
    "v_mfma_f32_16x16x32_bf16 v[20:23], a[232:235], a[180:183], v[20:23]\n"
    "v_add_f32 v236, v236, v234\n"
    "buffer_load_dwordx4 v227, s[40:43], s50 offen lds\n"
    "v_mfma_f32_16x16x32_bf16 v[20:23], a[172:175], a[120:123], v[20:23]\n"
    "v_min_u32 v237, v237, v235\n"
    "v_mfma_f32_16x16x32_bf16 v[24:27], a[176:179], a[120:123], v[24:27]\n"
    "v_accvgpr_read_b32 v234, a246\n"
    "v_mfma_f32_16x16x32_bf16 v[24:27], a[236:239], a[180:183], v[24:27]\n"
    "v_mfma_f32_16x16x32_bf16 v[52:55], a[236:239], a[184:187], v[52:55]\n"
    "v_accvgpr_read_b32 v235, a247\n"
    "v_mfma_f32_16x16x32_bf16 v[52:55], a[176:179], a[124:127], v[52:55]\n"
    "v_add_f32 v236, v236, v234\n"
    "s_add_u32 s50, s45, 458752\n"
    "v_mfma_f32_16x16x32_bf16 v[48:51], a[172:175], a[124:127], v[48:51]\n"
    "v_min_u32 v237, v237, v235\n"
    "s_add_u32 m0, s44, 57344\n"
    "v_mfma_f32_16x16x32_bf16 v[48:51], a[232:235], a[184:187], v[48:51]\n"
    "buffer_load_dwordx4 v224, s[40:43], s50 offen lds\n"
    "v_mfma_f32_16x16x32_bf16 v[44:47], a[228:231], a[184:187], v[44:47]\n"
    "v_accvgpr_read_b32 v234, a248\n"
    "v_mfma_f32_16x16x32_bf16 v[44:47], a[168:171], a[124:127], v[44:47]\n"
    "v_accvgpr_read_b32 v235, a249\n"
    "v_mfma_f32_16x16x32_bf16 v[40:43], a[164:167], a[124:127], v[40:43]\n"
    "v_add_f32 v236, v236, v234\n"
    "buffer_load_dwordx2 a[0:1], v241, s[8:11], 0 offen offset:0 sc1\n"
    "v_mfma_f32_16x16x32_bf16 v[40:43], a[224:227], a[184:187], v[40:43]\n"
    "v_min_u32 v237, v237, v235\n"
    "v_mfma_f32_16x16x32_bf16 v[36:39], a[220:223], a[184:187], v[36:39]\n"
    "buffer_load_dwordx2 a[2:3], v241, s[8:11], 0 offen offset:128 sc1\n"
    "v_mfma_f32_16x16x32_bf16 v[36:39], a[160:163], a[124:127], v[36:39]\n"
    "v_accvgpr_read_b32 v234, a250\n"
    "s_add_u32 m0, s44, 58368\n"
    "v_mfma_f32_16x16x32_bf16 v[32:35], a[156:159], a[124:127], v[32:35]\n"
    "v_accvgpr_read_b32 v235, a251\n"
    "buffer_load_dwordx2 a[4:5], v241, s[8:11], 0 offen offset:256 sc1\n"
    "buffer_load_dwordx4 v225, s[40:43], s50 offen lds\n"
    "v_mfma_f32_16x16x32_bf16 v[32:35], a[216:219], a[184:187], v[32:35]\n"
    "v_add_f32 v236, v236, v234\n"
    "v_mfma_f32_16x16x32_bf16 v[28:31], a[212:215], a[184:187], v[28:31]\n"
    "buffer_load_dwordx2 a[6:7], v241, s[8:11], 0 offen offset:384 sc1\n"
    "v_mfma_f32_16x16x32_bf16 v[28:31], a[152:155], a[124:127], v[28:31]\n"
    "v_min_u32 v237, v237, v235\n"
    "v_mfma_f32_16x16x32_bf16 v[56:59], a[152:155], a[128:131], v[56:59]\n"
    "v_accvgpr_read_b32 v234, a252\n"
    "buffer_load_dwordx2 a[8:9], v241, s[8:11], 0 offen offset:512 sc1\n"
    "v_mfma_f32_16x16x32_bf16 v[56:59], a[212:215], a[188:191], v[56:59]\n"
    "v_accvgpr_read_b32 v235, a253\n"
    "v_mfma_f32_16x16x32_bf16 v[60:63], a[216:219], a[188:191], v[60:63]\n"
    "buffer_load_dwordx2 a[10:11], v241, s[8:11], 0 offen offset:640 sc1\n"
    "v_mfma_f32_16x16x32_bf16 v[60:63], a[156:159], a[128:131], v[60:63]\n"
    "v_add_f32 v236, v236, v234\n"
    "v_mfma_f32_16x16x32_bf16 v[64:67], a[160:163], a[128:131], v[64:67]\n"
    "v_min_u32 v237, v237, v235\n"
    "buffer_load_dwordx2 a[12:13], v241, s[8:11], 0 offen offset:768 sc1\n"
    "v_mfma_f32_16x16x32_bf16 v[64:67], a[220:223], a[188:191], v[64:67]\n"
    "v_accvgpr_read_b32 v234, a254\n"
    "v_mfma_f32_16x16x32_bf16 v[68:71], a[224:227], a[188:191], v[68:71]\n"
    "v_accvgpr_read_b32 v235, a255\n"
    "buffer_load_dwordx2 a[14:15], v241, s[8:11], 0 offen offset:896 sc1\n"
    "v_mfma_f32_16x16x32_bf16 v[68:71], a[164:167], a[128:131], v[68:71]\n"
    "v_mfma_f32_16x16x32_bf16 v[72:75], a[168:171], a[128:131], v[72:75]\n"
    "v_add_f32 v236, v236, v234\n"
    "v_mfma_f32_16x16x32_bf16 v[72:75], a[228:231], a[188:191], v[72:75]\n"
    "v_min_u32 v237, v237, v235\n"
    "v_mfma_f32_16x16x32_bf16 v[76:79], a[232:235], a[188:191], v[76:79]\n"
    "v_cmp_eq_u32 vcc, 0, v237\n"
    "v_mfma_f32_16x16x32_bf16 v[76:79], a[172:175], a[128:131], v[76:79]\n"
    "v_mfma_f32_16x16x32_bf16 v[80:83], a[176:179], a[128:131], v[80:83]\n"
    "v_mfma_f32_16x16x32_bf16 v[80:83], a[236:239], a[188:191], v[80:83]\n"
    "v_mfma_f32_16x16x32_bf16 v[108:111], a[236:239], a[192:195], v[108:111]\n"
    "v_mfma_f32_16x16x32_bf16 v[108:111], a[176:179], a[132:135], v[108:111]\n"
    "v_mfma_f32_16x16x32_bf16 v[104:107], a[172:175], a[132:135], v[104:107]\n"
    "v_mfma_f32_16x16x32_bf16 v[104:107], a[232:235], a[192:195], v[104:107]\n"
    "v_mfma_f32_16x16x32_bf16 v[100:103], a[228:231], a[192:195], v[100:103]\n"
    "s_cbranch_vccnz G1_CSR%=\n"
    "G1_CSO%=:\n"
    "v_mfma_f32_16x16x32_bf16 v[100:103], a[168:171], a[132:135], v[100:103]\n"
    "v_mfma_f32_16x16x32_bf16 v[96:99], a[164:167], a[132:135], v[96:99]\n"
    "ds_write_b32 v250, v236\n"
    "v_mfma_f32_16x16x32_bf16 v[96:99], a[224:227], a[192:195], v[96:99]\n"
    "v_mfma_f32_16x16x32_bf16 v[92:95], a[220:223], a[192:195], v[92:95]\n"
    "v_mfma_f32_16x16x32_bf16 v[92:95], a[160:163], a[132:135], v[92:95]\n"
    "v_mfma_f32_16x16x32_bf16 v[88:91], a[156:159], a[132:135], v[88:91]\n"
    "v_mfma_f32_16x16x32_bf16 v[88:91], a[216:219], a[192:195], v[88:91]\n"
    "v_mfma_f32_16x16x32_bf16 v[84:87], a[212:215], a[192:195], v[84:87]\n"
    "v_mfma_f32_16x16x32_bf16 v[84:87], a[152:155], a[132:135], v[84:87]\n"
    "s_waitcnt lgkmcnt(0)\n"
    "s_barrier\n"
    "v_mfma_f32_16x16x32_bf16 v[112:115], a[152:155], a[136:139], v[112:115]\n"
    "v_mfma_f32_16x16x32_bf16 v[112:115], a[212:215], a[196:199], v[112:115]\n"
    "v_mfma_f32_16x16x32_bf16 v[116:119], a[216:219], a[196:199], v[116:119]\n"
    "v_mfma_f32_16x16x32_bf16 v[116:119], a[156:159], a[136:139], v[116:119]\n"
    "v_mfma_f32_16x16x32_bf16 v[120:123], a[160:163], a[136:139], v[120:123]\n"
    "v_mfma_f32_16x16x32_bf16 v[120:123], a[220:223], a[196:199], v[120:123]\n"
    "v_mfma_f32_16x16x32_bf16 v[124:127], a[224:227], a[196:199], v[124:127]\n"
    "v_mfma_f32_16x16x32_bf16 v[124:127], a[164:167], a[136:139], v[124:127]\n"
    "v_mfma_f32_16x16x32_bf16 v[128:131], a[168:171], a[136:139], v[128:131]\n"
    "v_mfma_f32_16x16x32_bf16 v[128:131], a[228:231], a[196:199], v[128:131]\n"
    "v_mfma_f32_16x16x32_bf16 v[132:135], a[232:235], a[196:199], v[132:135]\n"
    "ds_read_b128 v[248:251], v244\n"
    "v_mfma_f32_16x16x32_bf16 v[132:135], a[172:175], a[136:139], v[132:135]\n"
    "ds_read_b128 v[252:255], v244 offset:448\n"
    "v_mfma_f32_16x16x32_bf16 v[136:139], a[176:179], a[136:139], v[136:139]\n"
    "v_mfma_f32_16x16x32_bf16 v[136:139], a[236:239], a[196:199], v[136:139]\n"
    "v_mfma_f32_16x16x32_bf16 v[164:167], a[236:239], a[200:203], v[164:167]\n"
    "v_mfma_f32_16x16x32_bf16 v[164:167], a[176:179], a[140:143], v[164:167]\n"
    "v_mfma_f32_16x16x32_bf16 v[160:163], a[172:175], a[140:143], v[160:163]\n"
    "v_mfma_f32_16x16x32_bf16 v[160:163], a[232:235], a[200:203], v[160:163]\n"
    "v_mfma_f32_16x16x32_bf16 v[156:159], a[228:231], a[200:203], v[156:159]\n"
    "v_mfma_f32_16x16x32_bf16 v[156:159], a[168:171], a[140:143], v[156:159]\n"
    "v_mfma_f32_16x16x32_bf16 v[152:155], a[164:167], a[140:143], v[152:155]\n"
    "v_mfma_f32_16x16x32_bf16 v[152:155], a[224:227], a[200:203], v[152:155]\n"
    "v_mfma_f32_16x16x32_bf16 v[148:151], a[220:223], a[200:203], v[148:151]\n"
    "v_mfma_f32_16x16x32_bf16 v[148:151], a[160:163], a[140:143], v[148:151]\n"
    "v_mfma_f32_16x16x32_bf16 v[144:147], a[156:159], a[140:143], v[144:147]\n"
    "v_mfma_f32_16x16x32_bf16 v[144:147], a[216:219], a[200:203], v[144:147]\n"
    "v_mfma_f32_16x16x32_bf16 v[140:143], a[212:215], a[200:203], v[140:143]\n"
    "v_mfma_f32_16x16x32_bf16 v[140:143], a[152:155], a[140:143], v[140:143]\n"
    "v_mfma_f32_16x16x32_bf16 v[168:171], a[152:155], a[144:147], v[168:171]\n"
    "v_mfma_f32_16x16x32_bf16 v[168:171], a[212:215], a[204:207], v[168:171]\n"
    "v_mfma_f32_16x16x32_bf16 v[172:175], a[216:219], a[204:207], v[172:175]\n"
    "v_mfma_f32_16x16x32_bf16 v[172:175], a[156:159], a[144:147], v[172:175]\n"
    "v_mfma_f32_16x16x32_bf16 v[176:179], a[160:163], a[144:147], v[176:179]\n"
    "v_mfma_f32_16x16x32_bf16 v[176:179], a[220:223], a[204:207], v[176:179]\n"
    "v_mfma_f32_16x16x32_bf16 v[180:183], a[224:227], a[204:207], v[180:183]\n"
    "v_mfma_f32_16x16x32_bf16 v[180:183], a[164:167], a[144:147], v[180:183]\n"
    "v_mfma_f32_16x16x32_bf16 v[184:187], a[168:171], a[144:147], v[184:187]\n"
    "v_mfma_f32_16x16x32_bf16 v[184:187], a[228:231], a[204:207], v[184:187]\n"
    "v_mfma_f32_16x16x32_bf16 v[188:191], a[232:235], a[204:207], v[188:191]\n"
    "v_mfma_f32_16x16x32_bf16 v[188:191], a[172:175], a[144:147], v[188:191]\n"
    "v_mfma_f32_16x16x32_bf16 v[192:195], a[176:179], a[144:147], v[192:195]\n"
    "v_mfma_f32_16x16x32_bf16 v[192:195], a[236:239], a[204:207], v[192:195]\n"
    "v_mfma_f32_16x16x32_bf16 v[220:223], a[236:239], a[208:211], v[220:223]\n"
    "v_mfma_f32_16x16x32_bf16 v[220:223], a[176:179], a[148:151], v[220:223]\n"
    "v_mfma_f32_16x16x32_bf16 v[216:219], a[172:175], a[148:151], v[216:219]\n"
    "v_mfma_f32_16x16x32_bf16 v[216:219], a[232:235], a[208:211], v[216:219]\n"
    "v_mfma_f32_16x16x32_bf16 v[212:215], a[228:231], a[208:211], v[212:215]\n"
    "v_mfma_f32_16x16x32_bf16 v[212:215], a[168:171], a[148:151], v[212:215]\n"
    "v_mfma_f32_16x16x32_bf16 v[208:211], a[164:167], a[148:151], v[208:211]\n"
    "v_mfma_f32_16x16x32_bf16 v[208:211], a[224:227], a[208:211], v[208:211]\n"
    "v_mfma_f32_16x16x32_bf16 v[204:207], a[220:223], a[208:211], v[204:207]\n"
    "v_mfma_f32_16x16x32_bf16 v[204:207], a[160:163], a[148:151], v[204:207]\n"
    "v_mfma_f32_16x16x32_bf16 v[200:203], a[156:159], a[148:151], v[200:203]\n"
    "v_mfma_f32_16x16x32_bf16 v[200:203], a[216:219], a[208:211], v[200:203]\n"
    "v_mfma_f32_16x16x32_bf16 v[196:199], a[212:215], a[208:211], v[196:199]\n"
    "v_mfma_f32_16x16x32_bf16 v[196:199], a[152:155], a[148:151], v[196:199]\n"
    "s_mov_b32 s35, 0\n"
    "G1_RETRY%=:\n"
    "s_waitcnt vmcnt(0)\n"
    "v_accvgpr_read_b32 v224, a0\n"
    "v_accvgpr_read_b32 v225, a1\n"
    "v_accvgpr_read_b32 v226, a2\n"
    "v_accvgpr_read_b32 v227, a3\n"
    "v_accvgpr_read_b32 v228, a4\n"
    "v_accvgpr_read_b32 v229, a5\n"
    "v_accvgpr_read_b32 v230, a6\n"
    "v_accvgpr_read_b32 v231, a7\n"
    "v_accvgpr_read_b32 v232, a8\n"
    "v_accvgpr_read_b32 v233, a9\n"
    "v_accvgpr_read_b32 v234, a10\n"
    "v_accvgpr_read_b32 v235, a11\n"
    "v_accvgpr_read_b32 v236, a12\n"
    "v_accvgpr_read_b32 v237, a13\n"
    "v_accvgpr_read_b32 v238, a14\n"
    "v_accvgpr_read_b32 v239, a15\n"
    "v_min_u32 v240, v225, v227\n"
    "v_min3_u32 v240, v240, v229, v231\n"
    "v_min3_u32 v240, v240, v233, v235\n"
    "v_min3_u32 v240, v240, v237, v239\n"
    "v_cmp_eq_u32 vcc, 0, v240\n"
    "s_add_u32 s35, s35, 1\n"
    "s_nop 3\n"
    "s_cbranch_vccz G1_OK%=\n"
    "s_cmp_gt_u32 s35, 0x40000\n"
    "s_cbranch_scc1 G1_OK%=\n"
    "s_sleep 2\n"
    "buffer_load_dwordx2 a[0:1], v241, s[8:11], 0 offen offset:0 sc1\n"
    "buffer_load_dwordx2 a[2:3], v241, s[8:11], 0 offen offset:128 sc1\n"
    "buffer_load_dwordx2 a[4:5], v241, s[8:11], 0 offen offset:256 sc1\n"
    "S2_H4%=:\n"
    "s_cbranch_execz S2_R%=\n"
    "buffer_load_dwordx2 a[6:7], v241, s[8:11], 0 offen offset:384 sc1\n"
    "buffer_load_dwordx2 a[8:9], v241, s[8:11], 0 offen offset:512 sc1\n"
    "buffer_load_dwordx2 a[10:11], v241, s[8:11], 0 offen offset:640 sc1\n"
    "buffer_load_dwordx2 a[12:13], v241, s[8:11], 0 offen offset:768 sc1\n"
    "buffer_load_dwordx2 a[14:15], v241, s[8:11], 0 offen offset:896 sc1\n"
    "s_branch G1_RETRY%=\n"
    "G1_OK%=:\n"
    "s_mov_b32 s38, -1\n"
    "s_mov_b32 s39, 0\n"
    "s_waitcnt lgkmcnt(0)\n"
    "v_rsq_f32 v243, v225\n"
    "s_mov_b32 s36, 14680064\n"
    "v_mul_f32 v243, 0xbfb8aa3b, v243\n"
    "v_pk_fma_f32 v[8:9], v[224:225], v[248:249], v[8:9] op_sel:[0,0,0] op_sel_hi:[0,1,1] neg_lo:[1,0,0] neg_hi:[1,0,0]\n"
    "v_pk_fma_f32 v[10:11], v[224:225], v[250:251], v[10:11] op_sel:[0,0,0] op_sel_hi:[0,1,1] neg_lo:[1,0,0] neg_hi:[1,0,0]\n"
    "v_pk_mul_f32 v[244:245], v[8:9], v[242:243] op_sel:[0,1] op_sel_hi:[1,1]\n"
    "v_pk_mul_f32 v[246:247], v[10:11], v[242:243] op_sel:[0,1] op_sel_hi:[1,1]\n"
    "v_pk_fma_f32 v[20:21], v[224:225], v[252:253], v[20:21] op_sel:[0,0,0] op_sel_hi:[0,1,1] neg_lo:[1,0,0] neg_hi:[1,0,0]\n"
    "v_pk_fma_f32 v[22:23], v[224:225], v[254:255], v[22:23] op_sel:[0,0,0] op_sel_hi:[0,1,1] neg_lo:[1,0,0] neg_hi:[1,0,0]\n"
    "v_exp_f32 v244, v244\n"
    "v_exp_f32 v245, v245\n"
    "v_exp_f32 v246, v246\n"
    "v_exp_f32 v247, v247\n"
    "v_pk_mul_f32 v[8:9], v[8:9], v[20:21]\n"
    "v_pk_mul_f32 v[10:11], v[10:11], v[22:23]\n"
    "v_pk_fma_f32 v[244:245], v[244:245], v[224:225], v[224:225] op_sel:[0,1,1] op_sel_hi:[1,1,1]\n"
    "v_pk_fma_f32 v[246:247], v[246:247], v[224:225], v[224:225] op_sel:[0,1,1] op_sel_hi:[1,1,1]\n"
    "v_rcp_f32 v244, v244\n"
    "v_rcp_f32 v245, v245\n"
    "v_rcp_f32 v246, v246\n"
    "v_rcp_f32 v247, v247\n"
    "v_pk_mul_f32 v[8:9], v[8:9], v[244:245]\n"
    "v_pk_mul_f32 v[10:11], v[10:11], v[246:247]\n"
    "v_cvt_pk_bf16_f32 v8, v8, v9\n"
    "v_cvt_pk_bf16_f32 v9, v10, v11\n"
    "v_rsq_f32 v20, v227\n"
    "s_mov_b32 s36, 14680064\n"
    "v_mul_f32 v20, 0xbfb8aa3b, v20\n"
    "v_pk_fma_f32 v[36:37], v[226:227], v[248:249], v[36:37] op_sel:[0,0,0] op_sel_hi:[0,1,1] neg_lo:[1,0,0] neg_hi:[1,0,0]\n"
    "v_pk_fma_f32 v[38:39], v[226:227], v[250:251], v[38:39] op_sel:[0,0,0] op_sel_hi:[0,1,1] neg_lo:[1,0,0] neg_hi:[1,0,0]\n"
    "v_pk_mul_f32 v[244:245], v[36:37], v[20:21] op_sel:[0,0] op_sel_hi:[1,0]\n"
    "v_pk_mul_f32 v[246:247], v[38:39], v[20:21] op_sel:[0,0] op_sel_hi:[1,0]\n"
    "v_pk_fma_f32 v[48:49], v[226:227], v[252:253], v[48:49] op_sel:[0,0,0] op_sel_hi:[0,1,1] neg_lo:[1,0,0] neg_hi:[1,0,0]\n"
    "v_pk_fma_f32 v[50:51], v[226:227], v[254:255], v[50:51] op_sel:[0,0,0] op_sel_hi:[0,1,1] neg_lo:[1,0,0] neg_hi:[1,0,0]\n"
    "v_exp_f32 v244, v244\n"
    "v_exp_f32 v245, v245\n"
    "v_exp_f32 v246, v246\n"
    "v_exp_f32 v247, v247\n"
    "v_pk_mul_f32 v[36:37], v[36:37], v[48:49]\n"
    "v_pk_mul_f32 v[38:39], v[38:39], v[50:51]\n"
    "v_pk_fma_f32 v[244:245], v[244:245], v[226:227], v[226:227] op_sel:[0,1,1] op_sel_hi:[1,1,1]\n"
    "v_pk_fma_f32 v[246:247], v[246:247], v[226:227], v[226:227] op_sel:[0,1,1] op_sel_hi:[1,1,1]\n"
    "v_rcp_f32 v244, v244\n"
    "v_rcp_f32 v245, v245\n"
    "v_rcp_f32 v246, v246\n"
    "v_rcp_f32 v247, v247\n"
    "v_pk_mul_f32 v[36:37], v[36:37], v[244:245]\n"
    "v_pk_mul_f32 v[38:39], v[38:39], v[246:247]\n"
    "v_cvt_pk_bf16_f32 v10, v36, v37\n"
    "v_cvt_pk_bf16_f32 v11, v38, v39\n"
    "s_nop 0\n"
    "v_permlane16_swap_b32 v8, v10\n"
    "v_permlane16_swap_b32 v9, v11\n"
    "buffer_store_dwordx4 v[8:11], v242, s[8:11], s36 offen sc1\n"
    "v_accvgpr_read_b32 v21, a240\n"
    "v_accvgpr_read_b32 v22, a241\n"
    "v_accvgpr_read_b32 v23, a242\n"
    "v_accvgpr_read_b32 v49, a243\n"
    "v_rsq_f32 v48, v229\n"
    "s_mov_b32 s36, 14909440\n"
    "v_mul_f32 v48, 0xbfb8aa3b, v48\n"
    "v_pk_fma_f32 v[64:65], v[228:229], v[248:249], v[64:65] op_sel:[0,0,0] op_sel_hi:[0,1,1] neg_lo:[1,0,0] neg_hi:[1,0,0]\n"
    "v_pk_fma_f32 v[66:67], v[228:229], v[250:251], v[66:67] op_sel:[0,0,0] op_sel_hi:[0,1,1] neg_lo:[1,0,0] neg_hi:[1,0,0]\n"
    "v_pk_mul_f32 v[244:245], v[64:65], v[48:49] op_sel:[0,0] op_sel_hi:[1,0]\n"
    "v_pk_mul_f32 v[246:247], v[66:67], v[48:49] op_sel:[0,0] op_sel_hi:[1,0]\n"
    "v_pk_fma_f32 v[76:77], v[228:229], v[252:253], v[76:77] op_sel:[0,0,0] op_sel_hi:[0,1,1] neg_lo:[1,0,0] neg_hi:[1,0,0]\n"
    "v_pk_fma_f32 v[78:79], v[228:229], v[254:255], v[78:79] op_sel:[0,0,0] op_sel_hi:[0,1,1] neg_lo:[1,0,0] neg_hi:[1,0,0]\n"
    "v_exp_f32 v244, v244\n"
    "v_exp_f32 v245, v245\n"
    "v_exp_f32 v246, v246\n"
    "v_exp_f32 v247, v247\n"
    "v_pk_mul_f32 v[64:65], v[64:65], v[76:77]\n"
    "v_pk_mul_f32 v[66:67], v[66:67], v[78:79]\n"
    "v_pk_fma_f32 v[244:245], v[244:245], v[228:229], v[228:229] op_sel:[0,1,1] op_sel_hi:[1,1,1]\n"
    "v_pk_fma_f32 v[246:247], v[246:247], v[228:229], v[228:229] op_sel:[0,1,1] op_sel_hi:[1,1,1]\n"
    "v_rcp_f32 v244, v244\n"
    "v_rcp_f32 v245, v245\n"
    "v_rcp_f32 v246, v246\n"
    "v_rcp_f32 v247, v247\n"
    "v_pk_mul_f32 v[64:65], v[64:65], v[244:245]\n"
    "v_pk_mul_f32 v[66:67], v[66:67], v[246:247]\n"
    "v_cvt_pk_bf16_f32 v64, v64, v65\n"
    "v_cvt_pk_bf16_f32 v65, v66, v67\n"
    "v_rsq_f32 v76, v231\n"
    "s_mov_b32 s36, 14909440\n"
    "v_mul_f32 v76, 0xbfb8aa3b, v76\n"
    "v_pk_fma_f32 v[92:93], v[230:231], v[248:249], v[92:93] op_sel:[0,0,0] op_sel_hi:[0,1,1] neg_lo:[1,0,0] neg_hi:[1,0,0]\n"
    "v_pk_fma_f32 v[94:95], v[230:231], v[250:251], v[94:95] op_sel:[0,0,0] op_sel_hi:[0,1,1] neg_lo:[1,0,0] neg_hi:[1,0,0]\n"
    "v_pk_mul_f32 v[244:245], v[92:93], v[76:77] op_sel:[0,0] op_sel_hi:[1,0]\n"
    "v_pk_mul_f32 v[246:247], v[94:95], v[76:77] op_sel:[0,0] op_sel_hi:[1,0]\n"
    "v_pk_fma_f32 v[104:105], v[230:231], v[252:253], v[104:105] op_sel:[0,0,0] op_sel_hi:[0,1,1] neg_lo:[1,0,0] neg_hi:[1,0,0]\n"
    "v_pk_fma_f32 v[106:107], v[230:231], v[254:255], v[106:107] op_sel:[0,0,0] op_sel_hi:[0,1,1] neg_lo:[1,0,0] neg_hi:[1,0,0]\n"
    "v_exp_f32 v244, v244\n"
    "v_exp_f32 v245, v245\n"
    "v_exp_f32 v246, v246\n"
    "v_exp_f32 v247, v247\n"
    "v_pk_mul_f32 v[92:93], v[92:93], v[104:105]\n"
    "v_pk_mul_f32 v[94:95], v[94:95], v[106:107]\n"
    "v_pk_fma_f32 v[244:245], v[244:245], v[230:231], v[230:231] op_sel:[0,1,1] op_sel_hi:[1,1,1]\n"
    "v_pk_fma_f32 v[246:247], v[246:247], v[230:231], v[230:231] op_sel:[0,1,1] op_sel_hi:[1,1,1]\n"
    "v_rcp_f32 v244, v244\n"
    "v_rcp_f32 v245, v245\n"
    "v_rcp_f32 v246, v246\n"
    "v_rcp_f32 v247, v247\n"
    "v_pk_mul_f32 v[92:93], v[92:93], v[244:245]\n"
    "v_pk_mul_f32 v[94:95], v[94:95], v[246:247]\n"
    "v_cvt_pk_bf16_f32 v66, v92, v93\n"
    "v_cvt_pk_bf16_f32 v67, v94, v95\n"
    "s_nop 0\n"
    "v_permlane16_swap_b32 v64, v66\n"
    "v_permlane16_swap_b32 v65, v67\n"
    "buffer_store_dwordx4 v[64:67], v242, s[8:11], s36 offen sc1\n"
    "ds_read_b128 v[8:11], v23\n"
    "ds_read_b128 v[36:39], v23 offset:448\n"
    "v_rsq_f32 v104, v233\n"
    "s_mov_b32 s36, 15138816\n"
    "v_mul_f32 v104, 0xbfb8aa3b, v104\n"
    "v_pk_fma_f32 v[120:121], v[232:233], v[248:249], v[120:121] op_sel:[0,0,0] op_sel_hi:[0,1,1] neg_lo:[1,0,0] neg_hi:[1,0,0]\n"
    "v_pk_fma_f32 v[122:123], v[232:233], v[250:251], v[122:123] op_sel:[0,0,0] op_sel_hi:[0,1,1] neg_lo:[1,0,0] neg_hi:[1,0,0]\n"
    "v_pk_mul_f32 v[244:245], v[120:121], v[104:105] op_sel:[0,0] op_sel_hi:[1,0]\n"
    "v_pk_mul_f32 v[246:247], v[122:123], v[104:105] op_sel:[0,0] op_sel_hi:[1,0]\n"
    "v_pk_fma_f32 v[132:133], v[232:233], v[252:253], v[132:133] op_sel:[0,0,0] op_sel_hi:[0,1,1] neg_lo:[1,0,0] neg_hi:[1,0,0]\n"
    "v_pk_fma_f32 v[134:135], v[232:233], v[254:255], v[134:135] op_sel:[0,0,0] op_sel_hi:[0,1,1] neg_lo:[1,0,0] neg_hi:[1,0,0]\n"
    "v_exp_f32 v244, v244\n"
    "v_exp_f32 v245, v245\n"
    "v_exp_f32 v246, v246\n"
    "v_exp_f32 v247, v247\n"
    "v_pk_mul_f32 v[120:121], v[120:121], v[132:133]\n"
    "v_pk_mul_f32 v[122:123], v[122:123], v[134:135]\n"
    "v_pk_fma_f32 v[244:245], v[244:245], v[232:233], v[232:233] op_sel:[0,1,1] op_sel_hi:[1,1,1]\n"
    "v_pk_fma_f32 v[246:247], v[246:247], v[232:233], v[232:233] op_sel:[0,1,1] op_sel_hi:[1,1,1]\n"
    "v_rcp_f32 v244, v244\n"
    "v_rcp_f32 v245, v245\n"
    "v_rcp_f32 v246, v246\n"
    "v_rcp_f32 v247, v247\n"
    "v_pk_mul_f32 v[120:121], v[120:121], v[244:245]\n"
    "v_pk_mul_f32 v[122:123], v[122:123], v[246:247]\n"
    "v_cvt_pk_bf16_f32 v120, v120, v121\n"
    "v_cvt_pk_bf16_f32 v121, v122, v123\n"
    "v_rsq_f32 v132, v235\n"
    "s_mov_b32 s36, 15138816\n"
    "v_mul_f32 v132, 0xbfb8aa3b, v132\n"
    "v_pk_fma_f32 v[148:149], v[234:235], v[248:249], v[148:149] op_sel:[0,0,0] op_sel_hi:[0,1,1] neg_lo:[1,0,0] neg_hi:[1,0,0]\n"
    "v_pk_fma_f32 v[150:151], v[234:235], v[250:251], v[150:151] op_sel:[0,0,0] op_sel_hi:[0,1,1] neg_lo:[1,0,0] neg_hi:[1,0,0]\n"
    "v_pk_mul_f32 v[244:245], v[148:149], v[132:133] op_sel:[0,0] op_sel_hi:[1,0]\n"
    "v_pk_mul_f32 v[246:247], v[150:151], v[132:133] op_sel:[0,0] op_sel_hi:[1,0]\n"
    "v_pk_fma_f32 v[160:161], v[234:235], v[252:253], v[160:161] op_sel:[0,0,0] op_sel_hi:[0,1,1] neg_lo:[1,0,0] neg_hi:[1,0,0]\n"
    "v_pk_fma_f32 v[162:163], v[234:235], v[254:255], v[162:163] op_sel:[0,0,0] op_sel_hi:[0,1,1] neg_lo:[1,0,0] neg_hi:[1,0,0]\n"
    "v_exp_f32 v244, v244\n"
    "v_exp_f32 v245, v245\n"
    "v_exp_f32 v246, v246\n"
    "v_exp_f32 v247, v247\n"
    "v_pk_mul_f32 v[148:149], v[148:149], v[160:161]\n"
    "v_pk_mul_f32 v[150:151], v[150:151], v[162:163]\n"
    "v_pk_fma_f32 v[244:245], v[244:245], v[234:235], v[234:235] op_sel:[0,1,1] op_sel_hi:[1,1,1]\n"
    "v_pk_fma_f32 v[246:247], v[246:247], v[234:235], v[234:235] op_sel:[0,1,1] op_sel_hi:[1,1,1]\n"
    "v_rcp_f32 v244, v244\n"
    "v_rcp_f32 v245, v245\n"
    "v_rcp_f32 v246, v246\n"
    "v_rcp_f32 v247, v247\n"
    "v_pk_mul_f32 v[148:149], v[148:149], v[244:245]\n"
    "v_pk_mul_f32 v[150:151], v[150:151], v[246:247]\n"
    "v_cvt_pk_bf16_f32 v122, v148, v149\n"
    "v_cvt_pk_bf16_f32 v123, v150, v151\n"
    "s_nop 0\n"
    "v_permlane16_swap_b32 v120, v122\n"
    "v_permlane16_swap_b32 v121, v123\n"
    "buffer_store_dwordx4 v[120:123], v242, s[8:11], s36 offen sc1\n"
    "v_rsq_f32 v160, v237\n"
    "s_mov_b32 s36, 15368192\n"
    "v_mul_f32 v160, 0xbfb8aa3b, v160\n"
    "v_pk_fma_f32 v[176:177], v[236:237], v[248:249], v[176:177] op_sel:[0,0,0] op_sel_hi:[0,1,1] neg_lo:[1,0,0] neg_hi:[1,0,0]\n"
    "v_pk_fma_f32 v[178:179], v[236:237], v[250:251], v[178:179] op_sel:[0,0,0] op_sel_hi:[0,1,1] neg_lo:[1,0,0] neg_hi:[1,0,0]\n"
    "v_pk_mul_f32 v[244:245], v[176:177], v[160:161] op_sel:[0,0] op_sel_hi:[1,0]\n"
    "v_pk_mul_f32 v[246:247], v[178:179], v[160:161] op_sel:[0,0] op_sel_hi:[1,0]\n"
    "v_pk_fma_f32 v[188:189], v[236:237], v[252:253], v[188:189] op_sel:[0,0,0] op_sel_hi:[0,1,1] neg_lo:[1,0,0] neg_hi:[1,0,0]\n"
    "v_pk_fma_f32 v[190:191], v[236:237], v[254:255], v[190:191] op_sel:[0,0,0] op_sel_hi:[0,1,1] neg_lo:[1,0,0] neg_hi:[1,0,0]\n"
    "v_exp_f32 v244, v244\n"
    "v_exp_f32 v245, v245\n"
    "v_exp_f32 v246, v246\n"
    "v_exp_f32 v247, v247\n"
    "v_pk_mul_f32 v[176:177], v[176:177], v[188:189]\n"
    "v_pk_mul_f32 v[178:179], v[178:179], v[190:191]\n"
    "v_pk_fma_f32 v[244:245], v[244:245], v[236:237], v[236:237] op_sel:[0,1,1] op_sel_hi:[1,1,1]\n"
    "v_pk_fma_f32 v[246:247], v[246:247], v[236:237], v[236:237] op_sel:[0,1,1] op_sel_hi:[1,1,1]\n"
    "v_rcp_f32 v244, v244\n"
    "v_rcp_f32 v245, v245\n"
    "v_rcp_f32 v246, v246\n"
    "v_rcp_f32 v247, v247\n"
    "v_pk_mul_f32 v[176:177], v[176:177], v[244:245]\n"
    "v_pk_mul_f32 v[178:179], v[178:179], v[246:247]\n"
    "v_cvt_pk_bf16_f32 v176, v176, v177\n"
    "v_cvt_pk_bf16_f32 v177, v178, v179\n"
    "v_rsq_f32 v188, v239\n"
    "s_mov_b32 s36, 15368192\n"
    "v_mul_f32 v188, 0xbfb8aa3b, v188\n"
    "v_pk_fma_f32 v[204:205], v[238:239], v[248:249], v[204:205] op_sel:[0,0,0] op_sel_hi:[0,1,1] neg_lo:[1,0,0] neg_hi:[1,0,0]\n"
    "v_pk_fma_f32 v[206:207], v[238:239], v[250:251], v[206:207] op_sel:[0,0,0] op_sel_hi:[0,1,1] neg_lo:[1,0,0] neg_hi:[1,0,0]\n"
    "v_pk_mul_f32 v[244:245], v[204:205], v[188:189] op_sel:[0,0] op_sel_hi:[1,0]\n"
    "v_pk_mul_f32 v[246:247], v[206:207], v[188:189] op_sel:[0,0] op_sel_hi:[1,0]\n"
    "v_pk_fma_f32 v[216:217], v[238:239], v[252:253], v[216:217] op_sel:[0,0,0] op_sel_hi:[0,1,1] neg_lo:[1,0,0] neg_hi:[1,0,0]\n"
    "v_pk_fma_f32 v[218:219], v[238:239], v[254:255], v[218:219] op_sel:[0,0,0] op_sel_hi:[0,1,1] neg_lo:[1,0,0] neg_hi:[1,0,0]\n"
    "v_exp_f32 v244, v244\n"
    "v_exp_f32 v245, v245\n"
    "v_exp_f32 v246, v246\n"
    "v_exp_f32 v247, v247\n"
    "v_pk_mul_f32 v[204:205], v[204:205], v[216:217]\n"
    "v_pk_mul_f32 v[206:207], v[206:207], v[218:219]\n"
    "v_pk_fma_f32 v[244:245], v[244:245], v[238:239], v[238:239] op_sel:[0,1,1] op_sel_hi:[1,1,1]\n"
    "v_pk_fma_f32 v[246:247], v[246:247], v[238:239], v[238:239] op_sel:[0,1,1] op_sel_hi:[1,1,1]\n"
    "v_rcp_f32 v244, v244\n"
    "v_rcp_f32 v245, v245\n"
    "v_rcp_f32 v246, v246\n"
    "v_rcp_f32 v247, v247\n"
    "v_pk_mul_f32 v[204:205], v[204:205], v[244:245]\n"
    "v_pk_mul_f32 v[206:207], v[206:207], v[246:247]\n"
    "v_cvt_pk_bf16_f32 v178, v204, v205\n"
    "v_cvt_pk_bf16_f32 v179, v206, v207\n"
    "s_nop 0\n"
    "v_permlane16_swap_b32 v176, v178\n"
    "v_permlane16_swap_b32 v177, v179\n"
    "buffer_store_dwordx4 v[176:179], v242, s[8:11], s36 offen sc1\n"
    "ds_read_b128 v[64:67], v49 offset:0\n"
    "ds_read_b128 v[92:95], v49 offset:64\n"
    "ds_read_b128 v[120:123], v49 offset:448\n"
    "ds_read_b128 v[148:151], v49 offset:512\n"
    "v_permlane32_swap_b32 v24, v52\n"
    "v_permlane32_swap_b32 v25, v53\n"
    "v_permlane32_swap_b32 v26, v54\n"
    "v_permlane32_swap_b32 v27, v55\n"
    "v_permlane32_swap_b32 v80, v108\n"
    "v_permlane32_swap_b32 v81, v109\n"
    "v_permlane32_swap_b32 v82, v110\n"
    "v_permlane32_swap_b32 v83, v111\n"
    "v_permlane32_swap_b32 v136, v164\n"
    "v_permlane32_swap_b32 v137, v165\n"
    "v_permlane32_swap_b32 v138, v166\n"
    "v_permlane32_swap_b32 v139, v167\n"
    "v_permlane32_swap_b32 v192, v220\n"
    "v_permlane32_swap_b32 v193, v221\n"
    "v_permlane32_swap_b32 v194, v222\n"
    "v_permlane32_swap_b32 v195, v223\n"
    "s_waitcnt lgkmcnt(4)\n"
    "v_cndmask_b32 v216, v226, v224, s[38:39]\n"
    "v_cndmask_b32 v217, v227, v225, s[38:39]\n"
    "v_cndmask_b32 v218, v20, v243, s[38:39]\n"
    "s_mov_b32 s36, 14680064\n"
    "v_pk_fma_f32 v[24:25], v[216:217], v[8:9], v[24:25] op_sel:[0,0,0] op_sel_hi:[0,1,1] neg_lo:[1,0,0] neg_hi:[1,0,0]\n"
    "v_pk_fma_f32 v[26:27], v[216:217], v[10:11], v[26:27] op_sel:[0,0,0] op_sel_hi:[0,1,1] neg_lo:[1,0,0] neg_hi:[1,0,0]\n"
    "v_pk_mul_f32 v[244:245], v[24:25], v[218:219] op_sel:[0,0] op_sel_hi:[1,0]\n"
    "v_pk_mul_f32 v[246:247], v[26:27], v[218:219] op_sel:[0,0] op_sel_hi:[1,0]\n"
    "v_pk_fma_f32 v[52:53], v[216:217], v[36:37], v[52:53] op_sel:[0,0,0] op_sel_hi:[0,1,1] neg_lo:[1,0,0] neg_hi:[1,0,0]\n"
    "v_pk_fma_f32 v[54:55], v[216:217], v[38:39], v[54:55] op_sel:[0,0,0] op_sel_hi:[0,1,1] neg_lo:[1,0,0] neg_hi:[1,0,0]\n"
    "v_exp_f32 v244, v244\n"
    "v_exp_f32 v245, v245\n"
    "v_exp_f32 v246, v246\n"
    "v_exp_f32 v247, v247\n"
    "v_pk_mul_f32 v[24:25], v[24:25], v[52:53]\n"
    "v_pk_mul_f32 v[26:27], v[26:27], v[54:55]\n"
    "v_pk_fma_f32 v[244:245], v[244:245], v[216:217], v[216:217] op_sel:[0,1,1] op_sel_hi:[1,1,1]\n"
    "v_pk_fma_f32 v[246:247], v[246:247], v[216:217], v[216:217] op_sel:[0,1,1] op_sel_hi:[1,1,1]\n"
    "v_rcp_f32 v244, v244\n"
    "v_rcp_f32 v245, v245\n"
    "v_rcp_f32 v246, v246\n"
    "v_rcp_f32 v247, v247\n"
    "v_pk_mul_f32 v[24:25], v[24:25], v[244:245]\n"
    "v_pk_mul_f32 v[26:27], v[26:27], v[246:247]\n"
    "v_cvt_pk_bf16_f32 v24, v24, v25\n"
    "v_cvt_pk_bf16_f32 v25, v26, v27\n"
    "v_cndmask_b32 v216, v230, v228, s[38:39]\n"
    "v_cndmask_b32 v217, v231, v229, s[38:39]\n"
    "v_cndmask_b32 v218, v76, v48, s[38:39]\n"
    "s_mov_b32 s36, 14680064\n"
    "v_pk_fma_f32 v[80:81], v[216:217], v[8:9], v[80:81] op_sel:[0,0,0] op_sel_hi:[0,1,1] neg_lo:[1,0,0] neg_hi:[1,0,0]\n"
    "v_pk_fma_f32 v[82:83], v[216:217], v[10:11], v[82:83] op_sel:[0,0,0] op_sel_hi:[0,1,1] neg_lo:[1,0,0] neg_hi:[1,0,0]\n"
    "v_pk_mul_f32 v[244:245], v[80:81], v[218:219] op_sel:[0,0] op_sel_hi:[1,0]\n"
    "v_pk_mul_f32 v[246:247], v[82:83], v[218:219] op_sel:[0,0] op_sel_hi:[1,0]\n"
    "v_pk_fma_f32 v[108:109], v[216:217], v[36:37], v[108:109] op_sel:[0,0,0] op_sel_hi:[0,1,1] neg_lo:[1,0,0] neg_hi:[1,0,0]\n"
    "v_pk_fma_f32 v[110:111], v[216:217], v[38:39], v[110:111] op_sel:[0,0,0] op_sel_hi:[0,1,1] neg_lo:[1,0,0] neg_hi:[1,0,0]\n"
    "v_exp_f32 v244, v244\n"
    "v_exp_f32 v245, v245\n"
    "v_exp_f32 v246, v246\n"
    "v_exp_f32 v247, v247\n"
    "v_pk_mul_f32 v[80:81], v[80:81], v[108:109]\n"
    "v_pk_mul_f32 v[82:83], v[82:83], v[110:111]\n"
    "v_pk_fma_f32 v[244:245], v[244:245], v[216:217], v[216:217] op_sel:[0,1,1] op_sel_hi:[1,1,1]\n"
    "v_pk_fma_f32 v[246:247], v[246:247], v[216:217], v[216:217] op_sel:[0,1,1] op_sel_hi:[1,1,1]\n"
    "v_rcp_f32 v244, v244\n"
    "v_rcp_f32 v245, v245\n"
    "v_rcp_f32 v246, v246\n"
    "v_rcp_f32 v247, v247\n"
    "v_pk_mul_f32 v[80:81], v[80:81], v[244:245]\n"
    "v_pk_mul_f32 v[82:83], v[82:83], v[246:247]\n"
    "v_cvt_pk_bf16_f32 v26, v80, v81\n"
    "v_cvt_pk_bf16_f32 v27, v82, v83\n"
    "s_nop 0\n"
    "v_permlane16_swap_b32 v24, v26\n"
    "v_permlane16_swap_b32 v25, v27\n"
    "buffer_store_dwordx4 v[24:27], v22, s[8:11], s36 offen sc1\n"
    "v_cndmask_b32 v216, v234, v232, s[38:39]\n"
    "v_cndmask_b32 v217, v235, v233, s[38:39]\n"
    "v_cndmask_b32 v218, v132, v104, s[38:39]\n"
    "s_mov_b32 s36, 15138816\n"
    "v_pk_fma_f32 v[136:137], v[216:217], v[8:9], v[136:137] op_sel:[0,0,0] op_sel_hi:[0,1,1] neg_lo:[1,0,0] neg_hi:[1,0,0]\n"
    "v_pk_fma_f32 v[138:139], v[216:217], v[10:11], v[138:139] op_sel:[0,0,0] op_sel_hi:[0,1,1] neg_lo:[1,0,0] neg_hi:[1,0,0]\n"
    "v_pk_mul_f32 v[244:245], v[136:137], v[218:219] op_sel:[0,0] op_sel_hi:[1,0]\n"
    "v_pk_mul_f32 v[246:247], v[138:139], v[218:219] op_sel:[0,0] op_sel_hi:[1,0]\n"
    "v_pk_fma_f32 v[164:165], v[216:217], v[36:37], v[164:165] op_sel:[0,0,0] op_sel_hi:[0,1,1] neg_lo:[1,0,0] neg_hi:[1,0,0]\n"
    "v_pk_fma_f32 v[166:167], v[216:217], v[38:39], v[166:167] op_sel:[0,0,0] op_sel_hi:[0,1,1] neg_lo:[1,0,0] neg_hi:[1,0,0]\n"
    "v_exp_f32 v244, v244\n"
    "v_exp_f32 v245, v245\n"
    "v_exp_f32 v246, v246\n"
    "v_exp_f32 v247, v247\n"
    "v_pk_mul_f32 v[136:137], v[136:137], v[164:165]\n"
    "v_pk_mul_f32 v[138:139], v[138:139], v[166:167]\n"
    "v_pk_fma_f32 v[244:245], v[244:245], v[216:217], v[216:217] op_sel:[0,1,1] op_sel_hi:[1,1,1]\n"
    "v_pk_fma_f32 v[246:247], v[246:247], v[216:217], v[216:217] op_sel:[0,1,1] op_sel_hi:[1,1,1]\n"
    "v_rcp_f32 v244, v244\n"
    "v_rcp_f32 v245, v245\n"
    "v_rcp_f32 v246, v246\n"
    "v_rcp_f32 v247, v247\n"
    "v_pk_mul_f32 v[136:137], v[136:137], v[244:245]\n"
    "v_pk_mul_f32 v[138:139], v[138:139], v[246:247]\n"
    "v_cvt_pk_bf16_f32 v136, v136, v137\n"
    "v_cvt_pk_bf16_f32 v137, v138, v139\n"
    "v_cndmask_b32 v216, v238, v236, s[38:39]\n"
    "v_cndmask_b32 v217, v239, v237, s[38:39]\n"
    "v_cndmask_b32 v218, v188, v160, s[38:39]\n"
    "s_mov_b32 s36, 15138816\n"
    "v_pk_fma_f32 v[192:193], v[216:217], v[8:9], v[192:193] op_sel:[0,0,0] op_sel_hi:[0,1,1] neg_lo:[1,0,0] neg_hi:[1,0,0]\n"
    "v_pk_fma_f32 v[194:195], v[216:217], v[10:11], v[194:195] op_sel:[0,0,0] op_sel_hi:[0,1,1] neg_lo:[1,0,0] neg_hi:[1,0,0]\n"
    "v_pk_mul_f32 v[244:245], v[192:193], v[218:219] op_sel:[0,0] op_sel_hi:[1,0]\n"
    "v_pk_mul_f32 v[246:247], v[194:195], v[218:219] op_sel:[0,0] op_sel_hi:[1,0]\n"
    "v_pk_fma_f32 v[220:221], v[216:217], v[36:37], v[220:221] op_sel:[0,0,0] op_sel_hi:[0,1,1] neg_lo:[1,0,0] neg_hi:[1,0,0]\n"
    "v_pk_fma_f32 v[222:223], v[216:217], v[38:39], v[222:223] op_sel:[0,0,0] op_sel_hi:[0,1,1] neg_lo:[1,0,0] neg_hi:[1,0,0]\n"
    "v_exp_f32 v244, v244\n"
    "v_exp_f32 v245, v245\n"
    "v_exp_f32 v246, v246\n"
    "v_exp_f32 v247, v247\n"
    "v_pk_mul_f32 v[192:193], v[192:193], v[220:221]\n"
    "v_pk_mul_f32 v[194:195], v[194:195], v[222:223]\n"
    "v_pk_fma_f32 v[244:245], v[244:245], v[216:217], v[216:217] op_sel:[0,1,1] op_sel_hi:[1,1,1]\n"
    "v_pk_fma_f32 v[246:247], v[246:247], v[216:217], v[216:217] op_sel:[0,1,1] op_sel_hi:[1,1,1]\n"
    "v_rcp_f32 v244, v244\n"
    "v_rcp_f32 v245, v245\n"
    "v_rcp_f32 v246, v246\n"
    "v_rcp_f32 v247, v247\n"
    "v_pk_mul_f32 v[192:193], v[192:193], v[244:245]\n"
    "v_pk_mul_f32 v[194:195], v[194:195], v[246:247]\n"
    "v_cvt_pk_bf16_f32 v138, v192, v193\n"
    "v_cvt_pk_bf16_f32 v139, v194, v195\n"
    "s_nop 0\n"
    "v_permlane16_swap_b32 v136, v138\n"
    "v_permlane16_swap_b32 v137, v139\n"
    "buffer_store_dwordx4 v[136:139], v22, s[8:11], s36 offen sc1\n"
    "s_waitcnt lgkmcnt(0)\n"
    "s_mov_b32 s36, 14680064\n"
    "v_pk_fma_f32 v[0:1], v[224:225], v[64:65], v[0:1] op_sel:[0,0,0] op_sel_hi:[0,1,1] neg_lo:[1,0,0] neg_hi:[1,0,0]\n"
    "v_pk_fma_f32 v[2:3], v[224:225], v[66:67], v[2:3] op_sel:[0,0,0] op_sel_hi:[0,1,1] neg_lo:[1,0,0] neg_hi:[1,0,0]\n"
    "v_pk_mul_f32 v[244:245], v[0:1], v[242:243] op_sel:[0,1] op_sel_hi:[1,1]\n"
    "v_pk_mul_f32 v[246:247], v[2:3], v[242:243] op_sel:[0,1] op_sel_hi:[1,1]\n"
    "v_pk_fma_f32 v[12:13], v[224:225], v[120:121], v[12:13] op_sel:[0,0,0] op_sel_hi:[0,1,1] neg_lo:[1,0,0] neg_hi:[1,0,0]\n"
    "v_pk_fma_f32 v[14:15], v[224:225], v[122:123], v[14:15] op_sel:[0,0,0] op_sel_hi:[0,1,1] neg_lo:[1,0,0] neg_hi:[1,0,0]\n"
    "v_exp_f32 v244, v244\n"
    "v_exp_f32 v245, v245\n"
    "v_exp_f32 v246, v246\n"
    "v_exp_f32 v247, v247\n"
    "v_pk_mul_f32 v[0:1], v[0:1], v[12:13]\n"
    "v_pk_mul_f32 v[2:3], v[2:3], v[14:15]\n"
    "v_pk_fma_f32 v[244:245], v[244:245], v[224:225], v[224:225] op_sel:[0,1,1] op_sel_hi:[1,1,1]\n"
    "v_pk_fma_f32 v[246:247], v[246:247], v[224:225], v[224:225] op_sel:[0,1,1] op_sel_hi:[1,1,1]\n"
    "v_rcp_f32 v244, v244\n"
    "v_rcp_f32 v245, v245\n"
    "v_rcp_f32 v246, v246\n"
    "v_rcp_f32 v247, v247\n"
    "v_pk_mul_f32 v[0:1], v[0:1], v[244:245]\n"
    "v_pk_mul_f32 v[2:3], v[2:3], v[246:247]\n"
    "v_pk_fma_f32 v[4:5], v[224:225], v[92:93], v[4:5] op_sel:[0,0,0] op_sel_hi:[0,1,1] neg_lo:[1,0,0] neg_hi:[1,0,0]\n"
    "v_pk_fma_f32 v[6:7], v[224:225], v[94:95], v[6:7] op_sel:[0,0,0] op_sel_hi:[0,1,1] neg_lo:[1,0,0] neg_hi:[1,0,0]\n"
    "v_pk_mul_f32 v[244:245], v[4:5], v[242:243] op_sel:[0,1] op_sel_hi:[1,1]\n"
    "v_pk_mul_f32 v[246:247], v[6:7], v[242:243] op_sel:[0,1] op_sel_hi:[1,1]\n"
    "v_pk_fma_f32 v[16:17], v[224:225], v[148:149], v[16:17] op_sel:[0,0,0] op_sel_hi:[0,1,1] neg_lo:[1,0,0] neg_hi:[1,0,0]\n"
    "v_pk_fma_f32 v[18:19], v[224:225], v[150:151], v[18:19] op_sel:[0,0,0] op_sel_hi:[0,1,1] neg_lo:[1,0,0] neg_hi:[1,0,0]\n"
    "v_exp_f32 v244, v244\n"
    "v_exp_f32 v245, v245\n"
    "v_exp_f32 v246, v246\n"
    "v_exp_f32 v247, v247\n"
    "v_pk_mul_f32 v[4:5], v[4:5], v[16:17]\n"
    "v_pk_mul_f32 v[6:7], v[6:7], v[18:19]\n"
    "v_pk_fma_f32 v[244:245], v[244:245], v[224:225], v[224:225] op_sel:[0,1,1] op_sel_hi:[1,1,1]\n"
    "v_pk_fma_f32 v[246:247], v[246:247], v[224:225], v[224:225] op_sel:[0,1,1] op_sel_hi:[1,1,1]\n"
    "v_rcp_f32 v244, v244\n"
    "v_rcp_f32 v245, v245\n"
    "v_rcp_f32 v246, v246\n"
    "v_rcp_f32 v247, v247\n"
    "v_pk_mul_f32 v[4:5], v[4:5], v[244:245]\n"
    "v_pk_mul_f32 v[6:7], v[6:7], v[246:247]\n"
    "v_cvt_pk_bf16_f32 v0, v0, v1\n"
    "v_cvt_pk_bf16_f32 v1, v2, v3\n"
    "v_cvt_pk_bf16_f32 v2, v4, v5\n"
    "v_cvt_pk_bf16_f32 v3, v6, v7\n"
    "buffer_store_dwordx4 v[0:3], v21, s[8:11], s36 offen sc1\n"
    "s_mov_b32 s36, 14794752\n"
    "v_pk_fma_f32 v[28:29], v[226:227], v[64:65], v[28:29] op_sel:[0,0,0] op_sel_hi:[0,1,1] neg_lo:[1,0,0] neg_hi:[1,0,0]\n"
    "v_pk_fma_f32 v[30:31], v[226:227], v[66:67], v[30:31] op_sel:[0,0,0] op_sel_hi:[0,1,1] neg_lo:[1,0,0] neg_hi:[1,0,0]\n"
    "v_pk_mul_f32 v[244:245], v[28:29], v[20:21] op_sel:[0,0] op_sel_hi:[1,0]\n"
    "v_pk_mul_f32 v[246:247], v[30:31], v[20:21] op_sel:[0,0] op_sel_hi:[1,0]\n"
    "v_pk_fma_f32 v[40:41], v[226:227], v[120:121], v[40:41] op_sel:[0,0,0] op_sel_hi:[0,1,1] neg_lo:[1,0,0] neg_hi:[1,0,0]\n"
    "v_pk_fma_f32 v[42:43], v[226:227], v[122:123], v[42:43] op_sel:[0,0,0] op_sel_hi:[0,1,1] neg_lo:[1,0,0] neg_hi:[1,0,0]\n"
    "v_exp_f32 v244, v244\n"
    "v_exp_f32 v245, v245\n"
    "v_exp_f32 v246, v246\n"
    "v_exp_f32 v247, v247\n"
    "v_pk_mul_f32 v[28:29], v[28:29], v[40:41]\n"
    "v_pk_mul_f32 v[30:31], v[30:31], v[42:43]\n"
    "v_pk_fma_f32 v[244:245], v[244:245], v[226:227], v[226:227] op_sel:[0,1,1] op_sel_hi:[1,1,1]\n"
    "v_pk_fma_f32 v[246:247], v[246:247], v[226:227], v[226:227] op_sel:[0,1,1] op_sel_hi:[1,1,1]\n"
    "v_rcp_f32 v244, v244\n"
    "v_rcp_f32 v245, v245\n"
    "v_rcp_f32 v246, v246\n"
    "v_rcp_f32 v247, v247\n"
    "v_pk_mul_f32 v[28:29], v[28:29], v[244:245]\n"
    "v_pk_mul_f32 v[30:31], v[30:31], v[246:247]\n"
    "v_pk_fma_f32 v[32:33], v[226:227], v[92:93], v[32:33] op_sel:[0,0,0] op_sel_hi:[0,1,1] neg_lo:[1,0,0] neg_hi:[1,0,0]\n"
    "v_pk_fma_f32 v[34:35], v[226:227], v[94:95], v[34:35] op_sel:[0,0,0] op_sel_hi:[0,1,1] neg_lo:[1,0,0] neg_hi:[1,0,0]\n"
    "v_pk_mul_f32 v[244:245], v[32:33], v[20:21] op_sel:[0,0] op_sel_hi:[1,0]\n"
    "v_pk_mul_f32 v[246:247], v[34:35], v[20:21] op_sel:[0,0] op_sel_hi:[1,0]\n"
    "v_pk_fma_f32 v[44:45], v[226:227], v[148:149], v[44:45] op_sel:[0,0,0] op_sel_hi:[0,1,1] neg_lo:[1,0,0] neg_hi:[1,0,0]\n"
    "v_pk_fma_f32 v[46:47], v[226:227], v[150:151], v[46:47] op_sel:[0,0,0] op_sel_hi:[0,1,1] neg_lo:[1,0,0] neg_hi:[1,0,0]\n"
    "v_exp_f32 v244, v244\n"
    "v_exp_f32 v245, v245\n"
    "v_exp_f32 v246, v246\n"
    "v_exp_f32 v247, v247\n"
    "v_pk_mul_f32 v[32:33], v[32:33], v[44:45]\n"
    "v_pk_mul_f32 v[34:35], v[34:35], v[46:47]\n"
    "v_pk_fma_f32 v[244:245], v[244:245], v[226:227], v[226:227] op_sel:[0,1,1] op_sel_hi:[1,1,1]\n"
    "v_pk_fma_f32 v[246:247], v[246:247], v[226:227], v[226:227] op_sel:[0,1,1] op_sel_hi:[1,1,1]\n"
    "v_rcp_f32 v244, v244\n"
    "v_rcp_f32 v245, v245\n"
    "v_rcp_f32 v246, v246\n"
    "v_rcp_f32 v247, v247\n"
    "v_pk_mul_f32 v[32:33], v[32:33], v[244:245]\n"
    "v_pk_mul_f32 v[34:35], v[34:35], v[246:247]\n"
    "v_cvt_pk_bf16_f32 v28, v28, v29\n"
    "v_cvt_pk_bf16_f32 v29, v30, v31\n"
    "v_cvt_pk_bf16_f32 v30, v32, v33\n"
    "v_cvt_pk_bf16_f32 v31, v34, v35\n"
    "buffer_store_dwordx4 v[28:31], v21, s[8:11], s36 offen sc1\n"
    "s_mov_b32 s36, 14909440\n"
    "v_pk_fma_f32 v[56:57], v[228:229], v[64:65], v[56:57] op_sel:[0,0,0] op_sel_hi:[0,1,1] neg_lo:[1,0,0] neg_hi:[1,0,0]\n"
    "v_pk_fma_f32 v[58:59], v[228:229], v[66:67], v[58:59] op_sel:[0,0,0] op_sel_hi:[0,1,1] neg_lo:[1,0,0] neg_hi:[1,0,0]\n"
    "v_pk_mul_f32 v[244:245], v[56:57], v[48:49] op_sel:[0,0] op_sel_hi:[1,0]\n"
    "v_pk_mul_f32 v[246:247], v[58:59], v[48:49] op_sel:[0,0] op_sel_hi:[1,0]\n"
    "v_pk_fma_f32 v[68:69], v[228:229], v[120:121], v[68:69] op_sel:[0,0,0] op_sel_hi:[0,1,1] neg_lo:[1,0,0] neg_hi:[1,0,0]\n"
    "v_pk_fma_f32 v[70:71], v[228:229], v[122:123], v[70:71] op_sel:[0,0,0] op_sel_hi:[0,1,1] neg_lo:[1,0,0] neg_hi:[1,0,0]\n"
    "v_exp_f32 v244, v244\n"
    "v_exp_f32 v245, v245\n"
    "v_exp_f32 v246, v246\n"
    "v_exp_f32 v247, v247\n"
    "v_pk_mul_f32 v[56:57], v[56:57], v[68:69]\n"
    "v_pk_mul_f32 v[58:59], v[58:59], v[70:71]\n"
    "v_pk_fma_f32 v[244:245], v[244:245], v[228:229], v[228:229] op_sel:[0,1,1] op_sel_hi:[1,1,1]\n"
    "v_pk_fma_f32 v[246:247], v[246:247], v[228:229], v[228:229] op_sel:[0,1,1] op_sel_hi:[1,1,1]\n"
    "v_rcp_f32 v244, v244\n"
    "v_rcp_f32 v245, v245\n"
    "v_rcp_f32 v246, v246\n"
    "v_rcp_f32 v247, v247\n"
    "v_pk_mul_f32 v[56:57], v[56:57], v[244:245]\n"
    "v_pk_mul_f32 v[58:59], v[58:59], v[246:247]\n"
    "v_pk_fma_f32 v[60:61], v[228:229], v[92:93], v[60:61] op_sel:[0,0,0] op_sel_hi:[0,1,1] neg_lo:[1,0,0] neg_hi:[1,0,0]\n"
    "v_pk_fma_f32 v[62:63], v[228:229], v[94:95], v[62:63] op_sel:[0,0,0] op_sel_hi:[0,1,1] neg_lo:[1,0,0] neg_hi:[1,0,0]\n"
    "v_pk_mul_f32 v[244:245], v[60:61], v[48:49] op_sel:[0,0] op_sel_hi:[1,0]\n"
    "v_pk_mul_f32 v[246:247], v[62:63], v[48:49] op_sel:[0,0] op_sel_hi:[1,0]\n"
    "v_pk_fma_f32 v[72:73], v[228:229], v[148:149], v[72:73] op_sel:[0,0,0] op_sel_hi:[0,1,1] neg_lo:[1,0,0] neg_hi:[1,0,0]\n"
    "v_pk_fma_f32 v[74:75], v[228:229], v[150:151], v[74:75] op_sel:[0,0,0] op_sel_hi:[0,1,1] neg_lo:[1,0,0] neg_hi:[1,0,0]\n"
    "v_exp_f32 v244, v244\n"
    "v_exp_f32 v245, v245\n"
    "v_exp_f32 v246, v246\n"
    "v_exp_f32 v247, v247\n"
    "v_pk_mul_f32 v[60:61], v[60:61], v[72:73]\n"
    "v_pk_mul_f32 v[62:63], v[62:63], v[74:75]\n"
    "v_pk_fma_f32 v[244:245], v[244:245], v[228:229], v[228:229] op_sel:[0,1,1] op_sel_hi:[1,1,1]\n"
    "v_pk_fma_f32 v[246:247], v[246:247], v[228:229], v[228:229] op_sel:[0,1,1] op_sel_hi:[1,1,1]\n"
    "v_rcp_f32 v244, v244\n"
    "v_rcp_f32 v245, v245\n"
    "v_rcp_f32 v246, v246\n"
    "v_rcp_f32 v247, v247\n"
    "v_pk_mul_f32 v[60:61], v[60:61], v[244:245]\n"
    "v_pk_mul_f32 v[62:63], v[62:63], v[246:247]\n"
    "v_cvt_pk_bf16_f32 v56, v56, v57\n"
    "v_cvt_pk_bf16_f32 v57, v58, v59\n"
    "v_cvt_pk_bf16_f32 v58, v60, v61\n"
    "v_cvt_pk_bf16_f32 v59, v62, v63\n"
    "buffer_store_dwordx4 v[56:59], v21, s[8:11], s36 offen sc1\n"
    "s_mov_b32 s36, 15024128\n"
    "v_pk_fma_f32 v[84:85], v[230:231], v[64:65], v[84:85] op_sel:[0,0,0] op_sel_hi:[0,1,1] neg_lo:[1,0,0] neg_hi:[1,0,0]\n"
    "v_pk_fma_f32 v[86:87], v[230:231], v[66:67], v[86:87] op_sel:[0,0,0] op_sel_hi:[0,1,1] neg_lo:[1,0,0] neg_hi:[1,0,0]\n"
    "v_pk_mul_f32 v[244:245], v[84:85], v[76:77] op_sel:[0,0] op_sel_hi:[1,0]\n"
    "v_pk_mul_f32 v[246:247], v[86:87], v[76:77] op_sel:[0,0] op_sel_hi:[1,0]\n"
    "v_pk_fma_f32 v[96:97], v[230:231], v[120:121], v[96:97] op_sel:[0,0,0] op_sel_hi:[0,1,1] neg_lo:[1,0,0] neg_hi:[1,0,0]\n"
    "v_pk_fma_f32 v[98:99], v[230:231], v[122:123], v[98:99] op_sel:[0,0,0] op_sel_hi:[0,1,1] neg_lo:[1,0,0] neg_hi:[1,0,0]\n"
    "v_exp_f32 v244, v244\n"
    "v_exp_f32 v245, v245\n"
    "v_exp_f32 v246, v246\n"
    "v_exp_f32 v247, v247\n"
    "v_pk_mul_f32 v[84:85], v[84:85], v[96:97]\n"
    "v_pk_mul_f32 v[86:87], v[86:87], v[98:99]\n"
    "v_pk_fma_f32 v[244:245], v[244:245], v[230:231], v[230:231] op_sel:[0,1,1] op_sel_hi:[1,1,1]\n"
    "v_pk_fma_f32 v[246:247], v[246:247], v[230:231], v[230:231] op_sel:[0,1,1] op_sel_hi:[1,1,1]\n"
    "v_rcp_f32 v244, v244\n"
    "v_rcp_f32 v245, v245\n"
    "v_rcp_f32 v246, v246\n"
    "v_rcp_f32 v247, v247\n"
    "v_pk_mul_f32 v[84:85], v[84:85], v[244:245]\n"
    "v_pk_mul_f32 v[86:87], v[86:87], v[246:247]\n"
    "v_pk_fma_f32 v[88:89], v[230:231], v[92:93], v[88:89] op_sel:[0,0,0] op_sel_hi:[0,1,1] neg_lo:[1,0,0] neg_hi:[1,0,0]\n"
    "v_pk_fma_f32 v[90:91], v[230:231], v[94:95], v[90:91] op_sel:[0,0,0] op_sel_hi:[0,1,1] neg_lo:[1,0,0] neg_hi:[1,0,0]\n"
    "v_pk_mul_f32 v[244:245], v[88:89], v[76:77] op_sel:[0,0] op_sel_hi:[1,0]\n"
    "v_pk_mul_f32 v[246:247], v[90:91], v[76:77] op_sel:[0,0] op_sel_hi:[1,0]\n"
    "v_pk_fma_f32 v[100:101], v[230:231], v[148:149], v[100:101] op_sel:[0,0,0] op_sel_hi:[0,1,1] neg_lo:[1,0,0] neg_hi:[1,0,0]\n"
    "v_pk_fma_f32 v[102:103], v[230:231], v[150:151], v[102:103] op_sel:[0,0,0] op_sel_hi:[0,1,1] neg_lo:[1,0,0] neg_hi:[1,0,0]\n"
    "v_exp_f32 v244, v244\n"
    "v_exp_f32 v245, v245\n"
    "v_exp_f32 v246, v246\n"
    "v_exp_f32 v247, v247\n"
    "v_pk_mul_f32 v[88:89], v[88:89], v[100:101]\n"
    "v_pk_mul_f32 v[90:91], v[90:91], v[102:103]\n"
    "v_pk_fma_f32 v[244:245], v[244:245], v[230:231], v[230:231] op_sel:[0,1,1] op_sel_hi:[1,1,1]\n"
    "v_pk_fma_f32 v[246:247], v[246:247], v[230:231], v[230:231] op_sel:[0,1,1] op_sel_hi:[1,1,1]\n"
    "v_rcp_f32 v244, v244\n"
    "v_rcp_f32 v245, v245\n"
    "v_rcp_f32 v246, v246\n"
    "v_rcp_f32 v247, v247\n"
    "v_pk_mul_f32 v[88:89], v[88:89], v[244:245]\n"
    "v_pk_mul_f32 v[90:91], v[90:91], v[246:247]\n"
    "v_cvt_pk_bf16_f32 v84, v84, v85\n"
    "v_cvt_pk_bf16_f32 v85, v86, v87\n"
    "v_cvt_pk_bf16_f32 v86, v88, v89\n"
    "v_cvt_pk_bf16_f32 v87, v90, v91\n"
    "buffer_store_dwordx4 v[84:87], v21, s[8:11], s36 offen sc1\n"
    "s_mov_b32 s36, 15138816\n"
    "v_pk_fma_f32 v[112:113], v[232:233], v[64:65], v[112:113] op_sel:[0,0,0] op_sel_hi:[0,1,1] neg_lo:[1,0,0] neg_hi:[1,0,0]\n"
    "v_pk_fma_f32 v[114:115], v[232:233], v[66:67], v[114:115] op_sel:[0,0,0] op_sel_hi:[0,1,1] neg_lo:[1,0,0] neg_hi:[1,0,0]\n"
    "v_pk_mul_f32 v[244:245], v[112:113], v[104:105] op_sel:[0,0] op_sel_hi:[1,0]\n"
    "v_pk_mul_f32 v[246:247], v[114:115], v[104:105] op_sel:[0,0] op_sel_hi:[1,0]\n"
    "v_pk_fma_f32 v[124:125], v[232:233], v[120:121], v[124:125] op_sel:[0,0,0] op_sel_hi:[0,1,1] neg_lo:[1,0,0] neg_hi:[1,0,0]\n"
    "v_pk_fma_f32 v[126:127], v[232:233], v[122:123], v[126:127] op_sel:[0,0,0] op_sel_hi:[0,1,1] neg_lo:[1,0,0] neg_hi:[1,0,0]\n"
    "v_exp_f32 v244, v244\n"
    "v_exp_f32 v245, v245\n"
    "v_exp_f32 v246, v246\n"
    "v_exp_f32 v247, v247\n"
    "v_pk_mul_f32 v[112:113], v[112:113], v[124:125]\n"
    "v_pk_mul_f32 v[114:115], v[114:115], v[126:127]\n"
    "v_pk_fma_f32 v[244:245], v[244:245], v[232:233], v[232:233] op_sel:[0,1,1] op_sel_hi:[1,1,1]\n"
    "v_pk_fma_f32 v[246:247], v[246:247], v[232:233], v[232:233] op_sel:[0,1,1] op_sel_hi:[1,1,1]\n"
    "v_rcp_f32 v244, v244\n"
    "v_rcp_f32 v245, v245\n"
    "v_rcp_f32 v246, v246\n"
    "v_rcp_f32 v247, v247\n"
    "v_pk_mul_f32 v[112:113], v[112:113], v[244:245]\n"
    "v_pk_mul_f32 v[114:115], v[114:115], v[246:247]\n"
    "v_pk_fma_f32 v[116:117], v[232:233], v[92:93], v[116:117] op_sel:[0,0,0] op_sel_hi:[0,1,1] neg_lo:[1,0,0] neg_hi:[1,0,0]\n"
    "v_pk_fma_f32 v[118:119], v[232:233], v[94:95], v[118:119] op_sel:[0,0,0] op_sel_hi:[0,1,1] neg_lo:[1,0,0] neg_hi:[1,0,0]\n"
    "v_pk_mul_f32 v[244:245], v[116:117], v[104:105] op_sel:[0,0] op_sel_hi:[1,0]\n"
    "v_pk_mul_f32 v[246:247], v[118:119], v[104:105] op_sel:[0,0] op_sel_hi:[1,0]\n"
    "v_pk_fma_f32 v[128:129], v[232:233], v[148:149], v[128:129] op_sel:[0,0,0] op_sel_hi:[0,1,1] neg_lo:[1,0,0] neg_hi:[1,0,0]\n"
    "v_pk_fma_f32 v[130:131], v[232:233], v[150:151], v[130:131] op_sel:[0,0,0] op_sel_hi:[0,1,1] neg_lo:[1,0,0] neg_hi:[1,0,0]\n"
    "v_exp_f32 v244, v244\n"
    "v_exp_f32 v245, v245\n"
    "v_exp_f32 v246, v246\n"
    "v_exp_f32 v247, v247\n"
    "v_pk_mul_f32 v[116:117], v[116:117], v[128:129]\n"
    "v_pk_mul_f32 v[118:119], v[118:119], v[130:131]\n"
    "v_pk_fma_f32 v[244:245], v[244:245], v[232:233], v[232:233] op_sel:[0,1,1] op_sel_hi:[1,1,1]\n"
    "v_pk_fma_f32 v[246:247], v[246:247], v[232:233], v[232:233] op_sel:[0,1,1] op_sel_hi:[1,1,1]\n"
    "v_rcp_f32 v244, v244\n"
    "v_rcp_f32 v245, v245\n"
    "v_rcp_f32 v246, v246\n"
    "v_rcp_f32 v247, v247\n"
    "v_pk_mul_f32 v[116:117], v[116:117], v[244:245]\n"
    "v_pk_mul_f32 v[118:119], v[118:119], v[246:247]\n"
    "v_cvt_pk_bf16_f32 v112, v112, v113\n"
    "v_cvt_pk_bf16_f32 v113, v114, v115\n"
    "v_cvt_pk_bf16_f32 v114, v116, v117\n"
    "v_cvt_pk_bf16_f32 v115, v118, v119\n"
    "buffer_store_dwordx4 v[112:115], v21, s[8:11], s36 offen sc1\n"
    "s_mov_b32 s36, 15253504\n"
    "v_pk_fma_f32 v[140:141], v[234:235], v[64:65], v[140:141] op_sel:[0,0,0] op_sel_hi:[0,1,1] neg_lo:[1,0,0] neg_hi:[1,0,0]\n"
    "v_pk_fma_f32 v[142:143], v[234:235], v[66:67], v[142:143] op_sel:[0,0,0] op_sel_hi:[0,1,1] neg_lo:[1,0,0] neg_hi:[1,0,0]\n"
    "v_pk_mul_f32 v[244:245], v[140:141], v[132:133] op_sel:[0,0] op_sel_hi:[1,0]\n"
    "v_pk_mul_f32 v[246:247], v[142:143], v[132:133] op_sel:[0,0] op_sel_hi:[1,0]\n"
    "v_pk_fma_f32 v[152:153], v[234:235], v[120:121], v[152:153] op_sel:[0,0,0] op_sel_hi:[0,1,1] neg_lo:[1,0,0] neg_hi:[1,0,0]\n"
    "v_pk_fma_f32 v[154:155], v[234:235], v[122:123], v[154:155] op_sel:[0,0,0] op_sel_hi:[0,1,1] neg_lo:[1,0,0] neg_hi:[1,0,0]\n"
    "v_exp_f32 v244, v244\n"
    "v_exp_f32 v245, v245\n"
    "v_exp_f32 v246, v246\n"
    "v_exp_f32 v247, v247\n"
    "v_pk_mul_f32 v[140:141], v[140:141], v[152:153]\n"
    "v_pk_mul_f32 v[142:143], v[142:143], v[154:155]\n"
    "v_pk_fma_f32 v[244:245], v[244:245], v[234:235], v[234:235] op_sel:[0,1,1] op_sel_hi:[1,1,1]\n"
    "v_pk_fma_f32 v[246:247], v[246:247], v[234:235], v[234:235] op_sel:[0,1,1] op_sel_hi:[1,1,1]\n"
    "v_rcp_f32 v244, v244\n"
    "v_rcp_f32 v245, v245\n"
    "v_rcp_f32 v246, v246\n"
    "v_rcp_f32 v247, v247\n"
    "v_pk_mul_f32 v[140:141], v[140:141], v[244:245]\n"
    "v_pk_mul_f32 v[142:143], v[142:143], v[246:247]\n"
    "v_pk_fma_f32 v[144:145], v[234:235], v[92:93], v[144:145] op_sel:[0,0,0] op_sel_hi:[0,1,1] neg_lo:[1,0,0] neg_hi:[1,0,0]\n"
    "v_pk_fma_f32 v[146:147], v[234:235], v[94:95], v[146:147] op_sel:[0,0,0] op_sel_hi:[0,1,1] neg_lo:[1,0,0] neg_hi:[1,0,0]\n"
    "v_pk_mul_f32 v[244:245], v[144:145], v[132:133] op_sel:[0,0] op_sel_hi:[1,0]\n"
    "v_pk_mul_f32 v[246:247], v[146:147], v[132:133] op_sel:[0,0] op_sel_hi:[1,0]\n"
    "v_pk_fma_f32 v[156:157], v[234:235], v[148:149], v[156:157] op_sel:[0,0,0] op_sel_hi:[0,1,1] neg_lo:[1,0,0] neg_hi:[1,0,0]\n"
    "v_pk_fma_f32 v[158:159], v[234:235], v[150:151], v[158:159] op_sel:[0,0,0] op_sel_hi:[0,1,1] neg_lo:[1,0,0] neg_hi:[1,0,0]\n"
    "v_exp_f32 v244, v244\n"
    "v_exp_f32 v245, v245\n"
    "v_exp_f32 v246, v246\n"
    "v_exp_f32 v247, v247\n"
    "v_pk_mul_f32 v[144:145], v[144:145], v[156:157]\n"
    "v_pk_mul_f32 v[146:147], v[146:147], v[158:159]\n"
    "v_pk_fma_f32 v[244:245], v[244:245], v[234:235], v[234:235] op_sel:[0,1,1] op_sel_hi:[1,1,1]\n"
    "S2_H5%=:\n"
    "s_cbranch_execz S2_R%=\n"
    "v_pk_fma_f32 v[246:247], v[246:247], v[234:235], v[234:235] op_sel:[0,1,1] op_sel_hi:[1,1,1]\n"
    "v_rcp_f32 v244, v244\n"
    "v_rcp_f32 v245, v245\n"
    "v_rcp_f32 v246, v246\n"
    "v_rcp_f32 v247, v247\n"
    "v_pk_mul_f32 v[144:145], v[144:145], v[244:245]\n"
    "v_pk_mul_f32 v[146:147], v[146:147], v[246:247]\n"
    "v_cvt_pk_bf16_f32 v140, v140, v141\n"
    "v_cvt_pk_bf16_f32 v141, v142, v143\n"
    "v_cvt_pk_bf16_f32 v142, v144, v145\n"
    "v_cvt_pk_bf16_f32 v143, v146, v147\n"
    "buffer_store_dwordx4 v[140:143], v21, s[8:11], s36 offen sc1\n"
    "s_mov_b32 s36, 15368192\n"
    "v_pk_fma_f32 v[168:169], v[236:237], v[64:65], v[168:169] op_sel:[0,0,0] op_sel_hi:[0,1,1] neg_lo:[1,0,0] neg_hi:[1,0,0]\n"
    "v_pk_fma_f32 v[170:171], v[236:237], v[66:67], v[170:171] op_sel:[0,0,0] op_sel_hi:[0,1,1] neg_lo:[1,0,0] neg_hi:[1,0,0]\n"
    "v_pk_mul_f32 v[244:245], v[168:169], v[160:161] op_sel:[0,0] op_sel_hi:[1,0]\n"
    "v_pk_mul_f32 v[246:247], v[170:171], v[160:161] op_sel:[0,0] op_sel_hi:[1,0]\n"
    "v_pk_fma_f32 v[180:181], v[236:237], v[120:121], v[180:181] op_sel:[0,0,0] op_sel_hi:[0,1,1] neg_lo:[1,0,0] neg_hi:[1,0,0]\n"
    "v_pk_fma_f32 v[182:183], v[236:237], v[122:123], v[182:183] op_sel:[0,0,0] op_sel_hi:[0,1,1] neg_lo:[1,0,0] neg_hi:[1,0,0]\n"
    "v_exp_f32 v244, v244\n"
    "v_exp_f32 v245, v245\n"
    "v_exp_f32 v246, v246\n"
    "v_exp_f32 v247, v247\n"
    "v_pk_mul_f32 v[168:169], v[168:169], v[180:181]\n"
    "v_pk_mul_f32 v[170:171], v[170:171], v[182:183]\n"
    "v_pk_fma_f32 v[244:245], v[244:245], v[236:237], v[236:237] op_sel:[0,1,1] op_sel_hi:[1,1,1]\n"
    "v_pk_fma_f32 v[246:247], v[246:247], v[236:237], v[236:237] op_sel:[0,1,1] op_sel_hi:[1,1,1]\n"
    "v_rcp_f32 v244, v244\n"
    "v_rcp_f32 v245, v245\n"
    "v_rcp_f32 v246, v246\n"
    "v_rcp_f32 v247, v247\n"
    "v_pk_mul_f32 v[168:169], v[168:169], v[244:245]\n"
    "v_pk_mul_f32 v[170:171], v[170:171], v[246:247]\n"
    "v_pk_fma_f32 v[172:173], v[236:237], v[92:93], v[172:173] op_sel:[0,0,0] op_sel_hi:[0,1,1] neg_lo:[1,0,0] neg_hi:[1,0,0]\n"
    "v_pk_fma_f32 v[174:175], v[236:237], v[94:95], v[174:175] op_sel:[0,0,0] op_sel_hi:[0,1,1] neg_lo:[1,0,0] neg_hi:[1,0,0]\n"
    "v_pk_mul_f32 v[244:245], v[172:173], v[160:161] op_sel:[0,0] op_sel_hi:[1,0]\n"
    "v_pk_mul_f32 v[246:247], v[174:175], v[160:161] op_sel:[0,0] op_sel_hi:[1,0]\n"
    "v_pk_fma_f32 v[184:185], v[236:237], v[148:149], v[184:185] op_sel:[0,0,0] op_sel_hi:[0,1,1] neg_lo:[1,0,0] neg_hi:[1,0,0]\n"
    "v_pk_fma_f32 v[186:187], v[236:237], v[150:151], v[186:187] op_sel:[0,0,0] op_sel_hi:[0,1,1] neg_lo:[1,0,0] neg_hi:[1,0,0]\n"
    "v_exp_f32 v244, v244\n"
    "v_exp_f32 v245, v245\n"
    "v_exp_f32 v246, v246\n"
    "v_exp_f32 v247, v247\n"
    "v_pk_mul_f32 v[172:173], v[172:173], v[184:185]\n"
    "v_pk_mul_f32 v[174:175], v[174:175], v[186:187]\n"
    "v_pk_fma_f32 v[244:245], v[244:245], v[236:237], v[236:237] op_sel:[0,1,1] op_sel_hi:[1,1,1]\n"
    "v_pk_fma_f32 v[246:247], v[246:247], v[236:237], v[236:237] op_sel:[0,1,1] op_sel_hi:[1,1,1]\n"
    "v_rcp_f32 v244, v244\n"
    "v_rcp_f32 v245, v245\n"
    "v_rcp_f32 v246, v246\n"
    "v_rcp_f32 v247, v247\n"
    "v_pk_mul_f32 v[172:173], v[172:173], v[244:245]\n"
    "v_pk_mul_f32 v[174:175], v[174:175], v[246:247]\n"
    "v_cvt_pk_bf16_f32 v168, v168, v169\n"
    "v_cvt_pk_bf16_f32 v169, v170, v171\n"
    "v_cvt_pk_bf16_f32 v170, v172, v173\n"
    "v_cvt_pk_bf16_f32 v171, v174, v175\n"
    "buffer_store_dwordx4 v[168:171], v21, s[8:11], s36 offen sc1\n"
    "s_mov_b32 s36, 15482880\n"
    "v_pk_fma_f32 v[196:197], v[238:239], v[64:65], v[196:197] op_sel:[0,0,0] op_sel_hi:[0,1,1] neg_lo:[1,0,0] neg_hi:[1,0,0]\n"
    "v_pk_fma_f32 v[198:199], v[238:239], v[66:67], v[198:199] op_sel:[0,0,0] op_sel_hi:[0,1,1] neg_lo:[1,0,0] neg_hi:[1,0,0]\n"
    "v_pk_mul_f32 v[244:245], v[196:197], v[188:189] op_sel:[0,0] op_sel_hi:[1,0]\n"
    "v_pk_mul_f32 v[246:247], v[198:199], v[188:189] op_sel:[0,0] op_sel_hi:[1,0]\n"
    "v_pk_fma_f32 v[208:209], v[238:239], v[120:121], v[208:209] op_sel:[0,0,0] op_sel_hi:[0,1,1] neg_lo:[1,0,0] neg_hi:[1,0,0]\n"
    "v_pk_fma_f32 v[210:211], v[238:239], v[122:123], v[210:211] op_sel:[0,0,0] op_sel_hi:[0,1,1] neg_lo:[1,0,0] neg_hi:[1,0,0]\n"
    "v_exp_f32 v244, v244\n"
    "v_exp_f32 v245, v245\n"
    "v_exp_f32 v246, v246\n"
    "v_exp_f32 v247, v247\n"
    "v_pk_mul_f32 v[196:197], v[196:197], v[208:209]\n"
    "v_pk_mul_f32 v[198:199], v[198:199], v[210:211]\n"
    "v_pk_fma_f32 v[244:245], v[244:245], v[238:239], v[238:239] op_sel:[0,1,1] op_sel_hi:[1,1,1]\n"
    "v_pk_fma_f32 v[246:247], v[246:247], v[238:239], v[238:239] op_sel:[0,1,1] op_sel_hi:[1,1,1]\n"
    "v_rcp_f32 v244, v244\n"
    "v_rcp_f32 v245, v245\n"
    "v_rcp_f32 v246, v246\n"
    "v_rcp_f32 v247, v247\n"
    "v_pk_mul_f32 v[196:197], v[196:197], v[244:245]\n"
    "v_pk_mul_f32 v[198:199], v[198:199], v[246:247]\n"
    "v_pk_fma_f32 v[200:201], v[238:239], v[92:93], v[200:201] op_sel:[0,0,0] op_sel_hi:[0,1,1] neg_lo:[1,0,0] neg_hi:[1,0,0]\n"
    "v_pk_fma_f32 v[202:203], v[238:239], v[94:95], v[202:203] op_sel:[0,0,0] op_sel_hi:[0,1,1] neg_lo:[1,0,0] neg_hi:[1,0,0]\n"
    "v_pk_mul_f32 v[244:245], v[200:201], v[188:189] op_sel:[0,0] op_sel_hi:[1,0]\n"
    "v_pk_mul_f32 v[246:247], v[202:203], v[188:189] op_sel:[0,0] op_sel_hi:[1,0]\n"
    "v_pk_fma_f32 v[212:213], v[238:239], v[148:149], v[212:213] op_sel:[0,0,0] op_sel_hi:[0,1,1] neg_lo:[1,0,0] neg_hi:[1,0,0]\n"
    "v_pk_fma_f32 v[214:215], v[238:239], v[150:151], v[214:215] op_sel:[0,0,0] op_sel_hi:[0,1,1] neg_lo:[1,0,0] neg_hi:[1,0,0]\n"
    "v_exp_f32 v244, v244\n"
    "v_exp_f32 v245, v245\n"
    "v_exp_f32 v246, v246\n"
    "v_exp_f32 v247, v247\n"
    "v_pk_mul_f32 v[200:201], v[200:201], v[212:213]\n"
    "v_pk_mul_f32 v[202:203], v[202:203], v[214:215]\n"
    "v_pk_fma_f32 v[244:245], v[244:245], v[238:239], v[238:239] op_sel:[0,1,1] op_sel_hi:[1,1,1]\n"
    "v_pk_fma_f32 v[246:247], v[246:247], v[238:239], v[238:239] op_sel:[0,1,1] op_sel_hi:[1,1,1]\n"
    "v_rcp_f32 v244, v244\n"
    "v_rcp_f32 v245, v245\n"
    "v_rcp_f32 v246, v246\n"
    "v_rcp_f32 v247, v247\n"
    "v_pk_mul_f32 v[200:201], v[200:201], v[244:245]\n"
    "v_pk_mul_f32 v[202:203], v[202:203], v[246:247]\n"
    "v_cvt_pk_bf16_f32 v196, v196, v197\n"
    "v_cvt_pk_bf16_f32 v197, v198, v199\n"
    "v_cvt_pk_bf16_f32 v198, v200, v201\n"
    "v_cvt_pk_bf16_f32 v199, v202, v203\n"
    "buffer_store_dwordx4 v[196:199], v21, s[8:11], s36 offen sc1\n"
    "v_mbcnt_lo_u32_b32 v184, -1, 0\n"
    "v_mbcnt_hi_u32_b32 v184, -1, v184\n"
    "v_lshrrev_b32 v185, 4, v184\n"
    "v_and_b32 v184, 15, v184\n"
    "v_lshlrev_b32 v186, 1, v185\n"
    "v_xor_b32 v184, v184, v186\n"
    "v_lshlrev_b32 v185, 11, v185\n"
    "s_and_b32 s56, s5, 7\n"
    "s_lshl_b32 s56, s56, 8\n"
    "s_lshl_b32 s57, s37, 14\n"
    "s_add_u32 s57, s57, s56\n"
    "s_lshr_b32 s58, s37, 1\n"
    "s_lshl_b32 s58, s58, 13\n"
    "s_add_u32 s58, s58, s56\n"
    "v_lshl_add_u32 v186, v184, 4, v185\n"
    "v_add_u32 v132, s57, v186\n"
    "v_add_u32 v134, s58, v186\n"
    "v_xor_b32 v184, 8, v184\n"
    "s_add_u32 s57, s57, 8192\n"
    "s_add_u32 s58, s58, 16384\n"
    "v_lshl_add_u32 v186, v184, 4, v185\n"
    "v_add_u32 v133, s57, v186\n"
    "v_add_u32 v135, s58, v186\n"
    "v_mbcnt_lo_u32_b32 v176, -1, 0\n"
    "v_mbcnt_hi_u32_b32 v176, -1, v176\n"
    "v_lshrrev_b32 v177, 4, v176\n"
    "v_and_b32 v178, 15, v176\n"
    "v_bfe_u32 v179, v176, 2, 2\n"
    "v_and_b32 v180, 3, v176\n"
    "v_and_b32 v181, 7, v176\n"
    "s_and_b32 s56, s4, 3\n"
    "s_lshl_b32 s57, s6, 2\n"
    "s_add_u32 s57, s57, s37\n"
    "s_lshl_b32 s57, s57, 2\n"
    "s_add_u32 s58, s57, 0\n"
    "s_mul_i32 s58, s58, 114688\n"
    "v_lshl_add_u32 v128, v176, 4, s58\n"
    "s_add_u32 s58, s57, 1\n"
    "s_mul_i32 s58, s58, 114688\n"
    "v_lshl_add_u32 v129, v176, 4, s58\n"
    "s_add_u32 s58, s57, 2\n"
    "s_mul_i32 s58, s58, 114688\n"
    "v_lshl_add_u32 v130, v176, 4, s58\n"
    "s_add_u32 s58, s57, 3\n"
    "s_mul_i32 s58, s58, 114688\n"
    "v_lshl_add_u32 v131, v176, 4, s58\n"
    "v_lshl_add_u32 v184, v177, 2, v179\n"
    "v_and_b32 v185, 7, v184\n"
    "v_lshl_add_u32 v184, v184, 8, s3\n"
    "v_lshl_add_u32 v184, v180, 3, v184\n"
    "s_lshl_b32 s58, s56, 1\n"
    "v_xor_b32 v186, s58, v185\n"
    "v_lshl_add_u32 v136, v186, 5, v184\n"
    "s_add_u32 s58, s58, 1\n"
    "s_and_b32 s58, s58, 7\n"
    "v_xor_b32 v186, s58, v185\n"
    "v_lshl_add_u32 v137, v186, 5, v184\n"
    "s_add_u32 s58, s58, 1\n"
    "s_and_b32 s58, s58, 7\n"
    "v_xor_b32 v186, s58, v185\n"
    "v_lshl_add_u32 v138, v186, 5, v184\n"
    "s_add_u32 s58, s58, 1\n"
    "s_and_b32 s58, s58, 7\n"
    "v_xor_b32 v186, s58, v185\n"
    "v_lshl_add_u32 v139, v186, 5, v184\n"
    "s_add_u32 s58, s58, 1\n"
    "s_and_b32 s58, s58, 7\n"
    "v_xor_b32 v186, s58, v185\n"
    "v_lshl_add_u32 v140, v186, 5, v184\n"
    "s_add_u32 s58, s58, 1\n"
    "s_and_b32 s58, s58, 7\n"
    "v_xor_b32 v186, s58, v185\n"
    "v_lshl_add_u32 v141, v186, 5, v184\n"
    "s_add_u32 s58, s58, 1\n"
    "s_and_b32 s58, s58, 7\n"
    "v_xor_b32 v186, s58, v185\n"
    "v_lshl_add_u32 v142, v186, 5, v184\n"
    "s_add_u32 s58, s58, 1\n"
    "s_and_b32 s58, s58, 7\n"
    "v_xor_b32 v186, s58, v185\n"
    "v_lshl_add_u32 v143, v186, 5, v184\n"
    "s_and_b32 s58, s5, 7\n"
    "s_lshl_b32 s59, s6, 3\n"
    "s_add_u32 s59, s59, s58\n"
    "s_lshl_b32 s59, s59, 2\n"
    "s_add_u32 s36, s56, 1\n"
    "s_and_b32 s36, s36, 3\n"
    "s_add_u32 s36, s36, s59\n"
    "s_mul_i32 s36, s36, 12\n"
    "s_add_u32 s36, s36, s37\n"
    "s_lshl_b32 s36, s36, 12\n"
    "s_add_u32 s36, s36, 30408704\n"
    "v_lshl_add_u32 v144, v176, 4, s36\n"
    "s_add_u32 s36, s56, s59\n"
    "s_mul_i32 s36, s36, 12\n"
    "s_add_u32 s36, s36, s37\n"
    "s_lshl_b32 s36, s36, 12\n"
    "s_add_u32 s36, s36, 30408704\n"
    "v_lshl_add_u32 v147, v176, 4, s36\n"
    "s_add_u32 s36, s56, 2\n"
    "s_and_b32 s36, s36, 3\n"
    "s_add_u32 s36, s36, s59\n"
    "s_mul_i32 s36, s36, 12\n"
    "s_add_u32 s36, s36, s37\n"
    "s_lshl_b32 s36, s36, 12\n"
    "s_add_u32 s36, s36, 30425088\n"
    "v_lshl_add_u32 v145, v176, 4, s36\n"
    "s_add_u32 s36, s56, s59\n"
    "s_mul_i32 s36, s36, 12\n"
    "s_add_u32 s36, s36, s37\n"
    "s_lshl_b32 s36, s36, 12\n"
    "s_add_u32 s36, s36, 30425088\n"
    "v_lshl_add_u32 v148, v176, 4, s36\n"
    "s_add_u32 s36, s56, 3\n"
    "s_and_b32 s36, s36, 3\n"
    "s_add_u32 s36, s36, s59\n"
    "s_mul_i32 s36, s36, 12\n"
    "s_add_u32 s36, s36, s37\n"
    "s_lshl_b32 s36, s36, 12\n"
    "s_add_u32 s36, s36, 30441472\n"
    "v_lshl_add_u32 v146, v176, 4, s36\n"
    "s_add_u32 s36, s56, s59\n"
    "s_mul_i32 s36, s36, 12\n"
    "s_add_u32 s36, s36, s37\n"
    "s_lshl_b32 s36, s36, 12\n"
    "s_add_u32 s36, s36, 30441472\n"
    "v_lshl_add_u32 v149, v176, 4, s36\n"
    "v_min_u32 v184, 1, v176\n"
    "s_add_u32 s36, s56, s59\n"
    "s_lshl_b32 s36, s36, 2\n"
    "s_add_u32 s36, s36, s37\n"
    "s_mul_i32 s36, s36, 64\n"
    "s_add_u32 s36, s36, 151552\n"
    "v_lshl_or_b32 v150, v184, 31, s36\n"
    "s_lshl_b32 s36, s59, 2\n"
    "s_add_u32 s36, s36, s37\n"
    "s_mul_i32 s36, s36, 64\n"
    "s_add_u32 s36, s36, 151552\n"
    "v_and_b32 v186, 1, v176\n"
    "s_add_u32 s35, s56, 3\n"
    "v_sub_u32 v185, s35, v186\n"
    "v_and_b32 v185, 3, v185\n"
    "v_mul_u32_u24 v185, 16, v185\n"
    "v_add_u32 v185, v185, v186\n"
    "v_lshl_add_u32 v151, v185, 4, s36\n"
    "s_add_u32 s35, s56, 1\n"
    "s_and_b32 s35, s35, 3\n"
    "s_mul_i32 s35, s35, 256\n"
    "s_add_u32 s35, s35, s36\n"
    "s_add_u32 s35, s35, 32\n"
    "v_mov_b32 v248, s35\n"
    "s_lshl_b32 s36, s58, 2\n"
    "s_add_u32 s36, s36, s56\n"
    "s_lshl_b32 s53, s36, 6\n"
    "s_lshl_b32 s36, s36, 17\n"
    "s_lshl_b32 s35, s57, 7\n"
    "s_add_u32 s36, s36, s35\n"
    "s_add_u32 s36, s36, 10485760\n"
    "v_lshl_add_u32 v185, v177, 14, s36\n"
    "v_lshl_add_u32 v152, v178, 3, v185\n"
    "s_lshl_b32 s36, s57, 15\n"
    "s_add_u32 s36, s36, s53\n"
    "v_lshl_add_u32 v185, v178, 11, s36\n"
    "v_and_b32 v186, 1, v177\n"
    "v_lshl_add_u32 v185, v186, 5, v185\n"
    "v_lshrrev_b32 v186, 1, v177\n"
    "v_lshl_add_u32 v153, v186, 4, v185\n"
    "s_lshl_b32 s36, s6, 2\n"
    "s_add_u32 s36, s36, s56\n"
    "s_lshl_b32 s36, s36, 7\n"
    "s_add_u32 s36, s36, 217088\n"
    "v_lshl_add_u32 v155, v181, 2, s36\n"
    "s_lshl_b32 s35, s58, 2\n"
    "s_add_u32 s36, s36, s35\n"
    "v_lshl_or_b32 v154, v184, 31, s36\n"
    "s_mov_b32 s12, s54\n"
    "s_and_b32 s13, s55, 0xffff\n"
    "s_mov_b32 s14, 4194304\n"
    "s_mov_b32 s15, 0x20000\n"
    "v_mov_b32 v161, 1\n"
    "s_waitcnt vmcnt(0) lgkmcnt(0)\n"
    "s_barrier\n"
    "s_cmp_lg_u32 s37, 0\n"
    "s_cbranch_scc1 S3_A%=\n"
    "buffer_store_dword v161, v154, s[8:11], 0 offen sc1\n"
    "S3_A%=:\n"
    "s_cmp_lg_u32 s37, 0\n"
    "s_cbranch_scc1 S3_W%=\n"
    "s_mov_b32 s35, 0\n"
    "S3_L%=:\n"
    "buffer_load_dword v240, v155, s[8:11], 0 offen sc1\n"
    "s_waitcnt vmcnt(0)\n"
    "v_cmp_eq_u32 vcc, 0, v240\n"
    "s_add_u32 s35, s35, 1\n"
    "s_nop 3\n"
    "s_cbranch_vccz S3_D%=\n"
    "s_sleep 2\n"
    "s_cmp_lt_u32 s35, 0x40000\n"
    "s_cbranch_scc1 S3_L%=\n"
    "S3_D%=:\n"
    "s_branch S3_E%=\n"
    "S3_W%=:\n"
    "s_mov_b64 exec, 0\n"
    "s_cmp_eq_u32 s37, 1\n"
    "s_cbranch_scc1 S3_H0%=\n"
    "s_cmp_eq_u32 s37, 2\n"
    "s_cbranch_scc1 S3_H1%=\n"
    "s_branch S3_H2%=\n"
    "S3_R%=:\n"
    "s_mov_b64 exec, -1\n"
    "S3_E%=:\n"
    "s_barrier\n"
    "s_add_u32 s57, s46, 0\n"
    "buffer_load_dwordx4 a[128:131], v128, s[8:11], s57 offen sc1\n"
    "buffer_load_dwordx4 a[132:135], v129, s[8:11], s57 offen sc1\n"
    "buffer_load_dwordx4 a[136:139], v130, s[8:11], s57 offen sc1\n"
    "buffer_load_dwordx4 a[140:143], v131, s[8:11], s57 offen sc1\n"
    "s_add_u32 s57, s46, 1024\n"
    "buffer_load_dwordx4 a[144:147], v128, s[8:11], s57 offen sc1\n"
    "buffer_load_dwordx4 a[148:151], v129, s[8:11], s57 offen sc1\n"
    "buffer_load_dwordx4 a[152:155], v130, s[8:11], s57 offen sc1\n"
    "buffer_load_dwordx4 a[156:159], v131, s[8:11], s57 offen sc1\n"
    "ds_read_b64_tr_b16 a[0:1], v136 offset:0\n"
    "ds_read_b64_tr_b16 a[2:3], v136 offset:4096\n"
    "ds_read_b64_tr_b16 a[4:5], v137 offset:0\n"
    "ds_read_b64_tr_b16 a[6:7], v137 offset:4096\n"
    "ds_read_b64_tr_b16 a[8:9], v138 offset:0\n"
    "ds_read_b64_tr_b16 a[10:11], v138 offset:4096\n"
    "ds_read_b64_tr_b16 a[12:13], v139 offset:0\n"
    "ds_read_b64_tr_b16 a[14:15], v139 offset:4096\n"
    "ds_read_b64_tr_b16 a[16:17], v140 offset:0\n"
    "ds_read_b64_tr_b16 a[18:19], v140 offset:4096\n"
    "ds_read_b64_tr_b16 a[20:21], v141 offset:0\n"
    "ds_read_b64_tr_b16 a[22:23], v141 offset:4096\n"
    "ds_read_b64_tr_b16 a[24:25], v142 offset:0\n"
    "ds_read_b64_tr_b16 a[26:27], v142 offset:4096\n"
    "ds_read_b64_tr_b16 a[28:29], v143 offset:0\n"
    "ds_read_b64_tr_b16 a[30:31], v143 offset:4096\n"
    "ds_read_b64_tr_b16 a[32:33], v136 offset:8192\n"
    "ds_read_b64_tr_b16 a[34:35], v136 offset:12288\n"
    "ds_read_b64_tr_b16 a[36:37], v137 offset:8192\n"
    "ds_read_b64_tr_b16 a[38:39], v137 offset:12288\n"
    "ds_read_b64_tr_b16 a[40:41], v138 offset:8192\n"
    "ds_read_b64_tr_b16 a[42:43], v138 offset:12288\n"
    "ds_read_b64_tr_b16 a[44:45], v139 offset:8192\n"
    "ds_read_b64_tr_b16 a[46:47], v139 offset:12288\n"
    "ds_read_b64_tr_b16 a[48:49], v140 offset:8192\n"
    "ds_read_b64_tr_b16 a[50:51], v140 offset:12288\n"
    "ds_read_b64_tr_b16 a[52:53], v141 offset:8192\n"
    "ds_read_b64_tr_b16 a[54:55], v141 offset:12288\n"
    "ds_read_b64_tr_b16 a[56:57], v142 offset:8192\n"
    "ds_read_b64_tr_b16 a[58:59], v142 offset:12288\n"
    "ds_read_b64_tr_b16 a[60:61], v143 offset:8192\n"
    "ds_read_b64_tr_b16 a[62:63], v143 offset:12288\n"
    "s_add_u32 s57, s46, 2048\n"
    "buffer_load_dwordx4 a[160:163], v128, s[8:11], s57 offen sc1\n"
    "buffer_load_dwordx4 a[164:167], v129, s[8:11], s57 offen sc1\n"
    "buffer_load_dwordx4 a[168:171], v130, s[8:11], s57 offen sc1\n"
    "buffer_load_dwordx4 a[172:175], v131, s[8:11], s57 offen sc1\n"
    "s_add_u32 s57, s46, 3072\n"
    "buffer_load_dwordx4 a[176:179], v128, s[8:11], s57 offen sc1\n"
    "buffer_load_dwordx4 a[180:183], v129, s[8:11], s57 offen sc1\n"
    "buffer_load_dwordx4 a[184:187], v130, s[8:11], s57 offen sc1\n"
    "buffer_load_dwordx4 a[188:191], v131, s[8:11], s57 offen sc1\n"
    "s_add_u32 s57, s46, 4096\n"
    "buffer_load_dwordx4 a[192:195], v128, s[8:11], s57 offen sc1\n"
    "buffer_load_dwordx4 a[196:199], v129, s[8:11], s57 offen sc1\n"
    "buffer_load_dwordx4 a[200:203], v130, s[8:11], s57 offen sc1\n"
    "buffer_load_dwordx4 a[204:207], v131, s[8:11], s57 offen sc1\n"
    "s_add_u32 s57, s46, 5120\n"
    "buffer_load_dwordx4 a[208:211], v128, s[8:11], s57 offen sc1\n"
    "buffer_load_dwordx4 a[212:215], v129, s[8:11], s57 offen sc1\n"
    "buffer_load_dwordx4 a[216:219], v130, s[8:11], s57 offen sc1\n"
    "buffer_load_dwordx4 a[220:223], v131, s[8:11], s57 offen sc1\n"
    "v_add_u32 v236, 0x10000, v136\n"
    "v_add_u32 v237, 0x10000, v137\n"
    "v_add_u32 v238, 0x10000, v138\n"
    "v_add_u32 v239, 0x10000, v139\n"
    "v_add_u32 v240, 0x10000, v140\n"
    "v_add_u32 v241, 0x10000, v141\n"
    "v_add_u32 v242, 0x10000, v142\n"
    "v_add_u32 v243, 0x10000, v143\n"
    "v_mov_b32 v250, 1\n"
    "v_mov_b32 v249, 0\n"
    "s_waitcnt vmcnt(16) lgkmcnt(0)\n"
    "s_barrier\n"
    "v_mfma_f32_16x16x32_bf16 v[0:3], a[0:3], a[128:131], 0\n"
    "s_add_u32 s50, s45, 589824\n"
    "ds_read_b64_tr_b16 a[64:65], v136 offset:16384\n"
    "v_mfma_f32_16x16x32_bf16 v[0:3], a[32:35], a[144:147], v[0:3]\n"
    "s_add_u32 m0, s44, 65536\n"
    "ds_read_b64_tr_b16 a[66:67], v136 offset:20480\n"
    "v_mfma_f32_16x16x32_bf16 v[4:7], a[36:39], a[144:147], 0\n"
    "buffer_load_dwordx4 v132, s[40:43], s50 offen lds\n"
    "ds_read_b64_tr_b16 a[68:69], v137 offset:16384\n"
    "v_mfma_f32_16x16x32_bf16 v[4:7], a[4:7], a[128:131], v[4:7]\n"
    "ds_read_b64_tr_b16 a[70:71], v137 offset:20480\n"
    "ds_read_b64_tr_b16 a[72:73], v138 offset:16384\n"
    "v_mfma_f32_16x16x32_bf16 v[8:11], a[8:11], a[128:131], 0\n"
    "s_add_u32 s57, s46, 6144\n"
    "ds_read_b64_tr_b16 a[74:75], v138 offset:20480\n"
    "v_mfma_f32_16x16x32_bf16 v[8:11], a[40:43], a[144:147], v[8:11]\n"
    "buffer_load_dwordx4 a[224:227], v128, s[8:11], s57 offen sc1\n"
    "ds_read_b64_tr_b16 a[76:77], v139 offset:16384\n"
    "v_mfma_f32_16x16x32_bf16 v[12:15], a[44:47], a[144:147], 0\n"
    "ds_read_b64_tr_b16 a[78:79], v139 offset:20480\n"
    "ds_read_b64_tr_b16 a[80:81], v140 offset:16384\n"
    "v_mfma_f32_16x16x32_bf16 v[12:15], a[12:15], a[128:131], v[12:15]\n"
    "ds_read_b64_tr_b16 a[82:83], v140 offset:20480\n"
    "ds_read_b64_tr_b16 a[84:85], v141 offset:16384\n"
    "v_mfma_f32_16x16x32_bf16 v[16:19], a[16:19], a[128:131], 0\n"
    "ds_read_b64_tr_b16 a[86:87], v141 offset:20480\n"
    "ds_read_b64_tr_b16 a[88:89], v142 offset:16384\n"
    "v_mfma_f32_16x16x32_bf16 v[16:19], a[48:51], a[144:147], v[16:19]\n"
    "buffer_load_dwordx4 a[228:231], v129, s[8:11], s57 offen sc1\n"
    "ds_read_b64_tr_b16 a[90:91], v142 offset:20480\n"
    "v_mfma_f32_16x16x32_bf16 v[20:23], a[52:55], a[144:147], 0\n"
    "ds_read_b64_tr_b16 a[92:93], v143 offset:16384\n"
    "ds_read_b64_tr_b16 a[94:95], v143 offset:20480\n"
    "v_mfma_f32_16x16x32_bf16 v[20:23], a[20:23], a[128:131], v[20:23]\n"
    "ds_read_b64_tr_b16 a[96:97], v136 offset:24576\n"
    "ds_read_b64_tr_b16 a[98:99], v136 offset:28672\n"
    "v_mfma_f32_16x16x32_bf16 v[24:27], a[24:27], a[128:131], 0\n"
    "s_add_u32 m0, s44, 66560\n"
    "ds_read_b64_tr_b16 a[100:101], v137 offset:24576\n"
    "v_mfma_f32_16x16x32_bf16 v[24:27], a[56:59], a[144:147], v[24:27]\n"
    "buffer_load_dwordx4 v133, s[40:43], s50 offen lds\n"
    "ds_read_b64_tr_b16 a[102:103], v137 offset:28672\n"
    "v_mfma_f32_16x16x32_bf16 v[28:31], a[60:63], a[144:147], 0\n"
    "ds_read_b64_tr_b16 a[104:105], v138 offset:24576\n"
    "ds_read_b64_tr_b16 a[106:107], v138 offset:28672\n"
    "v_mfma_f32_16x16x32_bf16 v[28:31], a[28:31], a[128:131], v[28:31]\n"
    "ds_read_b64_tr_b16 a[108:109], v139 offset:24576\n"
    "ds_read_b64_tr_b16 a[110:111], v139 offset:28672\n"
    "v_mfma_f32_16x16x32_bf16 v[60:63], a[28:31], a[132:135], 0\n"
    "ds_read_b64_tr_b16 a[112:113], v140 offset:24576\n"
    "ds_read_b64_tr_b16 a[114:115], v140 offset:28672\n"
    "v_mfma_f32_16x16x32_bf16 v[60:63], a[60:63], a[148:151], v[60:63]\n"
    "buffer_load_dwordx4 a[232:235], v130, s[8:11], s57 offen sc1\n"
    "ds_read_b64_tr_b16 a[116:117], v141 offset:24576\n"
    "v_mfma_f32_16x16x32_bf16 v[56:59], a[56:59], a[148:151], 0\n"
    "ds_read_b64_tr_b16 a[118:119], v141 offset:28672\n"
    "ds_read_b64_tr_b16 a[120:121], v142 offset:24576\n"
    "v_mfma_f32_16x16x32_bf16 v[56:59], a[24:27], a[132:135], v[56:59]\n"
    "s_add_u32 s50, s60, 524288\n"
    "ds_read_b64_tr_b16 a[122:123], v142 offset:28672\n"
    "v_mfma_f32_16x16x32_bf16 v[52:55], a[20:23], a[132:135], 0\n"
    "s_add_u32 m0, s44, 73728\n"
    "ds_read_b64_tr_b16 a[124:125], v143 offset:24576\n"
    "v_mfma_f32_16x16x32_bf16 v[52:55], a[52:55], a[148:151], v[52:55]\n"
    "buffer_load_dwordx4 v134, s[40:43], s50 offen lds\n"
    "ds_read_b64_tr_b16 a[126:127], v143 offset:28672\n"
    "v_mfma_f32_16x16x32_bf16 v[48:51], a[48:51], a[148:151], 0\n"
    "v_mfma_f32_16x16x32_bf16 v[48:51], a[16:19], a[132:135], v[48:51]\n"
    "v_mfma_f32_16x16x32_bf16 v[44:47], a[12:15], a[132:135], 0\n"
    "v_mfma_f32_16x16x32_bf16 v[44:47], a[44:47], a[148:151], v[44:47]\n"
    "buffer_load_dwordx4 a[236:239], v131, s[8:11], s57 offen sc1\n"
    "v_mfma_f32_16x16x32_bf16 v[40:43], a[40:43], a[148:151], 0\n"
    "v_mfma_f32_16x16x32_bf16 v[40:43], a[8:11], a[132:135], v[40:43]\n"
    "v_mfma_f32_16x16x32_bf16 v[36:39], a[4:7], a[132:135], 0\n"
    "s_add_u32 m0, s44, 74752\n"
    "v_mfma_f32_16x16x32_bf16 v[36:39], a[36:39], a[148:151], v[36:39]\n"
    "buffer_load_dwordx4 v135, s[40:43], s50 offen lds\n"
    "v_mfma_f32_16x16x32_bf16 v[32:35], a[32:35], a[148:151], 0\n"
    "v_mfma_f32_16x16x32_bf16 v[32:35], a[0:3], a[132:135], v[32:35]\n"
    "v_mfma_f32_16x16x32_bf16 v[64:67], a[0:3], a[136:139], 0\n"
    "s_add_u32 s57, s46, 7168\n"
    "v_mfma_f32_16x16x32_bf16 v[64:67], a[32:35], a[152:155], v[64:67]\n"
    "buffer_load_dwordx4 a[240:243], v128, s[8:11], s57 offen sc1\n"
    "v_mfma_f32_16x16x32_bf16 v[68:71], a[36:39], a[152:155], 0\n"
    "v_mfma_f32_16x16x32_bf16 v[68:71], a[4:7], a[136:139], v[68:71]\n"
    "s_add_u32 s50, s61, 557056\n"
    "v_mfma_f32_16x16x32_bf16 v[72:75], a[8:11], a[136:139], 0\n"
    "s_add_u32 m0, s44, 81920\n"
    "v_mfma_f32_16x16x32_bf16 v[72:75], a[40:43], a[152:155], v[72:75]\n"
    "buffer_load_dwordx4 v134, s[40:43], s50 offen lds\n"
    "v_mfma_f32_16x16x32_bf16 v[76:79], a[44:47], a[152:155], 0\n"
    "v_mfma_f32_16x16x32_bf16 v[76:79], a[12:15], a[136:139], v[76:79]\n"
    "v_mfma_f32_16x16x32_bf16 v[80:83], a[16:19], a[136:139], 0\n"
    "v_mfma_f32_16x16x32_bf16 v[80:83], a[48:51], a[152:155], v[80:83]\n"
    "buffer_load_dwordx4 a[244:247], v129, s[8:11], s57 offen sc1\n"
    "v_mfma_f32_16x16x32_bf16 v[84:87], a[52:55], a[152:155], 0\n"
    "v_mfma_f32_16x16x32_bf16 v[84:87], a[20:23], a[136:139], v[84:87]\n"
    "v_mfma_f32_16x16x32_bf16 v[88:91], a[24:27], a[136:139], 0\n"
    "s_add_u32 m0, s44, 82944\n"
    "v_mfma_f32_16x16x32_bf16 v[88:91], a[56:59], a[152:155], v[88:91]\n"
    "buffer_load_dwordx4 v135, s[40:43], s50 offen lds\n"
    "v_mfma_f32_16x16x32_bf16 v[92:95], a[60:63], a[152:155], 0\n"
    "v_mfma_f32_16x16x32_bf16 v[92:95], a[28:31], a[136:139], v[92:95]\n"
    "v_mfma_f32_16x16x32_bf16 v[124:127], a[28:31], a[140:143], 0\n"
    "v_mfma_f32_16x16x32_bf16 v[124:127], a[60:63], a[156:159], v[124:127]\n"
    "buffer_load_dwordx4 a[248:251], v130, s[8:11], s57 offen sc1\n"
    "v_mfma_f32_16x16x32_bf16 v[120:123], a[56:59], a[156:159], 0\n"
    "v_mfma_f32_16x16x32_bf16 v[120:123], a[24:27], a[140:143], v[120:123]\n"
    "s_add_u32 s50, s45, 688128\n"
    "v_mfma_f32_16x16x32_bf16 v[116:119], a[20:23], a[140:143], 0\n"
    "s_add_u32 m0, s44, 90112\n"
    "v_mfma_f32_16x16x32_bf16 v[116:119], a[52:55], a[156:159], v[116:119]\n"
    "buffer_load_dwordx4 v132, s[40:43], s50 offen lds\n"
    "v_mfma_f32_16x16x32_bf16 v[112:115], a[48:51], a[156:159], 0\n"
    "v_mfma_f32_16x16x32_bf16 v[112:115], a[16:19], a[140:143], v[112:115]\n"
    "v_mfma_f32_16x16x32_bf16 v[108:111], a[12:15], a[140:143], 0\n"
    "v_mfma_f32_16x16x32_bf16 v[108:111], a[44:47], a[156:159], v[108:111]\n"
    "buffer_load_dwordx4 a[252:255], v131, s[8:11], s57 offen sc1\n"
    "v_mfma_f32_16x16x32_bf16 v[104:107], a[40:43], a[156:159], 0\n"
    "v_mfma_f32_16x16x32_bf16 v[104:107], a[8:11], a[140:143], v[104:107]\n"
    "v_mfma_f32_16x16x32_bf16 v[100:103], a[4:7], a[140:143], 0\n"
    "v_mfma_f32_16x16x32_bf16 v[100:103], a[36:39], a[156:159], v[100:103]\n"
    "v_mfma_f32_16x16x32_bf16 v[96:99], a[32:35], a[156:159], 0\n"
    "v_mfma_f32_16x16x32_bf16 v[96:99], a[0:3], a[140:143], v[96:99]\n"
    "s_waitcnt vmcnt(23) lgkmcnt(0)\n"
    "s_barrier\n"
    "v_mfma_f32_16x16x32_bf16 v[0:3], a[64:67], a[160:163], v[0:3]\n"
    "ds_read_b64_tr_b16 a[0:1], v136 offset:32768\n"
    "ds_read_b64_tr_b16 a[2:3], v136 offset:36864\n"
    "v_mfma_f32_16x16x32_bf16 v[0:3], a[96:99], a[176:179], v[0:3]\n"
    "s_add_u32 m0, s44, 91136\n"
    "ds_read_b64_tr_b16 a[4:5], v137 offset:32768\n"
    "v_mfma_f32_16x16x32_bf16 v[4:7], a[100:103], a[176:179], v[4:7]\n"
    "buffer_load_dwordx4 v133, s[40:43], s50 offen lds\n"
    "ds_read_b64_tr_b16 a[6:7], v137 offset:36864\n"
    "v_mfma_f32_16x16x32_bf16 v[4:7], a[68:71], a[160:163], v[4:7]\n"
    "ds_read_b64_tr_b16 a[8:9], v138 offset:32768\n"
    "ds_read_b64_tr_b16 a[10:11], v138 offset:36864\n"
    "v_mfma_f32_16x16x32_bf16 v[8:11], a[72:75], a[160:163], v[8:11]\n"
    "s_add_u32 s57, s46, 8192\n"
    "ds_read_b64_tr_b16 a[12:13], v139 offset:32768\n"
    "v_mfma_f32_16x16x32_bf16 v[8:11], a[104:107], a[176:179], v[8:11]\n"
    "buffer_load_dwordx4 a[128:131], v128, s[8:11], s57 offen sc1\n"
    "ds_read_b64_tr_b16 a[14:15], v139 offset:36864\n"
    "v_mfma_f32_16x16x32_bf16 v[12:15], a[108:111], a[176:179], v[12:15]\n"
    "ds_read_b64_tr_b16 a[16:17], v140 offset:32768\n"
    "ds_read_b64_tr_b16 a[18:19], v140 offset:36864\n"
    "v_mfma_f32_16x16x32_bf16 v[12:15], a[76:79], a[160:163], v[12:15]\n"
    "ds_read_b64_tr_b16 a[20:21], v141 offset:32768\n"
    "ds_read_b64_tr_b16 a[22:23], v141 offset:36864\n"
    "v_mfma_f32_16x16x32_bf16 v[16:19], a[80:83], a[160:163], v[16:19]\n"
    "ds_read_b64_tr_b16 a[24:25], v142 offset:32768\n"
    "ds_read_b64_tr_b16 a[26:27], v142 offset:36864\n"
    "v_mfma_f32_16x16x32_bf16 v[16:19], a[112:115], a[176:179], v[16:19]\n"
    "buffer_load_dwordx4 a[132:135], v129, s[8:11], s57 offen sc1\n"
    "ds_read_b64_tr_b16 a[28:29], v143 offset:32768\n"
    "v_mfma_f32_16x16x32_bf16 v[20:23], a[116:119], a[176:179], v[20:23]\n"
    "ds_read_b64_tr_b16 a[30:31], v143 offset:36864\n"
    "ds_read_b64_tr_b16 a[32:33], v136 offset:40960\n"
    "v_mfma_f32_16x16x32_bf16 v[20:23], a[84:87], a[160:163], v[20:23]\n"
    "s_add_u32 s50, s45, 819200\n"
    "ds_read_b64_tr_b16 a[34:35], v136 offset:45056\n"
    "v_mfma_f32_16x16x32_bf16 v[24:27], a[88:91], a[160:163], v[24:27]\n"
    "s_add_u32 m0, s44, 98304\n"
    "ds_read_b64_tr_b16 a[36:37], v137 offset:40960\n"
    "v_mfma_f32_16x16x32_bf16 v[24:27], a[120:123], a[176:179], v[24:27]\n"
    "buffer_load_dwordx4 v132, s[40:43], s50 offen lds\n"
    "ds_read_b64_tr_b16 a[38:39], v137 offset:45056\n"
    "v_mfma_f32_16x16x32_bf16 v[28:31], a[124:127], a[176:179], v[28:31]\n"
    "ds_read_b64_tr_b16 a[40:41], v138 offset:40960\n"
    "ds_read_b64_tr_b16 a[42:43], v138 offset:45056\n"
    "v_mfma_f32_16x16x32_bf16 v[28:31], a[92:95], a[160:163], v[28:31]\n"
    "ds_read_b64_tr_b16 a[44:45], v139 offset:40960\n"
    "ds_read_b64_tr_b16 a[46:47], v139 offset:45056\n"
    "v_mfma_f32_16x16x32_bf16 v[60:63], a[92:95], a[164:167], v[60:63]\n"
    "ds_read_b64_tr_b16 a[48:49], v140 offset:40960\n"
    "ds_read_b64_tr_b16 a[50:51], v140 offset:45056\n"
    "v_mfma_f32_16x16x32_bf16 v[60:63], a[124:127], a[180:183], v[60:63]\n"
    "buffer_load_dwordx4 a[136:139], v130, s[8:11], s57 offen sc1\n"
    "ds_read_b64_tr_b16 a[52:53], v141 offset:40960\n"
    "v_mfma_f32_16x16x32_bf16 v[56:59], a[120:123], a[180:183], v[56:59]\n"
    "ds_read_b64_tr_b16 a[54:55], v141 offset:45056\n"
    "ds_read_b64_tr_b16 a[56:57], v142 offset:40960\n"
    "v_mfma_f32_16x16x32_bf16 v[56:59], a[88:91], a[164:167], v[56:59]\n"
    "ds_read_b64_tr_b16 a[58:59], v142 offset:45056\n"
    "ds_read_b64_tr_b16 a[60:61], v143 offset:40960\n"
    "v_mfma_f32_16x16x32_bf16 v[52:55], a[84:87], a[164:167], v[52:55]\n"
    "s_add_u32 m0, s44, 99328\n"
    "ds_read_b64_tr_b16 a[62:63], v143 offset:45056\n"
    "v_mfma_f32_16x16x32_bf16 v[52:55], a[116:119], a[180:183], v[52:55]\n"
    "buffer_load_dwordx4 v133, s[40:43], s50 offen lds\n"
    "v_mfma_f32_16x16x32_bf16 v[48:51], a[112:115], a[180:183], v[48:51]\n"
    "v_mfma_f32_16x16x32_bf16 v[48:51], a[80:83], a[164:167], v[48:51]\n"
    "v_mfma_f32_16x16x32_bf16 v[44:47], a[76:79], a[164:167], v[44:47]\n"
    "v_mfma_f32_16x16x32_bf16 v[44:47], a[108:111], a[180:183], v[44:47]\n"
    "buffer_load_dwordx4 a[140:143], v131, s[8:11], s57 offen sc1\n"
    "v_mfma_f32_16x16x32_bf16 v[40:43], a[104:107], a[180:183], v[40:43]\n"
    "v_mfma_f32_16x16x32_bf16 v[40:43], a[72:75], a[164:167], v[40:43]\n"
    "s_add_u32 s50, s60, 753664\n"
    "v_mfma_f32_16x16x32_bf16 v[36:39], a[68:71], a[164:167], v[36:39]\n"
    "s_add_u32 m0, s44, 106496\n"
    "v_mfma_f32_16x16x32_bf16 v[36:39], a[100:103], a[180:183], v[36:39]\n"
    "buffer_load_dwordx4 v134, s[40:43], s50 offen lds\n"
    "v_mfma_f32_16x16x32_bf16 v[32:35], a[96:99], a[180:183], v[32:35]\n"
    "v_mfma_f32_16x16x32_bf16 v[32:35], a[64:67], a[164:167], v[32:35]\n"
    "v_mfma_f32_16x16x32_bf16 v[64:67], a[64:67], a[168:171], v[64:67]\n"
    "s_add_u32 s57, s46, 9216\n"
    "v_mfma_f32_16x16x32_bf16 v[64:67], a[96:99], a[184:187], v[64:67]\n"
    "buffer_load_dwordx4 a[144:147], v128, s[8:11], s57 offen sc1\n"
    "v_mfma_f32_16x16x32_bf16 v[68:71], a[100:103], a[184:187], v[68:71]\n"
    "v_mfma_f32_16x16x32_bf16 v[68:71], a[68:71], a[168:171], v[68:71]\n"
    "v_mfma_f32_16x16x32_bf16 v[72:75], a[72:75], a[168:171], v[72:75]\n"
    "s_add_u32 m0, s44, 107520\n"
    "v_mfma_f32_16x16x32_bf16 v[72:75], a[104:107], a[184:187], v[72:75]\n"
    "buffer_load_dwordx4 v135, s[40:43], s50 offen lds\n"
    "v_mfma_f32_16x16x32_bf16 v[76:79], a[108:111], a[184:187], v[76:79]\n"
    "v_mfma_f32_16x16x32_bf16 v[76:79], a[76:79], a[168:171], v[76:79]\n"
    "v_mfma_f32_16x16x32_bf16 v[80:83], a[80:83], a[168:171], v[80:83]\n"
    "v_mfma_f32_16x16x32_bf16 v[80:83], a[112:115], a[184:187], v[80:83]\n"
    "buffer_load_dwordx4 a[148:151], v129, s[8:11], s57 offen sc1\n"
    "v_mfma_f32_16x16x32_bf16 v[84:87], a[116:119], a[184:187], v[84:87]\n"
    "v_mfma_f32_16x16x32_bf16 v[84:87], a[84:87], a[168:171], v[84:87]\n"
    "S3_H0%=:\n"
    "s_cbranch_execz S3_H3%=\n"
    "s_add_u32 s50, s45, 917504\n"
    "v_mfma_f32_16x16x32_bf16 v[88:91], a[88:91], a[168:171], v[88:91]\n"
    "s_add_u32 m0, s44, 114688\n"
    "v_mfma_f32_16x16x32_bf16 v[88:91], a[120:123], a[184:187], v[88:91]\n"
    "buffer_load_dwordx4 v132, s[40:43], s50 offen lds\n"
    "v_mfma_f32_16x16x32_bf16 v[92:95], a[124:127], a[184:187], v[92:95]\n"
    "v_mfma_f32_16x16x32_bf16 v[92:95], a[92:95], a[168:171], v[92:95]\n"
    "v_mfma_f32_16x16x32_bf16 v[124:127], a[92:95], a[172:175], v[124:127]\n"
    "v_mfma_f32_16x16x32_bf16 v[124:127], a[124:127], a[188:191], v[124:127]\n"
    "buffer_load_dwordx4 a[152:155], v130, s[8:11], s57 offen sc1\n"
    "v_mfma_f32_16x16x32_bf16 v[120:123], a[120:123], a[188:191], v[120:123]\n"
    "v_mfma_f32_16x16x32_bf16 v[120:123], a[88:91], a[172:175], v[120:123]\n"
    "v_mfma_f32_16x16x32_bf16 v[116:119], a[84:87], a[172:175], v[116:119]\n"
    "s_add_u32 m0, s44, 115712\n"
    "v_mfma_f32_16x16x32_bf16 v[116:119], a[116:119], a[188:191], v[116:119]\n"
    "buffer_load_dwordx4 v133, s[40:43], s50 offen lds\n"
    "v_mfma_f32_16x16x32_bf16 v[112:115], a[112:115], a[188:191], v[112:115]\n"
    "v_mfma_f32_16x16x32_bf16 v[112:115], a[80:83], a[172:175], v[112:115]\n"
    "v_mfma_f32_16x16x32_bf16 v[108:111], a[76:79], a[172:175], v[108:111]\n"
    "v_mfma_f32_16x16x32_bf16 v[108:111], a[108:111], a[188:191], v[108:111]\n"
    "buffer_load_dwordx4 a[156:159], v131, s[8:11], s57 offen sc1\n"
    "v_mfma_f32_16x16x32_bf16 v[104:107], a[104:107], a[188:191], v[104:107]\n"
    "v_mfma_f32_16x16x32_bf16 v[104:107], a[72:75], a[172:175], v[104:107]\n"
    "v_mfma_f32_16x16x32_bf16 v[100:103], a[68:71], a[172:175], v[100:103]\n"
    "v_mfma_f32_16x16x32_bf16 v[100:103], a[100:103], a[188:191], v[100:103]\n"
    "v_mfma_f32_16x16x32_bf16 v[96:99], a[96:99], a[188:191], v[96:99]\n"
    "v_mfma_f32_16x16x32_bf16 v[96:99], a[64:67], a[172:175], v[96:99]\n"
    "s_waitcnt vmcnt(30) lgkmcnt(0)\n"
    "s_barrier\n"
    "v_mfma_f32_16x16x32_bf16 v[0:3], a[0:3], a[192:195], v[0:3]\n"
    "s_add_u32 s50, s45, 1048576\n"
    "ds_read_b64_tr_b16 a[64:65], v136 offset:49152\n"
    "v_mfma_f32_16x16x32_bf16 v[0:3], a[32:35], a[208:211], v[0:3]\n"
    "s_add_u32 m0, s44, 122880\n"
    "ds_read_b64_tr_b16 a[66:67], v136 offset:53248\n"
    "v_mfma_f32_16x16x32_bf16 v[4:7], a[36:39], a[208:211], v[4:7]\n"
    "buffer_load_dwordx4 v132, s[40:43], s50 offen lds\n"
    "ds_read_b64_tr_b16 a[68:69], v137 offset:49152\n"
    "v_mfma_f32_16x16x32_bf16 v[4:7], a[4:7], a[192:195], v[4:7]\n"
    "ds_read_b64_tr_b16 a[70:71], v137 offset:53248\n"
    "ds_read_b64_tr_b16 a[72:73], v138 offset:49152\n"
    "v_mfma_f32_16x16x32_bf16 v[8:11], a[8:11], a[192:195], v[8:11]\n"
    "s_add_u32 s57, s46, 10240\n"
    "ds_read_b64_tr_b16 a[74:75], v138 offset:53248\n"
    "v_mfma_f32_16x16x32_bf16 v[8:11], a[40:43], a[208:211], v[8:11]\n"
    "buffer_load_dwordx4 a[160:163], v128, s[8:11], s57 offen sc1\n"
    "ds_read_b64_tr_b16 a[76:77], v139 offset:49152\n"
    "v_mfma_f32_16x16x32_bf16 v[12:15], a[44:47], a[208:211], v[12:15]\n"
    "ds_read_b64_tr_b16 a[78:79], v139 offset:53248\n"
    "ds_read_b64_tr_b16 a[80:81], v140 offset:49152\n"
    "v_mfma_f32_16x16x32_bf16 v[12:15], a[12:15], a[192:195], v[12:15]\n"
    "ds_read_b64_tr_b16 a[82:83], v140 offset:53248\n"
    "ds_read_b64_tr_b16 a[84:85], v141 offset:49152\n"
    "v_mfma_f32_16x16x32_bf16 v[16:19], a[16:19], a[192:195], v[16:19]\n"
    "ds_read_b64_tr_b16 a[86:87], v141 offset:53248\n"
    "ds_read_b64_tr_b16 a[88:89], v142 offset:49152\n"
    "v_mfma_f32_16x16x32_bf16 v[16:19], a[48:51], a[208:211], v[16:19]\n"
    "buffer_load_dwordx4 a[164:167], v129, s[8:11], s57 offen sc1\n"
    "ds_read_b64_tr_b16 a[90:91], v142 offset:53248\n"
    "v_mfma_f32_16x16x32_bf16 v[20:23], a[52:55], a[208:211], v[20:23]\n"
    "ds_read_b64_tr_b16 a[92:93], v143 offset:49152\n"
    "ds_read_b64_tr_b16 a[94:95], v143 offset:53248\n"
    "v_mfma_f32_16x16x32_bf16 v[20:23], a[20:23], a[192:195], v[20:23]\n"
    "ds_read_b64_tr_b16 a[96:97], v136 offset:57344\n"
    "ds_read_b64_tr_b16 a[98:99], v136 offset:61440\n"
    "v_mfma_f32_16x16x32_bf16 v[24:27], a[24:27], a[192:195], v[24:27]\n"
    "s_add_u32 m0, s44, 123904\n"
    "ds_read_b64_tr_b16 a[100:101], v137 offset:57344\n"
    "v_mfma_f32_16x16x32_bf16 v[24:27], a[56:59], a[208:211], v[24:27]\n"
    "buffer_load_dwordx4 v133, s[40:43], s50 offen lds\n"
    "ds_read_b64_tr_b16 a[102:103], v137 offset:61440\n"
    "v_mfma_f32_16x16x32_bf16 v[28:31], a[60:63], a[208:211], v[28:31]\n"
    "ds_read_b64_tr_b16 a[104:105], v138 offset:57344\n"
    "ds_read_b64_tr_b16 a[106:107], v138 offset:61440\n"
    "v_mfma_f32_16x16x32_bf16 v[28:31], a[28:31], a[192:195], v[28:31]\n"
    "ds_read_b64_tr_b16 a[108:109], v139 offset:57344\n"
    "ds_read_b64_tr_b16 a[110:111], v139 offset:61440\n"
    "v_mfma_f32_16x16x32_bf16 v[60:63], a[28:31], a[196:199], v[60:63]\n"
    "ds_read_b64_tr_b16 a[112:113], v140 offset:57344\n"
    "ds_read_b64_tr_b16 a[114:115], v140 offset:61440\n"
    "v_mfma_f32_16x16x32_bf16 v[60:63], a[60:63], a[212:215], v[60:63]\n"
    "buffer_load_dwordx4 a[168:171], v130, s[8:11], s57 offen sc1\n"
    "ds_read_b64_tr_b16 a[116:117], v141 offset:57344\n"
    "v_mfma_f32_16x16x32_bf16 v[56:59], a[56:59], a[212:215], v[56:59]\n"
    "ds_read_b64_tr_b16 a[118:119], v141 offset:61440\n"
    "ds_read_b64_tr_b16 a[120:121], v142 offset:57344\n"
    "v_mfma_f32_16x16x32_bf16 v[56:59], a[24:27], a[196:199], v[56:59]\n"
    "s_add_u32 s50, s60, 983040\n"
    "ds_read_b64_tr_b16 a[122:123], v142 offset:61440\n"
    "v_mfma_f32_16x16x32_bf16 v[52:55], a[20:23], a[196:199], v[52:55]\n"
    "s_add_u32 m0, s44, 0\n"
    "ds_read_b64_tr_b16 a[124:125], v143 offset:57344\n"
    "v_mfma_f32_16x16x32_bf16 v[52:55], a[52:55], a[212:215], v[52:55]\n"
    "buffer_load_dwordx4 v134, s[40:43], s50 offen lds\n"
    "ds_read_b64_tr_b16 a[126:127], v143 offset:61440\n"
    "v_mfma_f32_16x16x32_bf16 v[48:51], a[48:51], a[212:215], v[48:51]\n"
    "v_mfma_f32_16x16x32_bf16 v[48:51], a[16:19], a[196:199], v[48:51]\n"
    "v_mfma_f32_16x16x32_bf16 v[44:47], a[12:15], a[196:199], v[44:47]\n"
    "v_mfma_f32_16x16x32_bf16 v[44:47], a[44:47], a[212:215], v[44:47]\n"
    "buffer_load_dwordx4 a[172:175], v131, s[8:11], s57 offen sc1\n"
    "v_mfma_f32_16x16x32_bf16 v[40:43], a[40:43], a[212:215], v[40:43]\n"
    "v_mfma_f32_16x16x32_bf16 v[40:43], a[8:11], a[196:199], v[40:43]\n"
    "v_mfma_f32_16x16x32_bf16 v[36:39], a[4:7], a[196:199], v[36:39]\n"
    "s_add_u32 m0, s44, 1024\n"
    "v_mfma_f32_16x16x32_bf16 v[36:39], a[36:39], a[212:215], v[36:39]\n"
    "buffer_load_dwordx4 v135, s[40:43], s50 offen lds\n"
    "v_mfma_f32_16x16x32_bf16 v[32:35], a[32:35], a[212:215], v[32:35]\n"
    "v_mfma_f32_16x16x32_bf16 v[32:35], a[0:3], a[196:199], v[32:35]\n"
    "v_mfma_f32_16x16x32_bf16 v[64:67], a[0:3], a[200:203], v[64:67]\n"
    "s_add_u32 s57, s46, 11264\n"
    "v_mfma_f32_16x16x32_bf16 v[64:67], a[32:35], a[216:219], v[64:67]\n"
    "buffer_load_dwordx4 a[176:179], v128, s[8:11], s57 offen sc1\n"
    "v_mfma_f32_16x16x32_bf16 v[68:71], a[36:39], a[216:219], v[68:71]\n"
    "v_mfma_f32_16x16x32_bf16 v[68:71], a[4:7], a[200:203], v[68:71]\n"
    "s_add_u32 s50, s61, 1015808\n"
    "v_mfma_f32_16x16x32_bf16 v[72:75], a[8:11], a[200:203], v[72:75]\n"
    "s_add_u32 m0, s44, 8192\n"
    "v_mfma_f32_16x16x32_bf16 v[72:75], a[40:43], a[216:219], v[72:75]\n"
    "buffer_load_dwordx4 v134, s[40:43], s50 offen lds\n"
    "v_mfma_f32_16x16x32_bf16 v[76:79], a[44:47], a[216:219], v[76:79]\n"
    "v_mfma_f32_16x16x32_bf16 v[76:79], a[12:15], a[200:203], v[76:79]\n"
    "v_mfma_f32_16x16x32_bf16 v[80:83], a[16:19], a[200:203], v[80:83]\n"
    "v_mfma_f32_16x16x32_bf16 v[80:83], a[48:51], a[216:219], v[80:83]\n"
    "buffer_load_dwordx4 a[180:183], v129, s[8:11], s57 offen sc1\n"
    "v_mfma_f32_16x16x32_bf16 v[84:87], a[52:55], a[216:219], v[84:87]\n"
    "v_mfma_f32_16x16x32_bf16 v[84:87], a[20:23], a[200:203], v[84:87]\n"
    "v_mfma_f32_16x16x32_bf16 v[88:91], a[24:27], a[200:203], v[88:91]\n"
    "s_add_u32 m0, s44, 9216\n"
    "v_mfma_f32_16x16x32_bf16 v[88:91], a[56:59], a[216:219], v[88:91]\n"
    "buffer_load_dwordx4 v135, s[40:43], s50 offen lds\n"
    "v_mfma_f32_16x16x32_bf16 v[92:95], a[60:63], a[216:219], v[92:95]\n"
    "v_mfma_f32_16x16x32_bf16 v[92:95], a[28:31], a[200:203], v[92:95]\n"
    "v_mfma_f32_16x16x32_bf16 v[124:127], a[28:31], a[204:207], v[124:127]\n"
    "v_mfma_f32_16x16x32_bf16 v[124:127], a[60:63], a[220:223], v[124:127]\n"
    "buffer_load_dwordx4 a[184:187], v130, s[8:11], s57 offen sc1\n"
    "v_mfma_f32_16x16x32_bf16 v[120:123], a[56:59], a[220:223], v[120:123]\n"
    "v_mfma_f32_16x16x32_bf16 v[120:123], a[24:27], a[204:207], v[120:123]\n"
    "s_add_u32 s50, s45, 1146880\n"
    "v_mfma_f32_16x16x32_bf16 v[116:119], a[20:23], a[204:207], v[116:119]\n"
    "s_add_u32 m0, s44, 16384\n"
    "v_mfma_f32_16x16x32_bf16 v[116:119], a[52:55], a[220:223], v[116:119]\n"
    "buffer_load_dwordx4 v132, s[40:43], s50 offen lds\n"
    "v_mfma_f32_16x16x32_bf16 v[112:115], a[48:51], a[220:223], v[112:115]\n"
    "v_mfma_f32_16x16x32_bf16 v[112:115], a[16:19], a[204:207], v[112:115]\n"
    "v_mfma_f32_16x16x32_bf16 v[108:111], a[12:15], a[204:207], v[108:111]\n"
    "v_mfma_f32_16x16x32_bf16 v[108:111], a[44:47], a[220:223], v[108:111]\n"
    "buffer_load_dwordx4 a[188:191], v131, s[8:11], s57 offen sc1\n"
    "v_mfma_f32_16x16x32_bf16 v[104:107], a[40:43], a[220:223], v[104:107]\n"
    "v_mfma_f32_16x16x32_bf16 v[104:107], a[8:11], a[204:207], v[104:107]\n"
    "v_mfma_f32_16x16x32_bf16 v[100:103], a[4:7], a[204:207], v[100:103]\n"
    "v_mfma_f32_16x16x32_bf16 v[100:103], a[36:39], a[220:223], v[100:103]\n"
    "v_mfma_f32_16x16x32_bf16 v[96:99], a[32:35], a[220:223], v[96:99]\n"
    "v_mfma_f32_16x16x32_bf16 v[96:99], a[0:3], a[204:207], v[96:99]\n"
    "s_waitcnt vmcnt(30) lgkmcnt(0)\n"
    "s_barrier\n"
    "v_mfma_f32_16x16x32_bf16 v[0:3], a[64:67], a[224:227], v[0:3]\n"
    "ds_read_b64_tr_b16 a[0:1], v236 offset:0\n"
    "ds_read_b64_tr_b16 a[2:3], v236 offset:4096\n"
    "v_mfma_f32_16x16x32_bf16 v[0:3], a[96:99], a[240:243], v[0:3]\n"
    "s_add_u32 m0, s44, 17408\n"
    "ds_read_b64_tr_b16 a[4:5], v237 offset:0\n"
    "v_mfma_f32_16x16x32_bf16 v[4:7], a[100:103], a[240:243], v[4:7]\n"
    "buffer_load_dwordx4 v133, s[40:43], s50 offen lds\n"
    "ds_read_b64_tr_b16 a[6:7], v237 offset:4096\n"
    "v_mfma_f32_16x16x32_bf16 v[4:7], a[68:71], a[224:227], v[4:7]\n"
    "ds_read_b64_tr_b16 a[8:9], v238 offset:0\n"
    "ds_read_b64_tr_b16 a[10:11], v238 offset:4096\n"
    "v_mfma_f32_16x16x32_bf16 v[8:11], a[72:75], a[224:227], v[8:11]\n"
    "s_add_u32 s57, s46, 12288\n"
    "ds_read_b64_tr_b16 a[12:13], v239 offset:0\n"
    "v_mfma_f32_16x16x32_bf16 v[8:11], a[104:107], a[240:243], v[8:11]\n"
    "buffer_load_dwordx4 a[192:195], v128, s[8:11], s57 offen sc1\n"
    "ds_read_b64_tr_b16 a[14:15], v239 offset:4096\n"
    "v_mfma_f32_16x16x32_bf16 v[12:15], a[108:111], a[240:243], v[12:15]\n"
    "ds_read_b64_tr_b16 a[16:17], v240 offset:0\n"
    "ds_read_b64_tr_b16 a[18:19], v240 offset:4096\n"
    "v_mfma_f32_16x16x32_bf16 v[12:15], a[76:79], a[224:227], v[12:15]\n"
    "ds_read_b64_tr_b16 a[20:21], v241 offset:0\n"
    "ds_read_b64_tr_b16 a[22:23], v241 offset:4096\n"
    "v_mfma_f32_16x16x32_bf16 v[16:19], a[80:83], a[224:227], v[16:19]\n"
    "ds_read_b64_tr_b16 a[24:25], v242 offset:0\n"
    "ds_read_b64_tr_b16 a[26:27], v242 offset:4096\n"
    "v_mfma_f32_16x16x32_bf16 v[16:19], a[112:115], a[240:243], v[16:19]\n"
    "buffer_load_dwordx4 a[196:199], v129, s[8:11], s57 offen sc1\n"
    "ds_read_b64_tr_b16 a[28:29], v243 offset:0\n"
    "v_mfma_f32_16x16x32_bf16 v[20:23], a[116:119], a[240:243], v[20:23]\n"
    "ds_read_b64_tr_b16 a[30:31], v243 offset:4096\n"
    "ds_read_b64_tr_b16 a[32:33], v236 offset:8192\n"
    "v_mfma_f32_16x16x32_bf16 v[20:23], a[84:87], a[224:227], v[20:23]\n"
    "s_add_u32 s50, s45, 1277952\n"
    "ds_read_b64_tr_b16 a[34:35], v236 offset:12288\n"
    "v_mfma_f32_16x16x32_bf16 v[24:27], a[88:91], a[224:227], v[24:27]\n"
    "s_add_u32 m0, s44, 24576\n"
    "ds_read_b64_tr_b16 a[36:37], v237 offset:8192\n"
    "v_mfma_f32_16x16x32_bf16 v[24:27], a[120:123], a[240:243], v[24:27]\n"
    "buffer_load_dwordx4 v132, s[40:43], s50 offen lds\n"
    "ds_read_b64_tr_b16 a[38:39], v237 offset:12288\n"
    "v_mfma_f32_16x16x32_bf16 v[28:31], a[124:127], a[240:243], v[28:31]\n"
    "ds_read_b64_tr_b16 a[40:41], v238 offset:8192\n"
    "ds_read_b64_tr_b16 a[42:43], v238 offset:12288\n"
    "v_mfma_f32_16x16x32_bf16 v[28:31], a[92:95], a[224:227], v[28:31]\n"
    "ds_read_b64_tr_b16 a[44:45], v239 offset:8192\n"
    "ds_read_b64_tr_b16 a[46:47], v239 offset:12288\n"
    "v_mfma_f32_16x16x32_bf16 v[60:63], a[92:95], a[228:231], v[60:63]\n"
    "ds_read_b64_tr_b16 a[48:49], v240 offset:8192\n"
    "ds_read_b64_tr_b16 a[50:51], v240 offset:12288\n"
    "v_mfma_f32_16x16x32_bf16 v[60:63], a[124:127], a[244:247], v[60:63]\n"
    "buffer_load_dwordx4 a[200:203], v130, s[8:11], s57 offen sc1\n"
    "ds_read_b64_tr_b16 a[52:53], v241 offset:8192\n"
    "v_mfma_f32_16x16x32_bf16 v[56:59], a[120:123], a[244:247], v[56:59]\n"
    "ds_read_b64_tr_b16 a[54:55], v241 offset:12288\n"
    "ds_read_b64_tr_b16 a[56:57], v242 offset:8192\n"
    "v_mfma_f32_16x16x32_bf16 v[56:59], a[88:91], a[228:231], v[56:59]\n"
    "ds_read_b64_tr_b16 a[58:59], v242 offset:12288\n"
    "ds_read_b64_tr_b16 a[60:61], v243 offset:8192\n"
    "v_mfma_f32_16x16x32_bf16 v[52:55], a[84:87], a[228:231], v[52:55]\n"
    "s_add_u32 m0, s44, 25600\n"
    "ds_read_b64_tr_b16 a[62:63], v243 offset:12288\n"
    "v_mfma_f32_16x16x32_bf16 v[52:55], a[116:119], a[244:247], v[52:55]\n"
    "buffer_load_dwordx4 v133, s[40:43], s50 offen lds\n"
    "v_mfma_f32_16x16x32_bf16 v[48:51], a[112:115], a[244:247], v[48:51]\n"
    "v_mfma_f32_16x16x32_bf16 v[48:51], a[80:83], a[228:231], v[48:51]\n"
    "v_mfma_f32_16x16x32_bf16 v[44:47], a[76:79], a[228:231], v[44:47]\n"
    "v_mfma_f32_16x16x32_bf16 v[44:47], a[108:111], a[244:247], v[44:47]\n"
    "buffer_load_dwordx4 a[204:207], v131, s[8:11], s57 offen sc1\n"
    "v_mfma_f32_16x16x32_bf16 v[40:43], a[104:107], a[244:247], v[40:43]\n"
    "v_mfma_f32_16x16x32_bf16 v[40:43], a[72:75], a[228:231], v[40:43]\n"
    "s_add_u32 s50, s60, 1212416\n"
    "v_mfma_f32_16x16x32_bf16 v[36:39], a[68:71], a[228:231], v[36:39]\n"
    "s_add_u32 m0, s44, 32768\n"
    "v_mfma_f32_16x16x32_bf16 v[36:39], a[100:103], a[244:247], v[36:39]\n"
    "buffer_load_dwordx4 v134, s[40:43], s50 offen lds\n"
    "v_mfma_f32_16x16x32_bf16 v[32:35], a[96:99], a[244:247], v[32:35]\n"
    "v_mfma_f32_16x16x32_bf16 v[32:35], a[64:67], a[228:231], v[32:35]\n"
    "v_mfma_f32_16x16x32_bf16 v[64:67], a[64:67], a[232:235], v[64:67]\n"
    "s_add_u32 s57, s46, 13312\n"
    "v_mfma_f32_16x16x32_bf16 v[64:67], a[96:99], a[248:251], v[64:67]\n"
    "buffer_load_dwordx4 a[208:211], v128, s[8:11], s57 offen sc1\n"
    "v_mfma_f32_16x16x32_bf16 v[68:71], a[100:103], a[248:251], v[68:71]\n"
    "v_mfma_f32_16x16x32_bf16 v[68:71], a[68:71], a[232:235], v[68:71]\n"
    "v_mfma_f32_16x16x32_bf16 v[72:75], a[72:75], a[232:235], v[72:75]\n"
    "s_add_u32 m0, s44, 33792\n"
    "v_mfma_f32_16x16x32_bf16 v[72:75], a[104:107], a[248:251], v[72:75]\n"
    "buffer_load_dwordx4 v135, s[40:43], s50 offen lds\n"
    "v_mfma_f32_16x16x32_bf16 v[76:79], a[108:111], a[248:251], v[76:79]\n"
    "v_mfma_f32_16x16x32_bf16 v[76:79], a[76:79], a[232:235], v[76:79]\n"
    "v_mfma_f32_16x16x32_bf16 v[80:83], a[80:83], a[232:235], v[80:83]\n"
    "v_mfma_f32_16x16x32_bf16 v[80:83], a[112:115], a[248:251], v[80:83]\n"
    "buffer_load_dwordx4 a[212:215], v129, s[8:11], s57 offen sc1\n"
    "v_mfma_f32_16x16x32_bf16 v[84:87], a[116:119], a[248:251], v[84:87]\n"
    "v_mfma_f32_16x16x32_bf16 v[84:87], a[84:87], a[232:235], v[84:87]\n"
    "s_add_u32 s50, s45, 1376256\n"
    "v_mfma_f32_16x16x32_bf16 v[88:91], a[88:91], a[232:235], v[88:91]\n"
    "s_add_u32 m0, s44, 40960\n"
    "v_mfma_f32_16x16x32_bf16 v[88:91], a[120:123], a[248:251], v[88:91]\n"
    "buffer_load_dwordx4 v132, s[40:43], s50 offen lds\n"
    "v_mfma_f32_16x16x32_bf16 v[92:95], a[124:127], a[248:251], v[92:95]\n"
    "v_mfma_f32_16x16x32_bf16 v[92:95], a[92:95], a[232:235], v[92:95]\n"
    "v_mfma_f32_16x16x32_bf16 v[124:127], a[92:95], a[236:239], v[124:127]\n"
    "v_mfma_f32_16x16x32_bf16 v[124:127], a[124:127], a[252:255], v[124:127]\n"
    "buffer_load_dwordx4 a[216:219], v130, s[8:11], s57 offen sc1\n"
    "v_mfma_f32_16x16x32_bf16 v[120:123], a[120:123], a[252:255], v[120:123]\n"
    "v_mfma_f32_16x16x32_bf16 v[120:123], a[88:91], a[236:239], v[120:123]\n"
    "v_mfma_f32_16x16x32_bf16 v[116:119], a[84:87], a[236:239], v[116:119]\n"
    "s_add_u32 m0, s44, 41984\n"
    "v_mfma_f32_16x16x32_bf16 v[116:119], a[116:119], a[252:255], v[116:119]\n"
    "buffer_load_dwordx4 v133, s[40:43], s50 offen lds\n"
    "v_mfma_f32_16x16x32_bf16 v[112:115], a[112:115], a[252:255], v[112:115]\n"
    "v_mfma_f32_16x16x32_bf16 v[112:115], a[80:83], a[236:239], v[112:115]\n"
    "v_mfma_f32_16x16x32_bf16 v[108:111], a[76:79], a[236:239], v[108:111]\n"
    "v_mfma_f32_16x16x32_bf16 v[108:111], a[108:111], a[252:255], v[108:111]\n"
    "buffer_load_dwordx4 a[220:223], v131, s[8:11], s57 offen sc1\n"
    "v_mfma_f32_16x16x32_bf16 v[104:107], a[104:107], a[252:255], v[104:107]\n"
    "v_mfma_f32_16x16x32_bf16 v[104:107], a[72:75], a[236:239], v[104:107]\n"
    "v_mfma_f32_16x16x32_bf16 v[100:103], a[68:71], a[236:239], v[100:103]\n"
    "v_mfma_f32_16x16x32_bf16 v[100:103], a[100:103], a[252:255], v[100:103]\n"
    "v_mfma_f32_16x16x32_bf16 v[96:99], a[96:99], a[252:255], v[96:99]\n"
    "v_mfma_f32_16x16x32_bf16 v[96:99], a[64:67], a[236:239], v[96:99]\n"
    "s_waitcnt vmcnt(30) lgkmcnt(0)\n"
    "s_barrier\n"
    "v_mfma_f32_16x16x32_bf16 v[0:3], a[0:3], a[128:131], v[0:3]\n"
    "s_add_u32 s50, s45, 1507328\n"
    "ds_read_b64_tr_b16 a[64:65], v236 offset:16384\n"
    "v_mfma_f32_16x16x32_bf16 v[0:3], a[32:35], a[144:147], v[0:3]\n"
    "s_add_u32 m0, s44, 49152\n"
    "ds_read_b64_tr_b16 a[66:67], v236 offset:20480\n"
    "v_mfma_f32_16x16x32_bf16 v[4:7], a[36:39], a[144:147], v[4:7]\n"
    "buffer_load_dwordx4 v132, s[40:43], s50 offen lds\n"
    "ds_read_b64_tr_b16 a[68:69], v237 offset:16384\n"
    "v_mfma_f32_16x16x32_bf16 v[4:7], a[4:7], a[128:131], v[4:7]\n"
    "s_add_u32 s57, s46, 14336\n"
    "ds_read_b64_tr_b16 a[70:71], v237 offset:20480\n"
    "v_mfma_f32_16x16x32_bf16 v[8:11], a[8:11], a[128:131], v[8:11]\n"
    "buffer_load_dwordx4 a[224:227], v128, s[8:11], s57 offen sc1\n"
    "ds_read_b64_tr_b16 a[72:73], v238 offset:16384\n"
    "v_mfma_f32_16x16x32_bf16 v[8:11], a[40:43], a[144:147], v[8:11]\n"
    "ds_read_b64_tr_b16 a[74:75], v238 offset:20480\n"
    "ds_read_b64_tr_b16 a[76:77], v239 offset:16384\n"
    "v_mfma_f32_16x16x32_bf16 v[12:15], a[44:47], a[144:147], v[12:15]\n"
    "ds_read_b64_tr_b16 a[78:79], v239 offset:20480\n"
    "ds_read_b64_tr_b16 a[80:81], v240 offset:16384\n"
    "v_mfma_f32_16x16x32_bf16 v[12:15], a[12:15], a[128:131], v[12:15]\n"
    "buffer_load_dwordx4 a[228:231], v129, s[8:11], s57 offen sc1\n"
    "ds_read_b64_tr_b16 a[82:83], v240 offset:20480\n"
    "v_mfma_f32_16x16x32_bf16 v[16:19], a[16:19], a[128:131], v[16:19]\n"
    "ds_read_b64_tr_b16 a[84:85], v241 offset:16384\n"
    "ds_read_b64_tr_b16 a[86:87], v241 offset:20480\n"
    "v_mfma_f32_16x16x32_bf16 v[16:19], a[48:51], a[144:147], v[16:19]\n"
    "buffer_load_dwordx4 a[232:235], v130, s[8:11], s57 offen sc1\n"
    "ds_read_b64_tr_b16 a[88:89], v242 offset:16384\n"
    "v_mfma_f32_16x16x32_bf16 v[20:23], a[52:55], a[144:147], v[20:23]\n"
    "ds_read_b64_tr_b16 a[90:91], v242 offset:20480\n"
    "ds_read_b64_tr_b16 a[92:93], v243 offset:16384\n"
    "v_mfma_f32_16x16x32_bf16 v[20:23], a[20:23], a[128:131], v[20:23]\n"
    "s_add_u32 m0, s44, 50176\n"
    "ds_read_b64_tr_b16 a[94:95], v243 offset:20480\n"
    "v_mfma_f32_16x16x32_bf16 v[24:27], a[24:27], a[128:131], v[24:27]\n"
    "buffer_load_dwordx4 v133, s[40:43], s50 offen lds\n"
    "ds_read_b64_tr_b16 a[96:97], v236 offset:24576\n"
    "v_mfma_f32_16x16x32_bf16 v[24:27], a[56:59], a[144:147], v[24:27]\n"
    "ds_read_b64_tr_b16 a[98:99], v236 offset:28672\n"
    "ds_read_b64_tr_b16 a[100:101], v237 offset:24576\n"
    "v_mfma_f32_16x16x32_bf16 v[28:31], a[60:63], a[144:147], v[28:31]\n"
    "buffer_load_dwordx4 a[236:239], v131, s[8:11], s57 offen sc1\n"
    "ds_read_b64_tr_b16 a[102:103], v237 offset:28672\n"
    "v_mfma_f32_16x16x32_bf16 v[28:31], a[28:31], a[128:131], v[28:31]\n"
    "ds_read_b64_tr_b16 a[104:105], v238 offset:24576\n"
    "ds_read_b64_tr_b16 a[106:107], v238 offset:28672\n"
    "v_mfma_f32_16x16x32_bf16 v[60:63], a[28:31], a[132:135], v[60:63]\n"
    "s_add_u32 s57, s46, 15360\n"
    "ds_read_b64_tr_b16 a[108:109], v239 offset:24576\n"
    "v_mfma_f32_16x16x32_bf16 v[60:63], a[60:63], a[148:151], v[60:63]\n"
    "buffer_load_dwordx4 a[240:243], v128, s[8:11], s57 offen sc1\n"
    "s_add_u32 s50, s60, 1441792\n"
    "v_mfma_f32_16x16x32_bf16 v[56:59], a[56:59], a[148:151], v[56:59]\n"
    "s_add_u32 m0, s44, 57344\n"
    "ds_read_b64_tr_b16 a[110:111], v239 offset:28672\n"
    "v_mfma_f32_16x16x32_bf16 v[56:59], a[24:27], a[132:135], v[56:59]\n"
    "buffer_load_dwordx4 v134, s[40:43], s50 offen lds\n"
    "ds_read_b64_tr_b16 a[112:113], v240 offset:24576\n"
    "v_mfma_f32_16x16x32_bf16 v[52:55], a[20:23], a[132:135], v[52:55]\n"
    "ds_read_b64_tr_b16 a[114:115], v240 offset:28672\n"
    "ds_read_b64_tr_b16 a[116:117], v241 offset:24576\n"
    "v_mfma_f32_16x16x32_bf16 v[52:55], a[52:55], a[148:151], v[52:55]\n"
    "ds_read_b64_tr_b16 a[118:119], v241 offset:28672\n"
    "ds_read_b64_tr_b16 a[120:121], v242 offset:24576\n"
    "v_mfma_f32_16x16x32_bf16 v[48:51], a[48:51], a[148:151], v[48:51]\n"
    "buffer_load_dwordx4 a[244:247], v129, s[8:11], s57 offen sc1\n"
    "ds_read_b64_tr_b16 a[122:123], v242 offset:28672\n"
    "v_mfma_f32_16x16x32_bf16 v[48:51], a[16:19], a[132:135], v[48:51]\n"
    "ds_read_b64_tr_b16 a[124:125], v243 offset:24576\n"
    "ds_read_b64_tr_b16 a[126:127], v243 offset:28672\n"
    "v_mfma_f32_16x16x32_bf16 v[44:47], a[12:15], a[132:135], v[44:47]\n"
    "v_mfma_f32_16x16x32_bf16 v[44:47], a[44:47], a[148:151], v[44:47]\n"
    "buffer_load_dwordx4 a[248:251], v130, s[8:11], s57 offen sc1\n"
    "v_mfma_f32_16x16x32_bf16 v[40:43], a[40:43], a[148:151], v[40:43]\n"
    "s_add_u32 m0, s44, 58368\n"
    "v_mfma_f32_16x16x32_bf16 v[40:43], a[8:11], a[132:135], v[40:43]\n"
    "buffer_load_dwordx4 v135, s[40:43], s50 offen lds\n"
    "v_mfma_f32_16x16x32_bf16 v[36:39], a[4:7], a[132:135], v[36:39]\n"
    "v_mfma_f32_16x16x32_bf16 v[36:39], a[36:39], a[148:151], v[36:39]\n"
    "v_mfma_f32_16x16x32_bf16 v[32:35], a[32:35], a[148:151], v[32:35]\n"
    "buffer_load_dwordx4 a[252:255], v131, s[8:11], s57 offen sc1\n"
    "v_mfma_f32_16x16x32_bf16 v[32:35], a[0:3], a[132:135], v[32:35]\n"
    "s_add_u32 s57, s46, 24576\n"
    "v_mfma_f32_16x16x32_bf16 v[64:67], a[0:3], a[136:139], v[64:67]\n"
    "buffer_load_dwordx4 v[172:175], v128, s[8:11], s57 offen sc1\n"
    "v_mfma_f32_16x16x32_bf16 v[64:67], a[32:35], a[152:155], v[64:67]\n"
    "v_mfma_f32_16x16x32_bf16 v[68:71], a[36:39], a[152:155], v[68:71]\n"
    "v_mfma_f32_16x16x32_bf16 v[68:71], a[4:7], a[136:139], v[68:71]\n"
    "buffer_load_dwordx4 v[176:179], v129, s[8:11], s57 offen sc1\n"
    "s_add_u32 s50, s61, 1474560\n"
    "v_mfma_f32_16x16x32_bf16 v[72:75], a[8:11], a[136:139], v[72:75]\n"
    "s_add_u32 m0, s44, 65536\n"
    "v_mfma_f32_16x16x32_bf16 v[72:75], a[40:43], a[152:155], v[72:75]\n"
    "buffer_load_dwordx4 v134, s[40:43], s50 offen lds\n"
    "v_mfma_f32_16x16x32_bf16 v[76:79], a[44:47], a[152:155], v[76:79]\n"
    "v_mfma_f32_16x16x32_bf16 v[76:79], a[12:15], a[136:139], v[76:79]\n"
    "v_mfma_f32_16x16x32_bf16 v[80:83], a[16:19], a[136:139], v[80:83]\n"
    "buffer_load_dwordx4 v[180:183], v130, s[8:11], s57 offen sc1\n"
    "v_mfma_f32_16x16x32_bf16 v[80:83], a[48:51], a[152:155], v[80:83]\n"
    "v_mfma_f32_16x16x32_bf16 v[84:87], a[52:55], a[152:155], v[84:87]\n"
    "v_mfma_f32_16x16x32_bf16 v[84:87], a[20:23], a[136:139], v[84:87]\n"
    "buffer_load_dwordx4 v[184:187], v131, s[8:11], s57 offen sc1\n"
    "v_mfma_f32_16x16x32_bf16 v[88:91], a[24:27], a[136:139], v[88:91]\n"
    "s_add_u32 m0, s44, 66560\n"
    "v_mfma_f32_16x16x32_bf16 v[88:91], a[56:59], a[152:155], v[88:91]\n"
    "buffer_load_dwordx4 v135, s[40:43], s50 offen lds\n"
    "v_mfma_f32_16x16x32_bf16 v[92:95], a[60:63], a[152:155], v[92:95]\n"
    "v_mfma_f32_16x16x32_bf16 v[92:95], a[28:31], a[136:139], v[92:95]\n"
    "s_add_u32 s57, s46, 25600\n"
    "v_mfma_f32_16x16x32_bf16 v[124:127], a[28:31], a[140:143], v[124:127]\n"
    "buffer_load_dwordx4 v[188:191], v128, s[8:11], s57 offen sc1\n"
    "v_mfma_f32_16x16x32_bf16 v[124:127], a[60:63], a[156:159], v[124:127]\n"
    "v_mfma_f32_16x16x32_bf16 v[120:123], a[56:59], a[156:159], v[120:123]\n"
    "buffer_load_dwordx4 v[192:195], v129, s[8:11], s57 offen sc1\n"
    "v_mfma_f32_16x16x32_bf16 v[120:123], a[24:27], a[140:143], v[120:123]\n"
    "s_add_u32 s50, s45, 1605632\n"
    "v_mfma_f32_16x16x32_bf16 v[116:119], a[20:23], a[140:143], v[116:119]\n"
    "s_add_u32 m0, s44, 73728\n"
    "v_mfma_f32_16x16x32_bf16 v[116:119], a[52:55], a[156:159], v[116:119]\n"
    "buffer_load_dwordx4 v132, s[40:43], s50 offen lds\n"
    "v_mfma_f32_16x16x32_bf16 v[112:115], a[48:51], a[156:159], v[112:115]\n"
    "v_mfma_f32_16x16x32_bf16 v[112:115], a[16:19], a[140:143], v[112:115]\n"
    "buffer_load_dwordx4 v[196:199], v130, s[8:11], s57 offen sc1\n"
    "v_mfma_f32_16x16x32_bf16 v[108:111], a[12:15], a[140:143], v[108:111]\n"
    "v_mfma_f32_16x16x32_bf16 v[108:111], a[44:47], a[156:159], v[108:111]\n"
    "v_mfma_f32_16x16x32_bf16 v[104:107], a[40:43], a[156:159], v[104:107]\n"
    "buffer_load_dwordx4 v[200:203], v131, s[8:11], s57 offen sc1\n"
    "v_mfma_f32_16x16x32_bf16 v[104:107], a[8:11], a[140:143], v[104:107]\n"
    "v_mfma_f32_16x16x32_bf16 v[100:103], a[4:7], a[140:143], v[100:103]\n"
    "v_mfma_f32_16x16x32_bf16 v[100:103], a[36:39], a[156:159], v[100:103]\n"
    "v_mfma_f32_16x16x32_bf16 v[96:99], a[32:35], a[156:159], v[96:99]\n"
    "v_mfma_f32_16x16x32_bf16 v[96:99], a[0:3], a[140:143], v[96:99]\n"
    "s_waitcnt vmcnt(38) lgkmcnt(0)\n"
    "s_barrier\n"
    "v_mfma_f32_16x16x32_bf16 v[0:3], a[64:67], a[160:163], v[0:3]\n"
    "ds_read_b64_tr_b16 a[0:1], v236 offset:32768\n"
    "ds_read_b64_tr_b16 a[2:3], v236 offset:36864\n"
    "v_mfma_f32_16x16x32_bf16 v[0:3], a[96:99], a[176:179], v[0:3]\n"
    "s_add_u32 m0, s44, 74752\n"
    "ds_read_b64_tr_b16 a[4:5], v237 offset:32768\n"
    "v_mfma_f32_16x16x32_bf16 v[4:7], a[100:103], a[176:179], v[4:7]\n"
    "buffer_load_dwordx4 v133, s[40:43], s50 offen lds\n"
    "ds_read_b64_tr_b16 a[6:7], v237 offset:36864\n"
    "v_mfma_f32_16x16x32_bf16 v[4:7], a[68:71], a[160:163], v[4:7]\n"
    "s_add_u32 s57, s46, 16384\n"
    "ds_read_b64_tr_b16 a[8:9], v238 offset:32768\n"
    "v_mfma_f32_16x16x32_bf16 v[8:11], a[72:75], a[160:163], v[8:11]\n"
    "buffer_load_dwordx4 a[128:131], v128, s[8:11], s57 offen sc1\n"
    "ds_read_b64_tr_b16 a[10:11], v238 offset:36864\n"
    "v_mfma_f32_16x16x32_bf16 v[8:11], a[104:107], a[176:179], v[8:11]\n"
    "ds_read_b64_tr_b16 a[12:13], v239 offset:32768\n"
    "ds_read_b64_tr_b16 a[14:15], v239 offset:36864\n"
    "v_mfma_f32_16x16x32_bf16 v[12:15], a[108:111], a[176:179], v[12:15]\n"
    "ds_read_b64_tr_b16 a[16:17], v240 offset:32768\n"
    "ds_read_b64_tr_b16 a[18:19], v240 offset:36864\n"
    "v_mfma_f32_16x16x32_bf16 v[12:15], a[76:79], a[160:163], v[12:15]\n"
    "buffer_load_dwordx4 a[132:135], v129, s[8:11], s57 offen sc1\n"
    "ds_read_b64_tr_b16 a[20:21], v241 offset:32768\n"
    "v_mfma_f32_16x16x32_bf16 v[16:19], a[80:83], a[160:163], v[16:19]\n"
    "ds_read_b64_tr_b16 a[22:23], v241 offset:36864\n"
    "ds_read_b64_tr_b16 a[24:25], v242 offset:32768\n"
    "v_mfma_f32_16x16x32_bf16 v[16:19], a[112:115], a[176:179], v[16:19]\n"
    "ds_read_b64_tr_b16 a[26:27], v242 offset:36864\n"
    "ds_read_b64_tr_b16 a[28:29], v243 offset:32768\n"
    "v_mfma_f32_16x16x32_bf16 v[20:23], a[116:119], a[176:179], v[20:23]\n"
    "buffer_load_dwordx4 a[136:139], v130, s[8:11], s57 offen sc1\n"
    "ds_read_b64_tr_b16 a[30:31], v243 offset:36864\n"
    "v_mfma_f32_16x16x32_bf16 v[20:23], a[84:87], a[160:163], v[20:23]\n"
    "ds_read_b64_tr_b16 a[32:33], v236 offset:40960\n"
    "ds_read_b64_tr_b16 a[34:35], v236 offset:45056\n"
    "v_mfma_f32_16x16x32_bf16 v[24:27], a[88:91], a[160:163], v[24:27]\n"
    "ds_read_b64_tr_b16 a[36:37], v237 offset:40960\n"
    "ds_read_b64_tr_b16 a[38:39], v237 offset:45056\n"
    "v_mfma_f32_16x16x32_bf16 v[24:27], a[120:123], a[176:179], v[24:27]\n"
    "buffer_load_dwordx4 a[140:143], v131, s[8:11], s57 offen sc1\n"
    "ds_read_b64_tr_b16 a[40:41], v238 offset:40960\n"
    "v_mfma_f32_16x16x32_bf16 v[28:31], a[124:127], a[176:179], v[28:31]\n"
    "s_add_u32 s50, s45, 1736704\n"
    "ds_read_b64_tr_b16 a[42:43], v238 offset:45056\n"
    "v_mfma_f32_16x16x32_bf16 v[28:31], a[92:95], a[160:163], v[28:31]\n"
    "s_add_u32 m0, s44, 81920\n"
    "ds_read_b64_tr_b16 a[44:45], v239 offset:40960\n"
    "v_mfma_f32_16x16x32_bf16 v[60:63], a[92:95], a[164:167], v[60:63]\n"
    "buffer_load_dwordx4 v132, s[40:43], s50 offen lds\n"
    "ds_read_b64_tr_b16 a[46:47], v239 offset:45056\n"
    "v_mfma_f32_16x16x32_bf16 v[60:63], a[124:127], a[180:183], v[60:63]\n"
    "s_add_u32 s57, s46, 17408\n"
    "ds_read_b64_tr_b16 a[48:49], v240 offset:40960\n"
    "v_mfma_f32_16x16x32_bf16 v[56:59], a[120:123], a[180:183], v[56:59]\n"
    "buffer_load_dwordx4 a[144:147], v128, s[8:11], s57 offen sc1\n"
    "ds_read_b64_tr_b16 a[50:51], v240 offset:45056\n"
    "v_mfma_f32_16x16x32_bf16 v[56:59], a[88:91], a[164:167], v[56:59]\n"
    "ds_read_b64_tr_b16 a[52:53], v241 offset:40960\n"
    "ds_read_b64_tr_b16 a[54:55], v241 offset:45056\n"
    "v_mfma_f32_16x16x32_bf16 v[52:55], a[84:87], a[164:167], v[52:55]\n"
    "ds_read_b64_tr_b16 a[56:57], v242 offset:40960\n"
    "ds_read_b64_tr_b16 a[58:59], v242 offset:45056\n"
    "v_mfma_f32_16x16x32_bf16 v[52:55], a[116:119], a[180:183], v[52:55]\n"
    "buffer_load_dwordx4 a[148:151], v129, s[8:11], s57 offen sc1\n"
    "ds_read_b64_tr_b16 a[60:61], v243 offset:40960\n"
    "v_mfma_f32_16x16x32_bf16 v[48:51], a[112:115], a[180:183], v[48:51]\n"
    "ds_read_b64_tr_b16 a[62:63], v243 offset:45056\n"
    "v_mfma_f32_16x16x32_bf16 v[48:51], a[80:83], a[164:167], v[48:51]\n"
    "v_mfma_f32_16x16x32_bf16 v[44:47], a[76:79], a[164:167], v[44:47]\n"
    "buffer_load_dwordx4 a[152:155], v130, s[8:11], s57 offen sc1\n"
    "v_mfma_f32_16x16x32_bf16 v[44:47], a[108:111], a[180:183], v[44:47]\n"
    "v_mfma_f32_16x16x32_bf16 v[40:43], a[104:107], a[180:183], v[40:43]\n"
    "s_add_u32 m0, s44, 82944\n"
    "v_mfma_f32_16x16x32_bf16 v[40:43], a[72:75], a[164:167], v[40:43]\n"
    "buffer_load_dwordx4 v133, s[40:43], s50 offen lds\n"
    "v_mfma_f32_16x16x32_bf16 v[36:39], a[68:71], a[164:167], v[36:39]\n"
    "v_mfma_f32_16x16x32_bf16 v[36:39], a[100:103], a[180:183], v[36:39]\n"
    "v_mfma_f32_16x16x32_bf16 v[32:35], a[96:99], a[180:183], v[32:35]\n"
    "buffer_load_dwordx4 a[156:159], v131, s[8:11], s57 offen sc1\n"
    "v_mfma_f32_16x16x32_bf16 v[32:35], a[64:67], a[164:167], v[32:35]\n"
    "s_add_u32 s57, s46, 26624\n"
    "v_mfma_f32_16x16x32_bf16 v[64:67], a[64:67], a[168:171], v[64:67]\n"
    "buffer_load_dwordx4 v[204:207], v128, s[8:11], s57 offen sc1\n"
    "v_mfma_f32_16x16x32_bf16 v[64:67], a[96:99], a[184:187], v[64:67]\n"
    "v_mfma_f32_16x16x32_bf16 v[68:71], a[100:103], a[184:187], v[68:71]\n"
    "v_mfma_f32_16x16x32_bf16 v[68:71], a[68:71], a[168:171], v[68:71]\n"
    "buffer_load_dwordx4 v[208:211], v129, s[8:11], s57 offen sc1\n"
    "v_mfma_f32_16x16x32_bf16 v[72:75], a[72:75], a[168:171], v[72:75]\n"
    "s_add_u32 s50, s60, 1671168\n"
    "v_mfma_f32_16x16x32_bf16 v[72:75], a[104:107], a[184:187], v[72:75]\n"
    "s_add_u32 m0, s44, 90112\n"
    "v_mfma_f32_16x16x32_bf16 v[76:79], a[108:111], a[184:187], v[76:79]\n"
    "buffer_load_dwordx4 v134, s[40:43], s50 offen lds\n"
    "v_mfma_f32_16x16x32_bf16 v[76:79], a[76:79], a[168:171], v[76:79]\n"
    "v_mfma_f32_16x16x32_bf16 v[80:83], a[80:83], a[168:171], v[80:83]\n"
    "v_mfma_f32_16x16x32_bf16 v[80:83], a[112:115], a[184:187], v[80:83]\n"
    "buffer_load_dwordx4 v[212:215], v130, s[8:11], s57 offen sc1\n"
    "v_mfma_f32_16x16x32_bf16 v[84:87], a[116:119], a[184:187], v[84:87]\n"
    "v_mfma_f32_16x16x32_bf16 v[84:87], a[84:87], a[168:171], v[84:87]\n"
    "S3_H1%=:\n"
    "s_cbranch_execz S3_R%=\n"
    "v_mfma_f32_16x16x32_bf16 v[88:91], a[88:91], a[168:171], v[88:91]\n"
    "buffer_load_dwordx4 v[216:219], v131, s[8:11], s57 offen sc1\n"
    "v_mfma_f32_16x16x32_bf16 v[88:91], a[120:123], a[184:187], v[88:91]\n"
    "s_add_u32 s57, s46, 27648\n"
    "v_mfma_f32_16x16x32_bf16 v[92:95], a[124:127], a[184:187], v[92:95]\n"
    "buffer_load_dwordx4 v[220:223], v128, s[8:11], s57 offen sc1\n"
    "v_mfma_f32_16x16x32_bf16 v[92:95], a[92:95], a[168:171], v[92:95]\n"
    "v_mfma_f32_16x16x32_bf16 v[124:127], a[92:95], a[172:175], v[124:127]\n"
    "s_add_u32 m0, s44, 91136\n"
    "v_mfma_f32_16x16x32_bf16 v[124:127], a[124:127], a[188:191], v[124:127]\n"
    "buffer_load_dwordx4 v135, s[40:43], s50 offen lds\n"
    "v_mfma_f32_16x16x32_bf16 v[120:123], a[120:123], a[188:191], v[120:123]\n"
    "v_mfma_f32_16x16x32_bf16 v[120:123], a[88:91], a[172:175], v[120:123]\n"
    "v_mfma_f32_16x16x32_bf16 v[116:119], a[84:87], a[172:175], v[116:119]\n"
    "buffer_load_dwordx4 v[224:227], v129, s[8:11], s57 offen sc1\n"
    "v_mfma_f32_16x16x32_bf16 v[116:119], a[116:119], a[188:191], v[116:119]\n"
    "v_mfma_f32_16x16x32_bf16 v[112:115], a[112:115], a[188:191], v[112:115]\n"
    "v_mfma_f32_16x16x32_bf16 v[112:115], a[80:83], a[172:175], v[112:115]\n"
    "buffer_load_dwordx4 v[228:231], v130, s[8:11], s57 offen sc1\n"
    "v_mfma_f32_16x16x32_bf16 v[108:111], a[76:79], a[172:175], v[108:111]\n"
    "v_mfma_f32_16x16x32_bf16 v[108:111], a[108:111], a[188:191], v[108:111]\n"
    "v_mfma_f32_16x16x32_bf16 v[104:107], a[104:107], a[188:191], v[104:107]\n"
    "buffer_load_dwordx4 v[232:235], v131, s[8:11], s57 offen sc1\n"
    "v_mfma_f32_16x16x32_bf16 v[104:107], a[72:75], a[172:175], v[104:107]\n"
    "v_mfma_f32_16x16x32_bf16 v[100:103], a[68:71], a[172:175], v[100:103]\n"
    "v_mfma_f32_16x16x32_bf16 v[100:103], a[100:103], a[188:191], v[100:103]\n"
    "v_mfma_f32_16x16x32_bf16 v[96:99], a[96:99], a[188:191], v[96:99]\n"
    "v_mfma_f32_16x16x32_bf16 v[96:99], a[64:67], a[172:175], v[96:99]\n"
    "s_waitcnt vmcnt(44) lgkmcnt(0)\n"
    "s_barrier\n"
    "v_mfma_f32_16x16x32_bf16 v[0:3], a[0:3], a[192:195], v[0:3]\n"
    "ds_read_b64_tr_b16 a[64:65], v236 offset:49152\n"
    "v_mfma_f32_16x16x32_bf16 v[0:3], a[32:35], a[208:211], v[0:3]\n"
    "s_add_u32 s57, s46, 18432\n"
    "v_mfma_f32_16x16x32_bf16 v[4:7], a[36:39], a[208:211], v[4:7]\n"
    "buffer_load_dwordx4 a[160:163], v128, s[8:11], s57 offen sc1\n"
    "v_mfma_f32_16x16x32_bf16 v[4:7], a[4:7], a[192:195], v[4:7]\n"
    "s_mov_b32 s56, 65536\n"
    "v_mfma_f32_16x16x32_bf16 v[8:11], a[8:11], a[192:195], v[8:11]\n"
    "ds_read_b64_tr_b16 a[66:67], v236 offset:53248\n"
    "v_mfma_f32_16x16x32_bf16 v[8:11], a[40:43], a[208:211], v[8:11]\n"
    "buffer_load_dwordx2 v[156:157], v152, s[8:11], 0 offen offset:0 sc1\n"
    "v_mfma_f32_16x16x32_bf16 v[12:15], a[44:47], a[208:211], v[12:15]\n"
    "ds_read_b64_tr_b16 a[68:69], v237 offset:49152\n"
    "v_mfma_f32_16x16x32_bf16 v[12:15], a[12:15], a[192:195], v[12:15]\n"
    "ds_read_b64_tr_b16 a[70:71], v237 offset:53248\n"
    "v_mfma_f32_16x16x32_bf16 v[16:19], a[16:19], a[192:195], v[16:19]\n"
    "ds_read_b64_tr_b16 a[72:73], v238 offset:49152\n"
    "v_mfma_f32_16x16x32_bf16 v[16:19], a[48:51], a[208:211], v[16:19]\n"
    "buffer_load_dwordx4 a[164:167], v129, s[8:11], s57 offen sc1\n"
    "v_mfma_f32_16x16x32_bf16 v[20:23], a[52:55], a[208:211], v[20:23]\n"
    "ds_read_b64_tr_b16 a[74:75], v238 offset:53248\n"
    "v_mfma_f32_16x16x32_bf16 v[20:23], a[20:23], a[192:195], v[20:23]\n"
    "buffer_load_dwordx2 v[158:159], v152, s[8:11], s56 offen offset:0 sc1\n"
    "v_mfma_f32_16x16x32_bf16 v[24:27], a[24:27], a[192:195], v[24:27]\n"
    "ds_read_b64_tr_b16 a[76:77], v239 offset:49152\n"
    "v_mfma_f32_16x16x32_bf16 v[24:27], a[56:59], a[208:211], v[24:27]\n"
    "ds_read_b64_tr_b16 a[78:79], v239 offset:53248\n"
    "v_mfma_f32_16x16x32_bf16 v[28:31], a[60:63], a[208:211], v[28:31]\n"
    "ds_read_b64_tr_b16 a[80:81], v240 offset:49152\n"
    "v_mfma_f32_16x16x32_bf16 v[28:31], a[28:31], a[192:195], v[28:31]\n"
    "ds_read_b64_tr_b16 a[82:83], v240 offset:53248\n"
    "v_mfma_f32_16x16x32_bf16 v[60:63], a[28:31], a[196:199], v[60:63]\n"
    "buffer_load_dwordx4 a[168:171], v130, s[8:11], s57 offen sc1\n"
    "v_mfma_f32_16x16x32_bf16 v[60:63], a[60:63], a[212:215], v[60:63]\n"
    "buffer_load_dwordx2 v[160:161], v152, s[8:11], 0 offen offset:128 sc1\n"
    "v_mfma_f32_16x16x32_bf16 v[56:59], a[56:59], a[212:215], v[56:59]\n"
    "ds_read_b64_tr_b16 a[84:85], v241 offset:49152\n"
    "v_mfma_f32_16x16x32_bf16 v[56:59], a[24:27], a[196:199], v[56:59]\n"
    "ds_read_b64_tr_b16 a[86:87], v241 offset:53248\n"
    "v_mfma_f32_16x16x32_bf16 v[52:55], a[20:23], a[196:199], v[52:55]\n"
    "ds_read_b64_tr_b16 a[88:89], v242 offset:49152\n"
    "v_mfma_f32_16x16x32_bf16 v[52:55], a[52:55], a[212:215], v[52:55]\n"
    "ds_read_b64_tr_b16 a[90:91], v242 offset:53248\n"
    "v_mfma_f32_16x16x32_bf16 v[48:51], a[48:51], a[212:215], v[48:51]\n"
    "ds_read_b64_tr_b16 a[92:93], v243 offset:49152\n"
    "v_mfma_f32_16x16x32_bf16 v[48:51], a[16:19], a[196:199], v[48:51]\n"
    "buffer_load_dwordx2 v[162:163], v152, s[8:11], s56 offen offset:128 sc1\n"
    "v_mfma_f32_16x16x32_bf16 v[44:47], a[12:15], a[196:199], v[44:47]\n"
    "buffer_load_dwordx4 a[172:175], v131, s[8:11], s57 offen sc1\n"
    "v_mfma_f32_16x16x32_bf16 v[44:47], a[44:47], a[212:215], v[44:47]\n"
    "ds_read_b64_tr_b16 a[94:95], v243 offset:53248\n"
    "v_mfma_f32_16x16x32_bf16 v[40:43], a[40:43], a[212:215], v[40:43]\n"
    "ds_read_b64_tr_b16 a[96:97], v236 offset:57344\n"
    "v_mfma_f32_16x16x32_bf16 v[40:43], a[8:11], a[196:199], v[40:43]\n"
    "ds_read_b64_tr_b16 a[98:99], v236 offset:61440\n"
    "v_mfma_f32_16x16x32_bf16 v[36:39], a[4:7], a[196:199], v[36:39]\n"
    "ds_read_b64_tr_b16 a[100:101], v237 offset:57344\n"
    "v_mfma_f32_16x16x32_bf16 v[36:39], a[36:39], a[212:215], v[36:39]\n"
    "buffer_load_dwordx2 v[164:165], v152, s[8:11], 0 offen offset:256 sc1\n"
    "v_mfma_f32_16x16x32_bf16 v[32:35], a[32:35], a[212:215], v[32:35]\n"
    "s_add_u32 s57, s46, 19456\n"
    "v_mfma_f32_16x16x32_bf16 v[32:35], a[0:3], a[196:199], v[32:35]\n"
    "buffer_load_dwordx4 a[176:179], v128, s[8:11], s57 offen sc1\n"
    "v_mfma_f32_16x16x32_bf16 v[64:67], a[0:3], a[200:203], v[64:67]\n"
    "ds_read_b64_tr_b16 a[102:103], v237 offset:61440\n"
    "v_mfma_f32_16x16x32_bf16 v[64:67], a[32:35], a[216:219], v[64:67]\n"
    "ds_read_b64_tr_b16 a[104:105], v238 offset:57344\n"
    "v_mfma_f32_16x16x32_bf16 v[68:71], a[36:39], a[216:219], v[68:71]\n"
    "ds_read_b64_tr_b16 a[106:107], v238 offset:61440\n"
    "v_mfma_f32_16x16x32_bf16 v[68:71], a[4:7], a[200:203], v[68:71]\n"
    "buffer_load_dwordx2 v[166:167], v152, s[8:11], s56 offen offset:256 sc1\n"
    "v_mfma_f32_16x16x32_bf16 v[72:75], a[8:11], a[200:203], v[72:75]\n"
    "ds_read_b64_tr_b16 a[108:109], v239 offset:57344\n"
    "v_mfma_f32_16x16x32_bf16 v[72:75], a[40:43], a[216:219], v[72:75]\n"
    "ds_read_b64_tr_b16 a[110:111], v239 offset:61440\n"
    "v_mfma_f32_16x16x32_bf16 v[76:79], a[44:47], a[216:219], v[76:79]\n"
    "buffer_load_dwordx4 a[180:183], v129, s[8:11], s57 offen sc1\n"
    "v_mfma_f32_16x16x32_bf16 v[76:79], a[12:15], a[200:203], v[76:79]\n"
    "ds_read_b64_tr_b16 a[112:113], v240 offset:57344\n"
    "v_mfma_f32_16x16x32_bf16 v[80:83], a[16:19], a[200:203], v[80:83]\n"
    "ds_read_b64_tr_b16 a[114:115], v240 offset:61440\n"
    "v_mfma_f32_16x16x32_bf16 v[80:83], a[48:51], a[216:219], v[80:83]\n"
    "buffer_load_dwordx2 v[168:169], v152, s[8:11], 0 offen offset:384 sc1\n"
    "v_mfma_f32_16x16x32_bf16 v[84:87], a[52:55], a[216:219], v[84:87]\n"
    "ds_read_b64_tr_b16 a[116:117], v241 offset:57344\n"
    "v_mfma_f32_16x16x32_bf16 v[84:87], a[20:23], a[200:203], v[84:87]\n"
    "ds_read_b64_tr_b16 a[118:119], v241 offset:61440\n"
    "v_mfma_f32_16x16x32_bf16 v[88:91], a[24:27], a[200:203], v[88:91]\n"
    "ds_read_b64_tr_b16 a[120:121], v242 offset:57344\n"
    "v_mfma_f32_16x16x32_bf16 v[88:91], a[56:59], a[216:219], v[88:91]\n"
    "ds_read_b64_tr_b16 a[122:123], v242 offset:61440\n"
    "v_mfma_f32_16x16x32_bf16 v[92:95], a[60:63], a[216:219], v[92:95]\n"
    "buffer_load_dwordx4 a[184:187], v130, s[8:11], s57 offen sc1\n"
    "v_mfma_f32_16x16x32_bf16 v[92:95], a[28:31], a[200:203], v[92:95]\n"
    "buffer_load_dwordx2 v[170:171], v152, s[8:11], s56 offen offset:384 sc1\n"
    "v_mfma_f32_16x16x32_bf16 v[124:127], a[28:31], a[204:207], v[124:127]\n"
    "ds_read_b64_tr_b16 a[124:125], v243 offset:57344\n"
    "v_mfma_f32_16x16x32_bf16 v[124:127], a[60:63], a[220:223], v[124:127]\n"
    "ds_read_b64_tr_b16 a[126:127], v243 offset:61440\n"
    "v_mfma_f32_16x16x32_bf16 v[120:123], a[56:59], a[220:223], v[120:123]\n"
    "v_mfma_f32_16x16x32_bf16 v[120:123], a[24:27], a[204:207], v[120:123]\n"
    "v_mfma_f32_16x16x32_bf16 v[116:119], a[20:23], a[204:207], v[116:119]\n"
    "v_mfma_f32_16x16x32_bf16 v[116:119], a[52:55], a[220:223], v[116:119]\n"
    "buffer_load_dwordx4 a[188:191], v131, s[8:11], s57 offen sc1\n"
    "v_mfma_f32_16x16x32_bf16 v[112:115], a[48:51], a[220:223], v[112:115]\n"
    "v_mfma_f32_16x16x32_bf16 v[112:115], a[16:19], a[204:207], v[112:115]\n"
    "v_mfma_f32_16x16x32_bf16 v[108:111], a[12:15], a[204:207], v[108:111]\n"
    "v_mfma_f32_16x16x32_bf16 v[108:111], a[44:47], a[220:223], v[108:111]\n"
    "v_mfma_f32_16x16x32_bf16 v[104:107], a[40:43], a[220:223], v[104:107]\n"
    "v_mfma_f32_16x16x32_bf16 v[104:107], a[8:11], a[204:207], v[104:107]\n"
    "v_mfma_f32_16x16x32_bf16 v[100:103], a[4:7], a[204:207], v[100:103]\n"
    "v_mfma_f32_16x16x32_bf16 v[100:103], a[36:39], a[220:223], v[100:103]\n"
    "v_mfma_f32_16x16x32_bf16 v[96:99], a[32:35], a[220:223], v[96:99]\n"
    "v_mfma_f32_16x16x32_bf16 v[96:99], a[0:3], a[204:207], v[96:99]\n"
    "s_waitcnt vmcnt(19) lgkmcnt(0)\n"
    "s_barrier\n"
    "v_mfma_f32_16x16x32_bf16 v[0:3], a[64:67], a[224:227], v[0:3]\n"
    "ds_read_b64_tr_b16 a[0:1], v138 offset:0\n"
    "v_mfma_f32_16x16x32_bf16 v[0:3], a[96:99], a[240:243], v[0:3]\n"
    "s_add_u32 s57, s46, 20480\n"
    "v_mfma_f32_16x16x32_bf16 v[4:7], a[100:103], a[240:243], v[4:7]\n"
    "buffer_load_dwordx4 a[192:195], v128, s[8:11], s57 offen sc1\n"
    "v_mfma_f32_16x16x32_bf16 v[4:7], a[68:71], a[224:227], v[4:7]\n"
    "ds_read_b64_tr_b16 a[2:3], v138 offset:4096\n"
    "v_mfma_f32_16x16x32_bf16 v[8:11], a[72:75], a[224:227], v[8:11]\n"
    "ds_read_b64_tr_b16 a[4:5], v139 offset:0\n"
    "v_mfma_f32_16x16x32_bf16 v[8:11], a[104:107], a[240:243], v[8:11]\n"
    "ds_read_b64_tr_b16 a[6:7], v139 offset:4096\n"
    "v_mfma_f32_16x16x32_bf16 v[12:15], a[108:111], a[240:243], v[12:15]\n"
    "ds_read_b64_tr_b16 a[8:9], v138 offset:8192\n"
    "v_mfma_f32_16x16x32_bf16 v[12:15], a[76:79], a[224:227], v[12:15]\n"
    "ds_read_b64_tr_b16 a[10:11], v138 offset:12288\n"
    "v_mfma_f32_16x16x32_bf16 v[16:19], a[80:83], a[224:227], v[16:19]\n"
    "ds_read_b64_tr_b16 a[12:13], v139 offset:8192\n"
    "v_mfma_f32_16x16x32_bf16 v[16:19], a[112:115], a[240:243], v[16:19]\n"
    "buffer_load_dwordx4 a[196:199], v129, s[8:11], s57 offen sc1\n"
    "v_mfma_f32_16x16x32_bf16 v[20:23], a[116:119], a[240:243], v[20:23]\n"
    "ds_read_b64_tr_b16 a[14:15], v139 offset:12288\n"
    "v_mfma_f32_16x16x32_bf16 v[20:23], a[84:87], a[224:227], v[20:23]\n"
    "ds_read_b64_tr_b16 a[16:17], v138 offset:16384\n"
    "v_mfma_f32_16x16x32_bf16 v[24:27], a[88:91], a[224:227], v[24:27]\n"
    "ds_read_b64_tr_b16 a[18:19], v138 offset:20480\n"
    "v_mfma_f32_16x16x32_bf16 v[24:27], a[120:123], a[240:243], v[24:27]\n"
    "ds_read_b64_tr_b16 a[20:21], v139 offset:16384\n"
    "v_mfma_f32_16x16x32_bf16 v[28:31], a[124:127], a[240:243], v[28:31]\n"
    "ds_read_b64_tr_b16 a[22:23], v139 offset:20480\n"
    "v_mfma_f32_16x16x32_bf16 v[28:31], a[92:95], a[224:227], v[28:31]\n"
    "ds_read_b64_tr_b16 a[24:25], v138 offset:24576\n"
    "v_mfma_f32_16x16x32_bf16 v[60:63], a[92:95], a[228:231], v[60:63]\n"
    "buffer_load_dwordx4 a[200:203], v130, s[8:11], s57 offen sc1\n"
    "v_mfma_f32_16x16x32_bf16 v[60:63], a[124:127], a[244:247], v[60:63]\n"
    "ds_read_b64_tr_b16 a[26:27], v138 offset:28672\n"
    "v_mfma_f32_16x16x32_bf16 v[56:59], a[120:123], a[244:247], v[56:59]\n"
    "ds_read_b64_tr_b16 a[28:29], v139 offset:24576\n"
    "v_mfma_f32_16x16x32_bf16 v[56:59], a[88:91], a[228:231], v[56:59]\n"
    "ds_read_b64_tr_b16 a[30:31], v139 offset:28672\n"
    "v_mfma_f32_16x16x32_bf16 v[52:55], a[84:87], a[228:231], v[52:55]\n"
    "ds_read_b64_tr_b16 a[32:33], v238 offset:0\n"
    "v_mfma_f32_16x16x32_bf16 v[52:55], a[116:119], a[244:247], v[52:55]\n"
    "ds_read_b64_tr_b16 a[34:35], v238 offset:4096\n"
    "v_mfma_f32_16x16x32_bf16 v[48:51], a[112:115], a[244:247], v[48:51]\n"
    "ds_read_b64_tr_b16 a[36:37], v239 offset:0\n"
    "v_mfma_f32_16x16x32_bf16 v[48:51], a[80:83], a[228:231], v[48:51]\n"
    "ds_read_b64_tr_b16 a[38:39], v239 offset:4096\n"
    "v_mfma_f32_16x16x32_bf16 v[44:47], a[76:79], a[228:231], v[44:47]\n"
    "buffer_load_dwordx4 a[204:207], v131, s[8:11], s57 offen sc1\n"
    "v_mfma_f32_16x16x32_bf16 v[44:47], a[108:111], a[244:247], v[44:47]\n"
    "ds_read_b64_tr_b16 a[40:41], v238 offset:8192\n"
    "v_mfma_f32_16x16x32_bf16 v[40:43], a[104:107], a[244:247], v[40:43]\n"
    "ds_read_b64_tr_b16 a[42:43], v238 offset:12288\n"
    "v_mfma_f32_16x16x32_bf16 v[40:43], a[72:75], a[228:231], v[40:43]\n"
    "ds_read_b64_tr_b16 a[44:45], v239 offset:8192\n"
    "v_mfma_f32_16x16x32_bf16 v[36:39], a[68:71], a[228:231], v[36:39]\n"
    "ds_read_b64_tr_b16 a[46:47], v239 offset:12288\n"
    "v_mfma_f32_16x16x32_bf16 v[36:39], a[100:103], a[244:247], v[36:39]\n"
    "ds_read_b64_tr_b16 a[48:49], v238 offset:16384\n"
    "v_mfma_f32_16x16x32_bf16 v[32:35], a[96:99], a[244:247], v[32:35]\n"
    "s_add_u32 s57, s46, 21504\n"
    "v_mfma_f32_16x16x32_bf16 v[32:35], a[64:67], a[228:231], v[32:35]\n"
    "buffer_load_dwordx4 a[208:211], v128, s[8:11], s57 offen sc1\n"
    "v_mfma_f32_16x16x32_bf16 v[64:67], a[64:67], a[232:235], v[64:67]\n"
    "ds_read_b64_tr_b16 a[50:51], v238 offset:20480\n"
    "v_mfma_f32_16x16x32_bf16 v[64:67], a[96:99], a[248:251], v[64:67]\n"
    "ds_read_b64_tr_b16 a[52:53], v239 offset:16384\n"
    "v_mfma_f32_16x16x32_bf16 v[68:71], a[100:103], a[248:251], v[68:71]\n"
    "ds_read_b64_tr_b16 a[54:55], v239 offset:20480\n"
    "v_mfma_f32_16x16x32_bf16 v[68:71], a[68:71], a[232:235], v[68:71]\n"
    "ds_read_b64_tr_b16 a[56:57], v238 offset:24576\n"
    "v_mfma_f32_16x16x32_bf16 v[72:75], a[72:75], a[232:235], v[72:75]\n"
    "ds_read_b64_tr_b16 a[58:59], v238 offset:28672\n"
    "v_mfma_f32_16x16x32_bf16 v[72:75], a[104:107], a[248:251], v[72:75]\n"
    "ds_read_b64_tr_b16 a[60:61], v239 offset:24576\n"
    "v_mfma_f32_16x16x32_bf16 v[76:79], a[108:111], a[248:251], v[76:79]\n"
    "buffer_load_dwordx4 a[212:215], v129, s[8:11], s57 offen sc1\n"
    "v_mfma_f32_16x16x32_bf16 v[76:79], a[76:79], a[232:235], v[76:79]\n"
    "ds_read_b64_tr_b16 a[62:63], v239 offset:28672\n"
    "v_mfma_f32_16x16x32_bf16 v[80:83], a[80:83], a[232:235], v[80:83]\n"
    "v_mfma_f32_16x16x32_bf16 v[80:83], a[112:115], a[248:251], v[80:83]\n"
    "v_mfma_f32_16x16x32_bf16 v[84:87], a[116:119], a[248:251], v[84:87]\n"
    "v_mfma_f32_16x16x32_bf16 v[84:87], a[84:87], a[232:235], v[84:87]\n"
    "v_mfma_f32_16x16x32_bf16 v[88:91], a[88:91], a[232:235], v[88:91]\n"
    "v_mfma_f32_16x16x32_bf16 v[88:91], a[120:123], a[248:251], v[88:91]\n"
    "v_mfma_f32_16x16x32_bf16 v[92:95], a[124:127], a[248:251], v[92:95]\n"
    "buffer_load_dwordx4 a[216:219], v130, s[8:11], s57 offen sc1\n"
    "v_mfma_f32_16x16x32_bf16 v[92:95], a[92:95], a[232:235], v[92:95]\n"
    "v_mfma_f32_16x16x32_bf16 v[124:127], a[92:95], a[236:239], v[124:127]\n"
    "v_mfma_f32_16x16x32_bf16 v[124:127], a[124:127], a[252:255], v[124:127]\n"
    "v_mfma_f32_16x16x32_bf16 v[120:123], a[120:123], a[252:255], v[120:123]\n"
    "v_mfma_f32_16x16x32_bf16 v[120:123], a[88:91], a[236:239], v[120:123]\n"
    "v_mfma_f32_16x16x32_bf16 v[116:119], a[84:87], a[236:239], v[116:119]\n"
    "v_mfma_f32_16x16x32_bf16 v[116:119], a[116:119], a[252:255], v[116:119]\n"
    "buffer_load_dwordx4 a[220:223], v131, s[8:11], s57 offen sc1\n"
    "v_mfma_f32_16x16x32_bf16 v[112:115], a[112:115], a[252:255], v[112:115]\n"
    "v_mfma_f32_16x16x32_bf16 v[112:115], a[80:83], a[236:239], v[112:115]\n"
    "v_mfma_f32_16x16x32_bf16 v[108:111], a[76:79], a[236:239], v[108:111]\n"
    "v_mfma_f32_16x16x32_bf16 v[108:111], a[108:111], a[252:255], v[108:111]\n"
    "v_mfma_f32_16x16x32_bf16 v[104:107], a[104:107], a[252:255], v[104:107]\n"
    "v_mfma_f32_16x16x32_bf16 v[104:107], a[72:75], a[236:239], v[104:107]\n"
    "v_mfma_f32_16x16x32_bf16 v[100:103], a[68:71], a[236:239], v[100:103]\n"
    "v_mfma_f32_16x16x32_bf16 v[100:103], a[100:103], a[252:255], v[100:103]\n"
    "v_mfma_f32_16x16x32_bf16 v[96:99], a[96:99], a[252:255], v[96:99]\n"
    "v_mfma_f32_16x16x32_bf16 v[96:99], a[64:67], a[236:239], v[96:99]\n"
    "s_waitcnt vmcnt(8) lgkmcnt(0)\n"
    "v_mfma_f32_16x16x32_bf16 v[8:11], a[0:3], a[128:131], v[8:11]\n"
    "s_add_u32 s57, s46, 22528\n"
    "buffer_load_dwordx4 a[224:227], v128, s[8:11], s57 offen sc1\n"
    "v_mfma_f32_16x16x32_bf16 v[8:11], a[8:11], a[144:147], v[8:11]\n"
    "ds_read_b64_tr_b16 a[64:65], v138 offset:32768\n"
    "buffer_load_dwordx4 a[228:231], v129, s[8:11], s57 offen sc1\n"
    "v_mfma_f32_16x16x32_bf16 v[8:11], a[16:19], a[160:163], v[8:11]\n"
    "ds_read_b64_tr_b16 a[66:67], v138 offset:36864\n"
    "v_mfma_f32_16x16x32_bf16 v[8:11], a[24:27], a[176:179], v[8:11]\n"
    "ds_read_b64_tr_b16 a[68:69], v139 offset:32768\n"
    "buffer_load_dwordx4 a[232:235], v130, s[8:11], s57 offen sc1\n"
    "v_mfma_f32_16x16x32_bf16 v[12:15], a[4:7], a[128:131], v[12:15]\n"
    "ds_read_b64_tr_b16 a[70:71], v139 offset:36864\n"
    "buffer_load_dwordx4 a[236:239], v131, s[8:11], s57 offen sc1\n"
    "v_mfma_f32_16x16x32_bf16 v[12:15], a[12:15], a[144:147], v[12:15]\n"
    "ds_read_b64_tr_b16 a[72:73], v138 offset:40960\n"
    "v_mfma_f32_16x16x32_bf16 v[12:15], a[20:23], a[160:163], v[12:15]\n"
    "ds_read_b64_tr_b16 a[74:75], v138 offset:45056\n"
    "s_add_u32 s57, s46, 23552\n"
    "buffer_load_dwordx4 a[240:243], v128, s[8:11], s57 offen sc1\n"
    "v_mfma_f32_16x16x32_bf16 v[12:15], a[28:31], a[176:179], v[12:15]\n"
    "ds_read_b64_tr_b16 a[76:77], v139 offset:40960\n"
    "buffer_load_dwordx4 a[244:247], v129, s[8:11], s57 offen sc1\n"
    "v_mfma_f32_16x16x32_bf16 v[40:43], a[0:3], a[132:135], v[40:43]\n"
    "ds_read_b64_tr_b16 a[78:79], v139 offset:45056\n"
    "v_mfma_f32_16x16x32_bf16 v[40:43], a[8:11], a[148:151], v[40:43]\n"
    "ds_read_b64_tr_b16 a[80:81], v138 offset:49152\n"
    "buffer_load_dwordx4 a[248:251], v130, s[8:11], s57 offen sc1\n"
    "v_mfma_f32_16x16x32_bf16 v[40:43], a[16:19], a[164:167], v[40:43]\n"
    "ds_read_b64_tr_b16 a[82:83], v138 offset:53248\n"
    "buffer_load_dwordx4 a[252:255], v131, s[8:11], s57 offen sc1\n"
    "v_mfma_f32_16x16x32_bf16 v[40:43], a[24:27], a[180:183], v[40:43]\n"
    "ds_read_b64_tr_b16 a[84:85], v139 offset:49152\n"
    "v_mfma_f32_16x16x32_bf16 v[44:47], a[4:7], a[132:135], v[44:47]\n"
    "ds_read_b64_tr_b16 a[86:87], v139 offset:53248\n"
    "v_mfma_f32_16x16x32_bf16 v[44:47], a[12:15], a[148:151], v[44:47]\n"
    "ds_read_b64_tr_b16 a[88:89], v138 offset:57344\n"
    "v_mfma_f32_16x16x32_bf16 v[44:47], a[20:23], a[164:167], v[44:47]\n"
    "ds_read_b64_tr_b16 a[90:91], v138 offset:61440\n"
    "v_mfma_f32_16x16x32_bf16 v[44:47], a[28:31], a[180:183], v[44:47]\n"
    "ds_read_b64_tr_b16 a[92:93], v139 offset:57344\n"
    "v_mfma_f32_16x16x32_bf16 v[72:75], a[0:3], a[136:139], v[72:75]\n"
    "ds_read_b64_tr_b16 a[94:95], v139 offset:61440\n"
    "v_mfma_f32_16x16x32_bf16 v[72:75], a[8:11], a[152:155], v[72:75]\n"
    "v_mfma_f32_16x16x32_bf16 v[72:75], a[16:19], a[168:171], v[72:75]\n"
    "v_mfma_f32_16x16x32_bf16 v[72:75], a[24:27], a[184:187], v[72:75]\n"
    "v_mfma_f32_16x16x32_bf16 v[76:79], a[4:7], a[136:139], v[76:79]\n"
    "v_mfma_f32_16x16x32_bf16 v[76:79], a[12:15], a[152:155], v[76:79]\n"
    "v_mfma_f32_16x16x32_bf16 v[76:79], a[20:23], a[168:171], v[76:79]\n"
    "v_mfma_f32_16x16x32_bf16 v[76:79], a[28:31], a[184:187], v[76:79]\n"
    "v_mfma_f32_16x16x32_bf16 v[104:107], a[0:3], a[140:143], v[104:107]\n"
    "v_mfma_f32_16x16x32_bf16 v[104:107], a[8:11], a[156:159], v[104:107]\n"
    "v_mfma_f32_16x16x32_bf16 v[104:107], a[16:19], a[172:175], v[104:107]\n"
    "v_mfma_f32_16x16x32_bf16 v[104:107], a[24:27], a[188:191], v[104:107]\n"
    "v_mfma_f32_16x16x32_bf16 v[108:111], a[4:7], a[140:143], v[108:111]\n"
    "v_mfma_f32_16x16x32_bf16 v[108:111], a[12:15], a[156:159], v[108:111]\n"
    "v_mfma_f32_16x16x32_bf16 v[108:111], a[20:23], a[172:175], v[108:111]\n"
    "v_mfma_f32_16x16x32_bf16 v[108:111], a[28:31], a[188:191], v[108:111]\n"
    "s_waitcnt lgkmcnt(0)\n"
    "v_mfma_f32_16x16x32_bf16 v[8:11], a[32:35], v[172:175], v[8:11]\n"
    "v_mfma_f32_16x16x32_bf16 v[8:11], a[40:43], v[188:191], v[8:11]\n"
    "ds_read_b64_tr_b16 a[96:97], v140 offset:0\n"
    "v_mfma_f32_16x16x32_bf16 v[8:11], a[48:51], v[204:207], v[8:11]\n"
    "ds_read_b64_tr_b16 a[98:99], v140 offset:4096\n"
    "v_mfma_f32_16x16x32_bf16 v[8:11], a[56:59], v[220:223], v[8:11]\n"
    "ds_read_b64_tr_b16 a[100:101], v141 offset:0\n"
    "v_mfma_f32_16x16x32_bf16 v[12:15], a[36:39], v[172:175], v[12:15]\n"
    "ds_read_b64_tr_b16 a[102:103], v141 offset:4096\n"
    "v_mfma_f32_16x16x32_bf16 v[12:15], a[44:47], v[188:191], v[12:15]\n"
    "ds_read_b64_tr_b16 a[104:105], v140 offset:8192\n"
    "v_mfma_f32_16x16x32_bf16 v[12:15], a[52:55], v[204:207], v[12:15]\n"
    "ds_read_b64_tr_b16 a[106:107], v140 offset:12288\n"
    "v_mfma_f32_16x16x32_bf16 v[12:15], a[60:63], v[220:223], v[12:15]\n"
    "ds_read_b64_tr_b16 a[108:109], v141 offset:8192\n"
    "v_mfma_f32_16x16x32_bf16 v[40:43], a[32:35], v[176:179], v[40:43]\n"
    "ds_read_b64_tr_b16 a[110:111], v141 offset:12288\n"
    "v_mfma_f32_16x16x32_bf16 v[40:43], a[40:43], v[192:195], v[40:43]\n"
    "ds_read_b64_tr_b16 a[112:113], v140 offset:16384\n"
    "v_mfma_f32_16x16x32_bf16 v[40:43], a[48:51], v[208:211], v[40:43]\n"
    "ds_read_b64_tr_b16 a[114:115], v140 offset:20480\n"
    "v_mfma_f32_16x16x32_bf16 v[40:43], a[56:59], v[224:227], v[40:43]\n"
    "ds_read_b64_tr_b16 a[116:117], v141 offset:16384\n"
    "v_mfma_f32_16x16x32_bf16 v[44:47], a[36:39], v[176:179], v[44:47]\n"
    "ds_read_b64_tr_b16 a[118:119], v141 offset:20480\n"
    "v_mfma_f32_16x16x32_bf16 v[44:47], a[44:47], v[192:195], v[44:47]\n"
    "ds_read_b64_tr_b16 a[120:121], v140 offset:24576\n"
    "v_mfma_f32_16x16x32_bf16 v[44:47], a[52:55], v[208:211], v[44:47]\n"
    "ds_read_b64_tr_b16 a[122:123], v140 offset:28672\n"
    "v_mfma_f32_16x16x32_bf16 v[44:47], a[60:63], v[224:227], v[44:47]\n"
    "ds_read_b64_tr_b16 a[124:125], v141 offset:24576\n"
    "v_mfma_f32_16x16x32_bf16 v[72:75], a[32:35], v[180:183], v[72:75]\n"
    "ds_read_b64_tr_b16 a[126:127], v141 offset:28672\n"
    "v_mfma_f32_16x16x32_bf16 v[72:75], a[40:43], v[196:199], v[72:75]\n"
    "v_mfma_f32_16x16x32_bf16 v[72:75], a[48:51], v[212:215], v[72:75]\n"
    "v_lshlrev_b32 v252, 16, v156\n"
    "v_mfma_f32_16x16x32_bf16 v[72:75], a[56:59], v[228:231], v[72:75]\n"
    "v_and_b32 v253, 0xffff0000, v156\n"
    "v_mfma_f32_16x16x32_bf16 v[76:79], a[36:39], v[180:183], v[76:79]\n"
    "v_add_f32 v0, v0, v252\n"
    "v_mfma_f32_16x16x32_bf16 v[76:79], a[44:47], v[196:199], v[76:79]\n"
    "v_add_f32 v1, v1, v253\n"
    "v_mfma_f32_16x16x32_bf16 v[76:79], a[52:55], v[212:215], v[76:79]\n"
    "v_lshlrev_b32 v252, 16, v157\n"
    "v_mfma_f32_16x16x32_bf16 v[76:79], a[60:63], v[228:231], v[76:79]\n"
    "v_and_b32 v253, 0xffff0000, v157\n"
    "v_mfma_f32_16x16x32_bf16 v[104:107], a[32:35], v[184:187], v[104:107]\n"
    "v_add_f32 v2, v2, v252\n"
    "v_mfma_f32_16x16x32_bf16 v[104:107], a[40:43], v[200:203], v[104:107]\n"
    "v_add_f32 v3, v3, v253\n"
    "v_mfma_f32_16x16x32_bf16 v[104:107], a[48:51], v[216:219], v[104:107]\n"
    "v_lshlrev_b32 v252, 16, v158\n"
    "v_mfma_f32_16x16x32_bf16 v[104:107], a[56:59], v[232:235], v[104:107]\n"
    "v_and_b32 v253, 0xffff0000, v158\n"
    "v_mfma_f32_16x16x32_bf16 v[108:111], a[36:39], v[184:187], v[108:111]\n"
    "v_add_f32 v4, v4, v252\n"
    "v_mfma_f32_16x16x32_bf16 v[108:111], a[44:47], v[200:203], v[108:111]\n"
    "v_add_f32 v5, v5, v253\n"
    "v_mfma_f32_16x16x32_bf16 v[108:111], a[52:55], v[216:219], v[108:111]\n"
    "v_lshlrev_b32 v252, 16, v159\n"
    "v_mfma_f32_16x16x32_bf16 v[108:111], a[60:63], v[232:235], v[108:111]\n"
    "s_waitcnt vmcnt(0) lgkmcnt(0)\n"
    "v_and_b32 v253, 0xffff0000, v159\n"
    "v_mfma_f32_16x16x32_bf16 v[8:11], a[64:67], a[192:195], v[8:11]\n"
    "v_mfma_f32_16x16x32_bf16 v[8:11], a[72:75], a[208:211], v[8:11]\n"
    "ds_read_b64_tr_b16 a[0:1], v240 offset:0\n"
    "v_mfma_f32_16x16x32_bf16 v[8:11], a[80:83], a[224:227], v[8:11]\n"
    "ds_read_b64_tr_b16 a[2:3], v240 offset:4096\n"
    "v_mfma_f32_16x16x32_bf16 v[8:11], a[88:91], a[240:243], v[8:11]\n"
    "ds_read_b64_tr_b16 a[4:5], v241 offset:0\n"
    "v_mfma_f32_16x16x32_bf16 v[12:15], a[68:71], a[192:195], v[12:15]\n"
    "ds_read_b64_tr_b16 a[6:7], v241 offset:4096\n"
    "v_mfma_f32_16x16x32_bf16 v[12:15], a[76:79], a[208:211], v[12:15]\n"
    "ds_read_b64_tr_b16 a[8:9], v240 offset:8192\n"
    "v_mfma_f32_16x16x32_bf16 v[12:15], a[84:87], a[224:227], v[12:15]\n"
    "ds_read_b64_tr_b16 a[10:11], v240 offset:12288\n"
    "v_mfma_f32_16x16x32_bf16 v[12:15], a[92:95], a[240:243], v[12:15]\n"
    "ds_read_b64_tr_b16 a[12:13], v241 offset:8192\n"
    "v_mfma_f32_16x16x32_bf16 v[40:43], a[64:67], a[196:199], v[40:43]\n"
    "ds_read_b64_tr_b16 a[14:15], v241 offset:12288\n"
    "v_mfma_f32_16x16x32_bf16 v[40:43], a[72:75], a[212:215], v[40:43]\n"
    "ds_read_b64_tr_b16 a[16:17], v240 offset:16384\n"
    "v_mfma_f32_16x16x32_bf16 v[40:43], a[80:83], a[228:231], v[40:43]\n"
    "ds_read_b64_tr_b16 a[18:19], v240 offset:20480\n"
    "v_mfma_f32_16x16x32_bf16 v[40:43], a[88:91], a[244:247], v[40:43]\n"
    "ds_read_b64_tr_b16 a[20:21], v241 offset:16384\n"
    "v_mfma_f32_16x16x32_bf16 v[44:47], a[68:71], a[196:199], v[44:47]\n"
    "ds_read_b64_tr_b16 a[22:23], v241 offset:20480\n"
    "v_mfma_f32_16x16x32_bf16 v[44:47], a[76:79], a[212:215], v[44:47]\n"
    "ds_read_b64_tr_b16 a[24:25], v240 offset:24576\n"
    "v_mfma_f32_16x16x32_bf16 v[44:47], a[84:87], a[228:231], v[44:47]\n"
    "ds_read_b64_tr_b16 a[26:27], v240 offset:28672\n"
    "v_mfma_f32_16x16x32_bf16 v[44:47], a[92:95], a[244:247], v[44:47]\n"
    "ds_read_b64_tr_b16 a[28:29], v241 offset:24576\n"
    "v_cvt_pk_bf16_f32 v8, v8, v9\n"
    "v_cvt_pk_bf16_f32 v9, v10, v11\n"
    "v_mfma_f32_16x16x32_bf16 v[72:75], a[64:67], a[200:203], v[72:75]\n"
    "ds_read_b64_tr_b16 a[30:31], v241 offset:28672\n"
    "v_cvt_pk_bf16_f32 v10, v12, v13\n"
    "v_mfma_f32_16x16x32_bf16 v[72:75], a[72:75], a[216:219], v[72:75]\n"
    "v_cvt_pk_bf16_f32 v11, v14, v15\n"
    "v_mfma_f32_16x16x32_bf16 v[72:75], a[80:83], a[232:235], v[72:75]\n"
    "buffer_store_dwordx4 v[8:11], v144, s[8:11], 0 offen offset:0 sc1\n"
    "v_add_f32 v6, v6, v252\n"
    "v_mfma_f32_16x16x32_bf16 v[72:75], a[88:91], a[248:251], v[72:75]\n"
    "v_add_f32 v7, v7, v253\n"
    "v_mfma_f32_16x16x32_bf16 v[76:79], a[68:71], a[200:203], v[76:79]\n"
    "v_lshlrev_b32 v252, 16, v160\n"
    "v_mfma_f32_16x16x32_bf16 v[76:79], a[76:79], a[216:219], v[76:79]\n"
    "v_and_b32 v253, 0xffff0000, v160\n"
    "v_mfma_f32_16x16x32_bf16 v[76:79], a[84:87], a[232:235], v[76:79]\n"
    "v_add_f32 v32, v32, v252\n"
    "v_mfma_f32_16x16x32_bf16 v[76:79], a[92:95], a[248:251], v[76:79]\n"
    "v_cvt_pk_bf16_f32 v40, v40, v41\n"
    "v_cvt_pk_bf16_f32 v41, v42, v43\n"
    "v_add_f32 v33, v33, v253\n"
    "v_mfma_f32_16x16x32_bf16 v[104:107], a[64:67], a[204:207], v[104:107]\n"
    "v_cvt_pk_bf16_f32 v42, v44, v45\n"
    "v_lshlrev_b32 v252, 16, v161\n"
    "v_mfma_f32_16x16x32_bf16 v[104:107], a[72:75], a[220:223], v[104:107]\n"
    "v_cvt_pk_bf16_f32 v43, v46, v47\n"
    "v_and_b32 v253, 0xffff0000, v161\n"
    "v_mfma_f32_16x16x32_bf16 v[104:107], a[80:83], a[236:239], v[104:107]\n"
    "buffer_store_dwordx4 v[40:43], v144, s[8:11], 0 offen offset:1024 sc1\n"
    "v_add_f32 v34, v34, v252\n"
    "v_mfma_f32_16x16x32_bf16 v[104:107], a[88:91], a[252:255], v[104:107]\n"
    "v_add_f32 v35, v35, v253\n"
    "v_mfma_f32_16x16x32_bf16 v[108:111], a[68:71], a[204:207], v[108:111]\n"
    "v_lshlrev_b32 v252, 16, v162\n"
    "v_mfma_f32_16x16x32_bf16 v[108:111], a[76:79], a[220:223], v[108:111]\n"
    "v_and_b32 v253, 0xffff0000, v162\n"
    "v_mfma_f32_16x16x32_bf16 v[108:111], a[84:87], a[236:239], v[108:111]\n"
    "v_add_f32 v36, v36, v252\n"
    "v_mfma_f32_16x16x32_bf16 v[108:111], a[92:95], a[252:255], v[108:111]\n"
    "v_cvt_pk_bf16_f32 v72, v72, v73\n"
    "v_cvt_pk_bf16_f32 v73, v74, v75\n"
    "s_waitcnt lgkmcnt(0)\n"
    "v_add_f32 v37, v37, v253\n"
    "v_mfma_f32_16x16x32_bf16 v[16:19], a[96:99], a[128:131], v[16:19]\n"
    "v_cvt_pk_bf16_f32 v74, v76, v77\n"
    "v_mfma_f32_16x16x32_bf16 v[16:19], a[104:107], a[144:147], v[16:19]\n"
    "v_cvt_pk_bf16_f32 v75, v78, v79\n"
    "ds_read_b64_tr_b16 a[32:33], v140 offset:32768\n"
    "v_mfma_f32_16x16x32_bf16 v[16:19], a[112:115], a[160:163], v[16:19]\n"
    "buffer_store_dwordx4 v[72:75], v144, s[8:11], 0 offen offset:2048 sc1\n"
    "ds_read_b64_tr_b16 a[34:35], v140 offset:36864\n"
    "v_mfma_f32_16x16x32_bf16 v[16:19], a[120:123], a[176:179], v[16:19]\n"
    "ds_read_b64_tr_b16 a[36:37], v141 offset:32768\n"
    "v_mfma_f32_16x16x32_bf16 v[20:23], a[100:103], a[128:131], v[20:23]\n"
    "ds_read_b64_tr_b16 a[38:39], v141 offset:36864\n"
    "v_mfma_f32_16x16x32_bf16 v[20:23], a[108:111], a[144:147], v[20:23]\n"
    "ds_read_b64_tr_b16 a[40:41], v140 offset:40960\n"
    "v_mfma_f32_16x16x32_bf16 v[20:23], a[116:119], a[160:163], v[20:23]\n"
    "ds_read_b64_tr_b16 a[42:43], v140 offset:45056\n"
    "v_mfma_f32_16x16x32_bf16 v[20:23], a[124:127], a[176:179], v[20:23]\n"
    "v_cvt_pk_bf16_f32 v104, v104, v105\n"
    "v_cvt_pk_bf16_f32 v105, v106, v107\n"
    "ds_read_b64_tr_b16 a[44:45], v141 offset:40960\n"
    "v_mfma_f32_16x16x32_bf16 v[48:51], a[96:99], a[132:135], v[48:51]\n"
    "v_cvt_pk_bf16_f32 v106, v108, v109\n"
    "ds_read_b64_tr_b16 a[46:47], v141 offset:45056\n"
    "v_mfma_f32_16x16x32_bf16 v[48:51], a[104:107], a[148:151], v[48:51]\n"
    "v_cvt_pk_bf16_f32 v107, v110, v111\n"
    "ds_read_b64_tr_b16 a[48:49], v140 offset:49152\n"
    "v_mfma_f32_16x16x32_bf16 v[48:51], a[112:115], a[164:167], v[48:51]\n"
    "buffer_store_dwordx4 v[104:107], v144, s[8:11], 0 offen offset:3072 sc1\n"
    "ds_read_b64_tr_b16 a[50:51], v140 offset:53248\n"
    "v_mfma_f32_16x16x32_bf16 v[48:51], a[120:123], a[180:183], v[48:51]\n"
    "ds_read_b64_tr_b16 a[52:53], v141 offset:49152\n"
    "v_mfma_f32_16x16x32_bf16 v[52:55], a[100:103], a[132:135], v[52:55]\n"
    "ds_read_b64_tr_b16 a[54:55], v141 offset:53248\n"
    "v_mfma_f32_16x16x32_bf16 v[52:55], a[108:111], a[148:151], v[52:55]\n"
    "ds_read_b64_tr_b16 a[56:57], v140 offset:57344\n"
    "v_mfma_f32_16x16x32_bf16 v[52:55], a[116:119], a[164:167], v[52:55]\n"
    "ds_read_b64_tr_b16 a[58:59], v140 offset:61440\n"
    "v_mfma_f32_16x16x32_bf16 v[52:55], a[124:127], a[180:183], v[52:55]\n"
    "ds_read_b64_tr_b16 a[60:61], v141 offset:57344\n"
    "v_mfma_f32_16x16x32_bf16 v[80:83], a[96:99], a[136:139], v[80:83]\n"
    "ds_read_b64_tr_b16 a[62:63], v141 offset:61440\n"
    "v_mfma_f32_16x16x32_bf16 v[80:83], a[104:107], a[152:155], v[80:83]\n"
    "v_mfma_f32_16x16x32_bf16 v[80:83], a[112:115], a[168:171], v[80:83]\n"
    "v_lshlrev_b32 v252, 16, v163\n"
    "v_mfma_f32_16x16x32_bf16 v[80:83], a[120:123], a[184:187], v[80:83]\n"
    "v_and_b32 v253, 0xffff0000, v163\n"
    "v_mfma_f32_16x16x32_bf16 v[84:87], a[100:103], a[136:139], v[84:87]\n"
    "v_add_f32 v38, v38, v252\n"
    "v_mfma_f32_16x16x32_bf16 v[84:87], a[108:111], a[152:155], v[84:87]\n"
    "v_add_f32 v39, v39, v253\n"
    "v_mfma_f32_16x16x32_bf16 v[84:87], a[116:119], a[168:171], v[84:87]\n"
    "v_lshlrev_b32 v252, 16, v164\n"
    "v_mfma_f32_16x16x32_bf16 v[84:87], a[124:127], a[184:187], v[84:87]\n"
    "v_and_b32 v253, 0xffff0000, v164\n"
    "v_mfma_f32_16x16x32_bf16 v[112:115], a[96:99], a[140:143], v[112:115]\n"
    "v_add_f32 v64, v64, v252\n"
    "v_mfma_f32_16x16x32_bf16 v[112:115], a[104:107], a[156:159], v[112:115]\n"
    "v_add_f32 v65, v65, v253\n"
    "v_mfma_f32_16x16x32_bf16 v[112:115], a[112:115], a[172:175], v[112:115]\n"
    "v_lshlrev_b32 v252, 16, v165\n"
    "v_mfma_f32_16x16x32_bf16 v[112:115], a[120:123], a[188:191], v[112:115]\n"
    "v_and_b32 v253, 0xffff0000, v165\n"
    "v_mfma_f32_16x16x32_bf16 v[116:119], a[100:103], a[140:143], v[116:119]\n"
    "v_add_f32 v66, v66, v252\n"
    "v_mfma_f32_16x16x32_bf16 v[116:119], a[108:111], a[156:159], v[116:119]\n"
    "v_add_f32 v67, v67, v253\n"
    "v_mfma_f32_16x16x32_bf16 v[116:119], a[116:119], a[172:175], v[116:119]\n"
    "v_lshlrev_b32 v252, 16, v166\n"
    "v_mfma_f32_16x16x32_bf16 v[116:119], a[124:127], a[188:191], v[116:119]\n"
    "s_waitcnt lgkmcnt(0)\n"
    "v_and_b32 v253, 0xffff0000, v166\n"
    "v_mfma_f32_16x16x32_bf16 v[16:19], a[0:3], v[172:175], v[16:19]\n"
    "v_mfma_f32_16x16x32_bf16 v[16:19], a[8:11], v[188:191], v[16:19]\n"
    "ds_read_b64_tr_b16 a[64:65], v142 offset:0\n"
    "v_mfma_f32_16x16x32_bf16 v[16:19], a[16:19], v[204:207], v[16:19]\n"
    "ds_read_b64_tr_b16 a[66:67], v142 offset:4096\n"
    "v_mfma_f32_16x16x32_bf16 v[16:19], a[24:27], v[220:223], v[16:19]\n"
    "ds_read_b64_tr_b16 a[68:69], v143 offset:0\n"
    "v_mfma_f32_16x16x32_bf16 v[20:23], a[4:7], v[172:175], v[20:23]\n"
    "ds_read_b64_tr_b16 a[70:71], v143 offset:4096\n"
    "v_mfma_f32_16x16x32_bf16 v[20:23], a[12:15], v[188:191], v[20:23]\n"
    "S3_H2%=:\n"
    "s_cbranch_execz S3_R%=\n"
    "ds_read_b64_tr_b16 a[72:73], v142 offset:8192\n"
    "v_mfma_f32_16x16x32_bf16 v[20:23], a[20:23], v[204:207], v[20:23]\n"
    "ds_read_b64_tr_b16 a[74:75], v142 offset:12288\n"
    "v_mfma_f32_16x16x32_bf16 v[20:23], a[28:31], v[220:223], v[20:23]\n"
    "ds_read_b64_tr_b16 a[76:77], v143 offset:8192\n"
    "v_mfma_f32_16x16x32_bf16 v[48:51], a[0:3], v[176:179], v[48:51]\n"
    "ds_read_b64_tr_b16 a[78:79], v143 offset:12288\n"
    "v_mfma_f32_16x16x32_bf16 v[48:51], a[8:11], v[192:195], v[48:51]\n"
    "ds_read_b64_tr_b16 a[80:81], v142 offset:16384\n"
    "v_mfma_f32_16x16x32_bf16 v[48:51], a[16:19], v[208:211], v[48:51]\n"
    "ds_read_b64_tr_b16 a[82:83], v142 offset:20480\n"
    "v_mfma_f32_16x16x32_bf16 v[48:51], a[24:27], v[224:227], v[48:51]\n"
    "ds_read_b64_tr_b16 a[84:85], v143 offset:16384\n"
    "v_mfma_f32_16x16x32_bf16 v[52:55], a[4:7], v[176:179], v[52:55]\n"
    "ds_read_b64_tr_b16 a[86:87], v143 offset:20480\n"
    "v_mfma_f32_16x16x32_bf16 v[52:55], a[12:15], v[192:195], v[52:55]\n"
    "ds_read_b64_tr_b16 a[88:89], v142 offset:24576\n"
    "v_mfma_f32_16x16x32_bf16 v[52:55], a[20:23], v[208:211], v[52:55]\n"
    "ds_read_b64_tr_b16 a[90:91], v142 offset:28672\n"
    "v_mfma_f32_16x16x32_bf16 v[52:55], a[28:31], v[224:227], v[52:55]\n"
    "ds_read_b64_tr_b16 a[92:93], v143 offset:24576\n"
    "v_mfma_f32_16x16x32_bf16 v[80:83], a[0:3], v[180:183], v[80:83]\n"
    "ds_read_b64_tr_b16 a[94:95], v143 offset:28672\n"
    "v_mfma_f32_16x16x32_bf16 v[80:83], a[8:11], v[196:199], v[80:83]\n"
    "v_mfma_f32_16x16x32_bf16 v[80:83], a[16:19], v[212:215], v[80:83]\n"
    "v_add_f32 v68, v68, v252\n"
    "v_mfma_f32_16x16x32_bf16 v[80:83], a[24:27], v[228:231], v[80:83]\n"
    "v_add_f32 v69, v69, v253\n"
    "v_mfma_f32_16x16x32_bf16 v[84:87], a[4:7], v[180:183], v[84:87]\n"
    "v_lshlrev_b32 v252, 16, v167\n"
    "v_mfma_f32_16x16x32_bf16 v[84:87], a[12:15], v[196:199], v[84:87]\n"
    "v_and_b32 v253, 0xffff0000, v167\n"
    "v_mfma_f32_16x16x32_bf16 v[84:87], a[20:23], v[212:215], v[84:87]\n"
    "v_add_f32 v70, v70, v252\n"
    "v_mfma_f32_16x16x32_bf16 v[84:87], a[28:31], v[228:231], v[84:87]\n"
    "v_add_f32 v71, v71, v253\n"
    "v_mfma_f32_16x16x32_bf16 v[112:115], a[0:3], v[184:187], v[112:115]\n"
    "v_lshlrev_b32 v252, 16, v168\n"
    "v_mfma_f32_16x16x32_bf16 v[112:115], a[8:11], v[200:203], v[112:115]\n"
    "v_and_b32 v253, 0xffff0000, v168\n"
    "v_mfma_f32_16x16x32_bf16 v[112:115], a[16:19], v[216:219], v[112:115]\n"
    "v_add_f32 v96, v96, v252\n"
    "v_mfma_f32_16x16x32_bf16 v[112:115], a[24:27], v[232:235], v[112:115]\n"
    "v_add_f32 v97, v97, v253\n"
    "v_mfma_f32_16x16x32_bf16 v[116:119], a[4:7], v[184:187], v[116:119]\n"
    "v_lshlrev_b32 v252, 16, v169\n"
    "v_mfma_f32_16x16x32_bf16 v[116:119], a[12:15], v[200:203], v[116:119]\n"
    "v_and_b32 v253, 0xffff0000, v169\n"
    "v_mfma_f32_16x16x32_bf16 v[116:119], a[20:23], v[216:219], v[116:119]\n"
    "s_waitcnt vmcnt(0)\n"
    "buffer_store_dword v250, v150, s[8:11], 0 offen offset:0 sc1\n"
    "v_add_f32 v98, v98, v252\n"
    "v_mfma_f32_16x16x32_bf16 v[116:119], a[28:31], v[232:235], v[116:119]\n"
    "s_waitcnt lgkmcnt(0)\n"
    "v_add_f32 v99, v99, v253\n"
    "v_mfma_f32_16x16x32_bf16 v[16:19], a[32:35], a[192:195], v[16:19]\n"
    "v_mfma_f32_16x16x32_bf16 v[16:19], a[40:43], a[208:211], v[16:19]\n"
    "ds_read_b64_tr_b16 a[96:97], v242 offset:0\n"
    "v_mfma_f32_16x16x32_bf16 v[16:19], a[48:51], a[224:227], v[16:19]\n"
    "ds_read_b64_tr_b16 a[98:99], v242 offset:4096\n"
    "v_mfma_f32_16x16x32_bf16 v[16:19], a[56:59], a[240:243], v[16:19]\n"
    "ds_read_b64_tr_b16 a[100:101], v243 offset:0\n"
    "v_mfma_f32_16x16x32_bf16 v[20:23], a[36:39], a[192:195], v[20:23]\n"
    "ds_read_b64_tr_b16 a[102:103], v243 offset:4096\n"
    "v_mfma_f32_16x16x32_bf16 v[20:23], a[44:47], a[208:211], v[20:23]\n"
    "ds_read_b64_tr_b16 a[104:105], v242 offset:8192\n"
    "v_mfma_f32_16x16x32_bf16 v[20:23], a[52:55], a[224:227], v[20:23]\n"
    "ds_read_b64_tr_b16 a[106:107], v242 offset:12288\n"
    "v_mfma_f32_16x16x32_bf16 v[20:23], a[60:63], a[240:243], v[20:23]\n"
    "ds_read_b64_tr_b16 a[108:109], v243 offset:8192\n"
    "v_mfma_f32_16x16x32_bf16 v[48:51], a[32:35], a[196:199], v[48:51]\n"
    "ds_read_b64_tr_b16 a[110:111], v243 offset:12288\n"
    "v_mfma_f32_16x16x32_bf16 v[48:51], a[40:43], a[212:215], v[48:51]\n"
    "ds_read_b64_tr_b16 a[112:113], v242 offset:16384\n"
    "v_mfma_f32_16x16x32_bf16 v[48:51], a[48:51], a[228:231], v[48:51]\n"
    "ds_read_b64_tr_b16 a[114:115], v242 offset:20480\n"
    "v_mfma_f32_16x16x32_bf16 v[48:51], a[56:59], a[244:247], v[48:51]\n"
    "ds_read_b64_tr_b16 a[116:117], v243 offset:16384\n"
    "v_mfma_f32_16x16x32_bf16 v[52:55], a[36:39], a[196:199], v[52:55]\n"
    "ds_read_b64_tr_b16 a[118:119], v243 offset:20480\n"
    "v_mfma_f32_16x16x32_bf16 v[52:55], a[44:47], a[212:215], v[52:55]\n"
    "ds_read_b64_tr_b16 a[120:121], v242 offset:24576\n"
    "v_mfma_f32_16x16x32_bf16 v[52:55], a[52:55], a[228:231], v[52:55]\n"
    "ds_read_b64_tr_b16 a[122:123], v242 offset:28672\n"
    "v_mfma_f32_16x16x32_bf16 v[52:55], a[60:63], a[244:247], v[52:55]\n"
    "ds_read_b64_tr_b16 a[124:125], v243 offset:24576\n"
    "v_cvt_pk_bf16_f32 v16, v16, v17\n"
    "v_cvt_pk_bf16_f32 v17, v18, v19\n"
    "v_mfma_f32_16x16x32_bf16 v[80:83], a[32:35], a[200:203], v[80:83]\n"
    "ds_read_b64_tr_b16 a[126:127], v243 offset:28672\n"
    "v_cvt_pk_bf16_f32 v18, v20, v21\n"
    "v_mfma_f32_16x16x32_bf16 v[80:83], a[40:43], a[216:219], v[80:83]\n"
    "v_cvt_pk_bf16_f32 v19, v22, v23\n"
    "v_mfma_f32_16x16x32_bf16 v[80:83], a[48:51], a[232:235], v[80:83]\n"
    "buffer_store_dwordx4 v[16:19], v145, s[8:11], 0 offen offset:0 sc1\n"
    "v_lshlrev_b32 v252, 16, v170\n"
    "v_mfma_f32_16x16x32_bf16 v[80:83], a[56:59], a[248:251], v[80:83]\n"
    "v_and_b32 v253, 0xffff0000, v170\n"
    "v_mfma_f32_16x16x32_bf16 v[84:87], a[36:39], a[200:203], v[84:87]\n"
    "v_add_f32 v100, v100, v252\n"
    "v_mfma_f32_16x16x32_bf16 v[84:87], a[44:47], a[216:219], v[84:87]\n"
    "v_add_f32 v101, v101, v253\n"
    "v_mfma_f32_16x16x32_bf16 v[84:87], a[52:55], a[232:235], v[84:87]\n"
    "v_lshlrev_b32 v252, 16, v171\n"
    "v_mfma_f32_16x16x32_bf16 v[84:87], a[60:63], a[248:251], v[84:87]\n"
    "v_cvt_pk_bf16_f32 v48, v48, v49\n"
    "v_cvt_pk_bf16_f32 v49, v50, v51\n"
    "v_and_b32 v253, 0xffff0000, v171\n"
    "v_mfma_f32_16x16x32_bf16 v[112:115], a[32:35], a[204:207], v[112:115]\n"
    "v_cvt_pk_bf16_f32 v50, v52, v53\n"
    "v_add_f32 v102, v102, v252\n"
    "v_mfma_f32_16x16x32_bf16 v[112:115], a[40:43], a[220:223], v[112:115]\n"
    "v_cvt_pk_bf16_f32 v51, v54, v55\n"
    "v_add_f32 v103, v103, v253\n"
    "v_mfma_f32_16x16x32_bf16 v[112:115], a[48:51], a[236:239], v[112:115]\n"
    "buffer_store_dwordx4 v[48:51], v145, s[8:11], 0 offen offset:1024 sc1\n"
    "v_mfma_f32_16x16x32_bf16 v[112:115], a[56:59], a[252:255], v[112:115]\n"
    "v_mfma_f32_16x16x32_bf16 v[116:119], a[36:39], a[204:207], v[116:119]\n"
    "v_mfma_f32_16x16x32_bf16 v[116:119], a[44:47], a[220:223], v[116:119]\n"
    "v_mfma_f32_16x16x32_bf16 v[116:119], a[52:55], a[236:239], v[116:119]\n"
    "v_mfma_f32_16x16x32_bf16 v[116:119], a[60:63], a[252:255], v[116:119]\n"
    "v_cvt_pk_bf16_f32 v80, v80, v81\n"
    "v_cvt_pk_bf16_f32 v81, v82, v83\n"
    "s_waitcnt lgkmcnt(0)\n"
    "v_mfma_f32_16x16x32_bf16 v[24:27], a[64:67], a[128:131], v[24:27]\n"
    "v_cvt_pk_bf16_f32 v82, v84, v85\n"
    "v_mfma_f32_16x16x32_bf16 v[24:27], a[72:75], a[144:147], v[24:27]\n"
    "v_cvt_pk_bf16_f32 v83, v86, v87\n"
    "ds_read_b64_tr_b16 a[0:1], v142 offset:32768\n"
    "v_mfma_f32_16x16x32_bf16 v[24:27], a[80:83], a[160:163], v[24:27]\n"
    "buffer_store_dwordx4 v[80:83], v145, s[8:11], 0 offen offset:2048 sc1\n"
    "ds_read_b64_tr_b16 a[2:3], v142 offset:36864\n"
    "v_mfma_f32_16x16x32_bf16 v[24:27], a[88:91], a[176:179], v[24:27]\n"
    "ds_read_b64_tr_b16 a[4:5], v143 offset:32768\n"
    "v_mfma_f32_16x16x32_bf16 v[28:31], a[68:71], a[128:131], v[28:31]\n"
    "ds_read_b64_tr_b16 a[6:7], v143 offset:36864\n"
    "v_mfma_f32_16x16x32_bf16 v[28:31], a[76:79], a[144:147], v[28:31]\n"
    "ds_read_b64_tr_b16 a[8:9], v142 offset:40960\n"
    "v_mfma_f32_16x16x32_bf16 v[28:31], a[84:87], a[160:163], v[28:31]\n"
    "ds_read_b64_tr_b16 a[10:11], v142 offset:45056\n"
    "v_mfma_f32_16x16x32_bf16 v[28:31], a[92:95], a[176:179], v[28:31]\n"
    "v_cvt_pk_bf16_f32 v112, v112, v113\n"
    "v_cvt_pk_bf16_f32 v113, v114, v115\n"
    "ds_read_b64_tr_b16 a[12:13], v143 offset:40960\n"
    "v_mfma_f32_16x16x32_bf16 v[56:59], a[64:67], a[132:135], v[56:59]\n"
    "v_cvt_pk_bf16_f32 v114, v116, v117\n"
    "ds_read_b64_tr_b16 a[14:15], v143 offset:45056\n"
    "v_mfma_f32_16x16x32_bf16 v[56:59], a[72:75], a[148:151], v[56:59]\n"
    "v_cvt_pk_bf16_f32 v115, v118, v119\n"
    "ds_read_b64_tr_b16 a[16:17], v142 offset:49152\n"
    "v_mfma_f32_16x16x32_bf16 v[56:59], a[80:83], a[164:167], v[56:59]\n"
    "buffer_store_dwordx4 v[112:115], v145, s[8:11], 0 offen offset:3072 sc1\n"
    "ds_read_b64_tr_b16 a[18:19], v142 offset:53248\n"
    "v_mfma_f32_16x16x32_bf16 v[56:59], a[88:91], a[180:183], v[56:59]\n"
    "ds_read_b64_tr_b16 a[20:21], v143 offset:49152\n"
    "v_mfma_f32_16x16x32_bf16 v[60:63], a[68:71], a[132:135], v[60:63]\n"
    "ds_read_b64_tr_b16 a[22:23], v143 offset:53248\n"
    "v_mfma_f32_16x16x32_bf16 v[60:63], a[76:79], a[148:151], v[60:63]\n"
    "ds_read_b64_tr_b16 a[24:25], v142 offset:57344\n"
    "v_mfma_f32_16x16x32_bf16 v[60:63], a[84:87], a[164:167], v[60:63]\n"
    "ds_read_b64_tr_b16 a[26:27], v142 offset:61440\n"
    "v_mfma_f32_16x16x32_bf16 v[60:63], a[92:95], a[180:183], v[60:63]\n"
    "ds_read_b64_tr_b16 a[28:29], v143 offset:57344\n"
    "v_mfma_f32_16x16x32_bf16 v[88:91], a[64:67], a[136:139], v[88:91]\n"
    "ds_read_b64_tr_b16 a[30:31], v143 offset:61440\n"
    "v_mfma_f32_16x16x32_bf16 v[88:91], a[72:75], a[152:155], v[88:91]\n"
    "v_mfma_f32_16x16x32_bf16 v[88:91], a[80:83], a[168:171], v[88:91]\n"
    "v_mfma_f32_16x16x32_bf16 v[88:91], a[88:91], a[184:187], v[88:91]\n"
    "v_mfma_f32_16x16x32_bf16 v[92:95], a[68:71], a[136:139], v[92:95]\n"
    "v_mfma_f32_16x16x32_bf16 v[92:95], a[76:79], a[152:155], v[92:95]\n"
    "v_mfma_f32_16x16x32_bf16 v[92:95], a[84:87], a[168:171], v[92:95]\n"
    "v_mfma_f32_16x16x32_bf16 v[92:95], a[92:95], a[184:187], v[92:95]\n"
    "v_mfma_f32_16x16x32_bf16 v[120:123], a[64:67], a[140:143], v[120:123]\n"
    "v_mfma_f32_16x16x32_bf16 v[120:123], a[72:75], a[156:159], v[120:123]\n"
    "v_mfma_f32_16x16x32_bf16 v[120:123], a[80:83], a[172:175], v[120:123]\n"
    "v_mfma_f32_16x16x32_bf16 v[120:123], a[88:91], a[188:191], v[120:123]\n"
    "v_mfma_f32_16x16x32_bf16 v[124:127], a[68:71], a[140:143], v[124:127]\n"
    "v_mfma_f32_16x16x32_bf16 v[124:127], a[76:79], a[156:159], v[124:127]\n"
    "v_mfma_f32_16x16x32_bf16 v[124:127], a[84:87], a[172:175], v[124:127]\n"
    "v_mfma_f32_16x16x32_bf16 v[124:127], a[92:95], a[188:191], v[124:127]\n"
    "s_waitcnt lgkmcnt(0)\n"
    "v_mfma_f32_16x16x32_bf16 v[24:27], a[96:99], v[172:175], v[24:27]\n"
    "v_mfma_f32_16x16x32_bf16 v[24:27], a[104:107], v[188:191], v[24:27]\n"
    "ds_read_b64_tr_b16 a[32:33], v136 offset:0\n"
    "v_mfma_f32_16x16x32_bf16 v[24:27], a[112:115], v[204:207], v[24:27]\n"
    "ds_read_b64_tr_b16 a[34:35], v136 offset:4096\n"
    "v_mfma_f32_16x16x32_bf16 v[24:27], a[120:123], v[220:223], v[24:27]\n"
    "ds_read_b64_tr_b16 a[36:37], v137 offset:0\n"
    "v_mfma_f32_16x16x32_bf16 v[28:31], a[100:103], v[172:175], v[28:31]\n"
    "ds_read_b64_tr_b16 a[38:39], v137 offset:4096\n"
    "v_mfma_f32_16x16x32_bf16 v[28:31], a[108:111], v[188:191], v[28:31]\n"
    "ds_read_b64_tr_b16 a[40:41], v136 offset:8192\n"
    "v_mfma_f32_16x16x32_bf16 v[28:31], a[116:119], v[204:207], v[28:31]\n"
    "ds_read_b64_tr_b16 a[42:43], v136 offset:12288\n"
    "v_mfma_f32_16x16x32_bf16 v[28:31], a[124:127], v[220:223], v[28:31]\n"
    "ds_read_b64_tr_b16 a[44:45], v137 offset:8192\n"
    "v_mfma_f32_16x16x32_bf16 v[56:59], a[96:99], v[176:179], v[56:59]\n"
    "ds_read_b64_tr_b16 a[46:47], v137 offset:12288\n"
    "v_mfma_f32_16x16x32_bf16 v[56:59], a[104:107], v[192:195], v[56:59]\n"
    "ds_read_b64_tr_b16 a[48:49], v136 offset:16384\n"
    "v_mfma_f32_16x16x32_bf16 v[56:59], a[112:115], v[208:211], v[56:59]\n"
    "ds_read_b64_tr_b16 a[50:51], v136 offset:20480\n"
    "v_mfma_f32_16x16x32_bf16 v[56:59], a[120:123], v[224:227], v[56:59]\n"
    "ds_read_b64_tr_b16 a[52:53], v137 offset:16384\n"
    "v_mfma_f32_16x16x32_bf16 v[60:63], a[100:103], v[176:179], v[60:63]\n"
    "ds_read_b64_tr_b16 a[54:55], v137 offset:20480\n"
    "v_mfma_f32_16x16x32_bf16 v[60:63], a[108:111], v[192:195], v[60:63]\n"
    "ds_read_b64_tr_b16 a[56:57], v136 offset:24576\n"
    "v_mfma_f32_16x16x32_bf16 v[60:63], a[116:119], v[208:211], v[60:63]\n"
    "ds_read_b64_tr_b16 a[58:59], v136 offset:28672\n"
    "v_mfma_f32_16x16x32_bf16 v[60:63], a[124:127], v[224:227], v[60:63]\n"
    "ds_read_b64_tr_b16 a[60:61], v137 offset:24576\n"
    "v_mfma_f32_16x16x32_bf16 v[88:91], a[96:99], v[180:183], v[88:91]\n"
    "ds_read_b64_tr_b16 a[62:63], v137 offset:28672\n"
    "v_mfma_f32_16x16x32_bf16 v[88:91], a[104:107], v[196:199], v[88:91]\n"
    "v_mfma_f32_16x16x32_bf16 v[88:91], a[112:115], v[212:215], v[88:91]\n"
    "v_mfma_f32_16x16x32_bf16 v[88:91], a[120:123], v[228:231], v[88:91]\n"
    "v_mfma_f32_16x16x32_bf16 v[92:95], a[100:103], v[180:183], v[92:95]\n"
    "v_mfma_f32_16x16x32_bf16 v[92:95], a[108:111], v[196:199], v[92:95]\n"
    "v_mfma_f32_16x16x32_bf16 v[92:95], a[116:119], v[212:215], v[92:95]\n"
    "v_mfma_f32_16x16x32_bf16 v[92:95], a[124:127], v[228:231], v[92:95]\n"
    "v_mfma_f32_16x16x32_bf16 v[120:123], a[96:99], v[184:187], v[120:123]\n"
    "v_mfma_f32_16x16x32_bf16 v[120:123], a[104:107], v[200:203], v[120:123]\n"
    "v_mfma_f32_16x16x32_bf16 v[120:123], a[112:115], v[216:219], v[120:123]\n"
    "v_mfma_f32_16x16x32_bf16 v[120:123], a[120:123], v[232:235], v[120:123]\n"
    "v_mfma_f32_16x16x32_bf16 v[124:127], a[100:103], v[184:187], v[124:127]\n"
    "v_mfma_f32_16x16x32_bf16 v[124:127], a[108:111], v[200:203], v[124:127]\n"
    "v_mfma_f32_16x16x32_bf16 v[124:127], a[116:119], v[216:219], v[124:127]\n"
    "s_waitcnt vmcnt(0)\n"
    "buffer_store_dword v250, v150, s[8:11], 0 offen offset:16 sc1\n"
    "v_mfma_f32_16x16x32_bf16 v[124:127], a[124:127], v[232:235], v[124:127]\n"
    "s_waitcnt lgkmcnt(0)\n"
    "v_mfma_f32_16x16x32_bf16 v[24:27], a[0:3], a[192:195], v[24:27]\n"
    "v_mfma_f32_16x16x32_bf16 v[24:27], a[8:11], a[208:211], v[24:27]\n"
    "ds_read_b64_tr_b16 a[64:65], v236 offset:0\n"
    "v_mfma_f32_16x16x32_bf16 v[24:27], a[16:19], a[224:227], v[24:27]\n"
    "ds_read_b64_tr_b16 a[66:67], v236 offset:4096\n"
    "v_mfma_f32_16x16x32_bf16 v[24:27], a[24:27], a[240:243], v[24:27]\n"
    "ds_read_b64_tr_b16 a[68:69], v237 offset:0\n"
    "v_mfma_f32_16x16x32_bf16 v[28:31], a[4:7], a[192:195], v[28:31]\n"
    "ds_read_b64_tr_b16 a[70:71], v237 offset:4096\n"
    "v_mfma_f32_16x16x32_bf16 v[28:31], a[12:15], a[208:211], v[28:31]\n"
    "ds_read_b64_tr_b16 a[72:73], v236 offset:8192\n"
    "v_mfma_f32_16x16x32_bf16 v[28:31], a[20:23], a[224:227], v[28:31]\n"
    "ds_read_b64_tr_b16 a[74:75], v236 offset:12288\n"
    "v_mfma_f32_16x16x32_bf16 v[28:31], a[28:31], a[240:243], v[28:31]\n"
    "ds_read_b64_tr_b16 a[76:77], v237 offset:8192\n"
    "v_mfma_f32_16x16x32_bf16 v[56:59], a[0:3], a[196:199], v[56:59]\n"
    "ds_read_b64_tr_b16 a[78:79], v237 offset:12288\n"
    "v_mfma_f32_16x16x32_bf16 v[56:59], a[8:11], a[212:215], v[56:59]\n"
    "ds_read_b64_tr_b16 a[80:81], v236 offset:16384\n"
    "v_mfma_f32_16x16x32_bf16 v[56:59], a[16:19], a[228:231], v[56:59]\n"
    "ds_read_b64_tr_b16 a[82:83], v236 offset:20480\n"
    "v_mfma_f32_16x16x32_bf16 v[56:59], a[24:27], a[244:247], v[56:59]\n"
    "ds_read_b64_tr_b16 a[84:85], v237 offset:16384\n"
    "v_mfma_f32_16x16x32_bf16 v[60:63], a[4:7], a[196:199], v[60:63]\n"
    "ds_read_b64_tr_b16 a[86:87], v237 offset:20480\n"
    "v_mfma_f32_16x16x32_bf16 v[60:63], a[12:15], a[212:215], v[60:63]\n"
    "ds_read_b64_tr_b16 a[88:89], v236 offset:24576\n"
    "v_mfma_f32_16x16x32_bf16 v[60:63], a[20:23], a[228:231], v[60:63]\n"
    "ds_read_b64_tr_b16 a[90:91], v236 offset:28672\n"
    "v_mfma_f32_16x16x32_bf16 v[60:63], a[28:31], a[244:247], v[60:63]\n"
    "ds_read_b64_tr_b16 a[92:93], v237 offset:24576\n"
    "v_cvt_pk_bf16_f32 v24, v24, v25\n"
    "v_cvt_pk_bf16_f32 v25, v26, v27\n"
    "v_mfma_f32_16x16x32_bf16 v[88:91], a[0:3], a[200:203], v[88:91]\n"
    "ds_read_b64_tr_b16 a[94:95], v237 offset:28672\n"
    "v_cvt_pk_bf16_f32 v26, v28, v29\n"
    "v_mfma_f32_16x16x32_bf16 v[88:91], a[8:11], a[216:219], v[88:91]\n"
    "v_cvt_pk_bf16_f32 v27, v30, v31\n"
    "v_mfma_f32_16x16x32_bf16 v[88:91], a[16:19], a[232:235], v[88:91]\n"
    "buffer_store_dwordx4 v[24:27], v146, s[8:11], 0 offen offset:0 sc1\n"
    "v_mfma_f32_16x16x32_bf16 v[88:91], a[24:27], a[248:251], v[88:91]\n"
    "v_mfma_f32_16x16x32_bf16 v[92:95], a[4:7], a[200:203], v[92:95]\n"
    "v_mfma_f32_16x16x32_bf16 v[92:95], a[12:15], a[216:219], v[92:95]\n"
    "v_mfma_f32_16x16x32_bf16 v[92:95], a[20:23], a[232:235], v[92:95]\n"
    "v_mfma_f32_16x16x32_bf16 v[92:95], a[28:31], a[248:251], v[92:95]\n"
    "v_cvt_pk_bf16_f32 v56, v56, v57\n"
    "v_cvt_pk_bf16_f32 v57, v58, v59\n"
    "v_mfma_f32_16x16x32_bf16 v[120:123], a[0:3], a[204:207], v[120:123]\n"
    "v_cvt_pk_bf16_f32 v58, v60, v61\n"
    "v_mfma_f32_16x16x32_bf16 v[120:123], a[8:11], a[220:223], v[120:123]\n"
    "v_cvt_pk_bf16_f32 v59, v62, v63\n"
    "v_mfma_f32_16x16x32_bf16 v[120:123], a[16:19], a[236:239], v[120:123]\n"
    "buffer_store_dwordx4 v[56:59], v146, s[8:11], 0 offen offset:1024 sc1\n"
    "v_mfma_f32_16x16x32_bf16 v[120:123], a[24:27], a[252:255], v[120:123]\n"
    "v_mfma_f32_16x16x32_bf16 v[124:127], a[4:7], a[204:207], v[124:127]\n"
    "v_mfma_f32_16x16x32_bf16 v[124:127], a[12:15], a[220:223], v[124:127]\n"
    "v_mfma_f32_16x16x32_bf16 v[124:127], a[20:23], a[236:239], v[124:127]\n"
    "v_mfma_f32_16x16x32_bf16 v[124:127], a[28:31], a[252:255], v[124:127]\n"
    "v_cvt_pk_bf16_f32 v88, v88, v89\n"
    "v_cvt_pk_bf16_f32 v89, v90, v91\n"
    "s_waitcnt lgkmcnt(0)\n"
    "v_mfma_f32_16x16x32_bf16 v[0:3], a[32:35], a[128:131], v[0:3]\n"
    "v_cvt_pk_bf16_f32 v90, v92, v93\n"
    "v_mfma_f32_16x16x32_bf16 v[0:3], a[40:43], a[144:147], v[0:3]\n"
    "v_cvt_pk_bf16_f32 v91, v94, v95\n"
    "ds_read_b64_tr_b16 a[96:97], v136 offset:32768\n"
    "v_mfma_f32_16x16x32_bf16 v[0:3], a[48:51], a[160:163], v[0:3]\n"
    "buffer_store_dwordx4 v[88:91], v146, s[8:11], 0 offen offset:2048 sc1\n"
    "ds_read_b64_tr_b16 a[98:99], v136 offset:36864\n"
    "v_mfma_f32_16x16x32_bf16 v[0:3], a[56:59], a[176:179], v[0:3]\n"
    "ds_read_b64_tr_b16 a[100:101], v137 offset:32768\n"
    "v_mfma_f32_16x16x32_bf16 v[4:7], a[36:39], a[128:131], v[4:7]\n"
    "ds_read_b64_tr_b16 a[102:103], v137 offset:36864\n"
    "v_mfma_f32_16x16x32_bf16 v[4:7], a[44:47], a[144:147], v[4:7]\n"
    "ds_read_b64_tr_b16 a[104:105], v136 offset:40960\n"
    "v_mfma_f32_16x16x32_bf16 v[4:7], a[52:55], a[160:163], v[4:7]\n"
    "ds_read_b64_tr_b16 a[106:107], v136 offset:45056\n"
    "v_mfma_f32_16x16x32_bf16 v[4:7], a[60:63], a[176:179], v[4:7]\n"
    "v_cvt_pk_bf16_f32 v120, v120, v121\n"
    "v_cvt_pk_bf16_f32 v121, v122, v123\n"
    "ds_read_b64_tr_b16 a[108:109], v137 offset:40960\n"
    "v_mfma_f32_16x16x32_bf16 v[32:35], a[32:35], a[132:135], v[32:35]\n"
    "v_cvt_pk_bf16_f32 v122, v124, v125\n"
    "ds_read_b64_tr_b16 a[110:111], v137 offset:45056\n"
    "v_mfma_f32_16x16x32_bf16 v[32:35], a[40:43], a[148:151], v[32:35]\n"
    "v_cvt_pk_bf16_f32 v123, v126, v127\n"
    "ds_read_b64_tr_b16 a[112:113], v136 offset:49152\n"
    "v_mfma_f32_16x16x32_bf16 v[32:35], a[48:51], a[164:167], v[32:35]\n"
    "buffer_store_dwordx4 v[120:123], v146, s[8:11], 0 offen offset:3072 sc1\n"
    "ds_read_b64_tr_b16 a[114:115], v136 offset:53248\n"
    "v_mfma_f32_16x16x32_bf16 v[32:35], a[56:59], a[180:183], v[32:35]\n"
    "ds_read_b64_tr_b16 a[116:117], v137 offset:49152\n"
    "v_mfma_f32_16x16x32_bf16 v[36:39], a[36:39], a[132:135], v[36:39]\n"
    "ds_read_b64_tr_b16 a[118:119], v137 offset:53248\n"
    "v_mfma_f32_16x16x32_bf16 v[36:39], a[44:47], a[148:151], v[36:39]\n"
    "ds_read_b64_tr_b16 a[120:121], v136 offset:57344\n"
    "v_mfma_f32_16x16x32_bf16 v[36:39], a[52:55], a[164:167], v[36:39]\n"
    "ds_read_b64_tr_b16 a[122:123], v136 offset:61440\n"
    "v_mfma_f32_16x16x32_bf16 v[36:39], a[60:63], a[180:183], v[36:39]\n"
    "ds_read_b64_tr_b16 a[124:125], v137 offset:57344\n"
    "v_mfma_f32_16x16x32_bf16 v[64:67], a[32:35], a[136:139], v[64:67]\n"
    "ds_read_b64_tr_b16 a[126:127], v137 offset:61440\n"
    "v_mfma_f32_16x16x32_bf16 v[64:67], a[40:43], a[152:155], v[64:67]\n"
    "v_mfma_f32_16x16x32_bf16 v[64:67], a[48:51], a[168:171], v[64:67]\n"
    "v_mfma_f32_16x16x32_bf16 v[64:67], a[56:59], a[184:187], v[64:67]\n"
    "v_mfma_f32_16x16x32_bf16 v[68:71], a[36:39], a[136:139], v[68:71]\n"
    "v_mfma_f32_16x16x32_bf16 v[68:71], a[44:47], a[152:155], v[68:71]\n"
    "v_mfma_f32_16x16x32_bf16 v[68:71], a[52:55], a[168:171], v[68:71]\n"
    "v_mfma_f32_16x16x32_bf16 v[68:71], a[60:63], a[184:187], v[68:71]\n"
    "v_mfma_f32_16x16x32_bf16 v[96:99], a[32:35], a[140:143], v[96:99]\n"
    "v_mfma_f32_16x16x32_bf16 v[96:99], a[40:43], a[156:159], v[96:99]\n"
    "v_mfma_f32_16x16x32_bf16 v[96:99], a[48:51], a[172:175], v[96:99]\n"
    "v_mfma_f32_16x16x32_bf16 v[96:99], a[56:59], a[188:191], v[96:99]\n"
    "v_mfma_f32_16x16x32_bf16 v[100:103], a[36:39], a[140:143], v[100:103]\n"
    "v_mfma_f32_16x16x32_bf16 v[100:103], a[44:47], a[156:159], v[100:103]\n"
    "v_mfma_f32_16x16x32_bf16 v[100:103], a[52:55], a[172:175], v[100:103]\n"
    "v_mfma_f32_16x16x32_bf16 v[100:103], a[60:63], a[188:191], v[100:103]\n"
    "s_waitcnt lgkmcnt(0)\n"
    "v_mfma_f32_16x16x32_bf16 v[0:3], a[64:67], v[172:175], v[0:3]\n"
    "v_mfma_f32_16x16x32_bf16 v[0:3], a[72:75], v[188:191], v[0:3]\n"
    "v_mfma_f32_16x16x32_bf16 v[0:3], a[80:83], v[204:207], v[0:3]\n"
    "v_mfma_f32_16x16x32_bf16 v[0:3], a[88:91], v[220:223], v[0:3]\n"
    "v_mfma_f32_16x16x32_bf16 v[4:7], a[68:71], v[172:175], v[4:7]\n"
    "v_mfma_f32_16x16x32_bf16 v[4:7], a[76:79], v[188:191], v[4:7]\n"
    "v_mfma_f32_16x16x32_bf16 v[4:7], a[84:87], v[204:207], v[4:7]\n"
    "v_mfma_f32_16x16x32_bf16 v[4:7], a[92:95], v[220:223], v[4:7]\n"
    "v_mfma_f32_16x16x32_bf16 v[32:35], a[64:67], v[176:179], v[32:35]\n"
    "v_mfma_f32_16x16x32_bf16 v[32:35], a[72:75], v[192:195], v[32:35]\n"
    "v_mfma_f32_16x16x32_bf16 v[32:35], a[80:83], v[208:211], v[32:35]\n"
    "v_mfma_f32_16x16x32_bf16 v[32:35], a[88:91], v[224:227], v[32:35]\n"
    "v_mfma_f32_16x16x32_bf16 v[36:39], a[68:71], v[176:179], v[36:39]\n"
    "v_mfma_f32_16x16x32_bf16 v[36:39], a[76:79], v[192:195], v[36:39]\n"
    "v_mfma_f32_16x16x32_bf16 v[36:39], a[84:87], v[208:211], v[36:39]\n"
    "v_mfma_f32_16x16x32_bf16 v[36:39], a[92:95], v[224:227], v[36:39]\n"
    "v_mfma_f32_16x16x32_bf16 v[64:67], a[64:67], v[180:183], v[64:67]\n"
    "v_mfma_f32_16x16x32_bf16 v[64:67], a[72:75], v[196:199], v[64:67]\n"
    "v_mfma_f32_16x16x32_bf16 v[64:67], a[80:83], v[212:215], v[64:67]\n"
    "v_mfma_f32_16x16x32_bf16 v[64:67], a[88:91], v[228:231], v[64:67]\n"
    "v_mfma_f32_16x16x32_bf16 v[68:71], a[68:71], v[180:183], v[68:71]\n"
    "s_waitcnt vmcnt(0)\n"
    "buffer_store_dword v250, v150, s[8:11], 0 offen offset:32 sc1\n"
    "v_mfma_f32_16x16x32_bf16 v[68:71], a[76:79], v[196:199], v[68:71]\n"
    "v_mfma_f32_16x16x32_bf16 v[68:71], a[84:87], v[212:215], v[68:71]\n"
    "v_mfma_f32_16x16x32_bf16 v[68:71], a[92:95], v[228:231], v[68:71]\n"
    "v_mfma_f32_16x16x32_bf16 v[96:99], a[64:67], v[184:187], v[96:99]\n"
    "v_mfma_f32_16x16x32_bf16 v[96:99], a[72:75], v[200:203], v[96:99]\n"
    "v_mfma_f32_16x16x32_bf16 v[96:99], a[80:83], v[216:219], v[96:99]\n"
    "v_mfma_f32_16x16x32_bf16 v[96:99], a[88:91], v[232:235], v[96:99]\n"
    "v_mfma_f32_16x16x32_bf16 v[100:103], a[68:71], v[184:187], v[100:103]\n"
    "v_mfma_f32_16x16x32_bf16 v[100:103], a[76:79], v[200:203], v[100:103]\n"
    "v_mfma_f32_16x16x32_bf16 v[100:103], a[84:87], v[216:219], v[100:103]\n"
    "v_mfma_f32_16x16x32_bf16 v[100:103], a[92:95], v[232:235], v[100:103]\n"
    "s_waitcnt lgkmcnt(0)\n"
    "v_mfma_f32_16x16x32_bf16 v[0:3], a[96:99], a[192:195], v[0:3]\n"
    "v_mfma_f32_16x16x32_bf16 v[0:3], a[104:107], a[208:211], v[0:3]\n"
    "v_mfma_f32_16x16x32_bf16 v[0:3], a[112:115], a[224:227], v[0:3]\n"
    "v_mfma_f32_16x16x32_bf16 v[0:3], a[120:123], a[240:243], v[0:3]\n"
    "v_mfma_f32_16x16x32_bf16 v[4:7], a[100:103], a[192:195], v[4:7]\n"
    "v_mfma_f32_16x16x32_bf16 v[4:7], a[108:111], a[208:211], v[4:7]\n"
    "v_mfma_f32_16x16x32_bf16 v[4:7], a[116:119], a[224:227], v[4:7]\n"
    "v_mfma_f32_16x16x32_bf16 v[4:7], a[124:127], a[240:243], v[4:7]\n"
    "v_mfma_f32_16x16x32_bf16 v[32:35], a[96:99], a[196:199], v[32:35]\n"
    "v_mfma_f32_16x16x32_bf16 v[32:35], a[104:107], a[212:215], v[32:35]\n"
    "v_mfma_f32_16x16x32_bf16 v[32:35], a[112:115], a[228:231], v[32:35]\n"
    "v_mfma_f32_16x16x32_bf16 v[32:35], a[120:123], a[244:247], v[32:35]\n"
    "v_mfma_f32_16x16x32_bf16 v[36:39], a[100:103], a[196:199], v[36:39]\n"
    "buffer_load_dword v251, v151, s[8:11], 0 offen sc1\n"
    "v_mfma_f32_16x16x32_bf16 v[36:39], a[108:111], a[212:215], v[36:39]\n"
    "buffer_load_dword v247, v248, s[8:11], 0 offen sc1\n"
    "v_mfma_f32_16x16x32_bf16 v[36:39], a[116:119], a[228:231], v[36:39]\n"
    "v_mfma_f32_16x16x32_bf16 v[36:39], a[124:127], a[244:247], v[36:39]\n"
    "v_mfma_f32_16x16x32_bf16 v[64:67], a[96:99], a[200:203], v[64:67]\n"
    "v_mfma_f32_16x16x32_bf16 v[64:67], a[104:107], a[216:219], v[64:67]\n"
    "v_mfma_f32_16x16x32_bf16 v[64:67], a[112:115], a[232:235], v[64:67]\n"
    "v_mfma_f32_16x16x32_bf16 v[64:67], a[120:123], a[248:251], v[64:67]\n"
    "v_mfma_f32_16x16x32_bf16 v[68:71], a[100:103], a[200:203], v[68:71]\n"
    "v_mfma_f32_16x16x32_bf16 v[68:71], a[108:111], a[216:219], v[68:71]\n"
    "v_mfma_f32_16x16x32_bf16 v[68:71], a[116:119], a[232:235], v[68:71]\n"
    "v_mfma_f32_16x16x32_bf16 v[68:71], a[124:127], a[248:251], v[68:71]\n"
    "v_mfma_f32_16x16x32_bf16 v[96:99], a[96:99], a[204:207], v[96:99]\n"
    "v_mfma_f32_16x16x32_bf16 v[96:99], a[104:107], a[220:223], v[96:99]\n"
    "v_mfma_f32_16x16x32_bf16 v[96:99], a[112:115], a[236:239], v[96:99]\n"
    "v_mfma_f32_16x16x32_bf16 v[96:99], a[120:123], a[252:255], v[96:99]\n"
    "v_mfma_f32_16x16x32_bf16 v[100:103], a[100:103], a[204:207], v[100:103]\n"
    "v_mfma_f32_16x16x32_bf16 v[100:103], a[108:111], a[220:223], v[100:103]\n"
    "v_mfma_f32_16x16x32_bf16 v[100:103], a[116:119], a[236:239], v[100:103]\n"
    "v_mfma_f32_16x16x32_bf16 v[100:103], a[124:127], a[252:255], v[100:103]\n"
    "s_waitcnt vmcnt(0)\n"
    "s_mov_b32 s35, 0\n"
    "G2_P0%=:\n"
    "v_cmp_eq_u32 vcc, 0, v251\n"
    "s_add_u32 s35, s35, 1\n"
    "s_nop 3\n"
    "s_cbranch_vccz G2_G0%=\n"
    "s_cmp_gt_u32 s35, 0x40000\n"
    "s_cbranch_scc1 G2_B0%=\n"
    "buffer_load_dword v251, v151, s[8:11], 0 offen sc1\n"
    "s_waitcnt vmcnt(0)\n"
    "s_branch G2_P0%=\n"
    "G2_B0%=:\n"
    "v_mov_b32 v249, 0x7fc00000\n"
    "G2_G0%=:\n"
    "buffer_load_dwordx4 v[8:11], v147, s[8:11], 0 offen offset:0 sc1\n"
    "buffer_load_dwordx4 v[40:43], v147, s[8:11], 0 offen offset:1024 sc1\n"
    "buffer_load_dwordx4 v[72:75], v147, s[8:11], 0 offen offset:2048 sc1\n"
    "buffer_load_dwordx4 v[104:107], v147, s[8:11], 0 offen offset:3072 sc1\n"
    "buffer_load_dwordx4 v[16:19], v148, s[8:11], 0 offen offset:0 sc1\n"
    "buffer_load_dwordx4 v[48:51], v148, s[8:11], 0 offen offset:1024 sc1\n"
    "buffer_load_dwordx4 v[80:83], v148, s[8:11], 0 offen offset:2048 sc1\n"
    "buffer_load_dwordx4 v[112:115], v148, s[8:11], 0 offen offset:3072 sc1\n"
    "s_mov_b32 s35, 0\n"
    "G2_P1%=:\n"
    "v_cmp_eq_u32 vcc, 0, v247\n"
    "s_add_u32 s35, s35, 1\n"
    "s_nop 3\n"
    "s_cbranch_vccz G2_G1%=\n"
    "s_cmp_gt_u32 s35, 0x40000\n"
    "s_cbranch_scc1 G2_B1%=\n"
    "buffer_load_dword v247, v248, s[8:11], 0 offen sc1\n"
    "s_waitcnt vmcnt(0)\n"
    "s_branch G2_P1%=\n"
    "G2_B1%=:\n"
    "v_mov_b32 v249, 0x7fc00000\n"
    "G2_G1%=:\n"
    "buffer_load_dwordx4 v[24:27], v149, s[8:11], 0 offen offset:0 sc1\n"
    "buffer_load_dwordx4 v[56:59], v149, s[8:11], 0 offen offset:1024 sc1\n"
    "buffer_load_dwordx4 v[88:91], v149, s[8:11], 0 offen offset:2048 sc1\n"
    "buffer_load_dwordx4 v[120:123], v149, s[8:11], 0 offen offset:3072 sc1\n"
    "s_waitcnt vmcnt(4)\n"
    "v_lshlrev_b32 v252, 16, v8\n"
    "v_and_b32 v253, 0xffff0000, v8\n"
    "v_pk_add_f32 v[0:1], v[0:1], v[252:253]\n"
    "v_lshlrev_b32 v252, 16, v9\n"
    "v_and_b32 v253, 0xffff0000, v9\n"
    "v_pk_add_f32 v[2:3], v[2:3], v[252:253]\n"
    "v_lshlrev_b32 v252, 16, v10\n"
    "v_and_b32 v253, 0xffff0000, v10\n"
    "v_pk_add_f32 v[4:5], v[4:5], v[252:253]\n"
    "v_lshlrev_b32 v252, 16, v11\n"
    "v_and_b32 v253, 0xffff0000, v11\n"
    "v_pk_add_f32 v[6:7], v[6:7], v[252:253]\n"
    "v_lshlrev_b32 v252, 16, v40\n"
    "v_and_b32 v253, 0xffff0000, v40\n"
    "v_pk_add_f32 v[32:33], v[32:33], v[252:253]\n"
    "v_lshlrev_b32 v252, 16, v41\n"
    "v_and_b32 v253, 0xffff0000, v41\n"
    "v_pk_add_f32 v[34:35], v[34:35], v[252:253]\n"
    "v_lshlrev_b32 v252, 16, v42\n"
    "v_and_b32 v253, 0xffff0000, v42\n"
    "v_pk_add_f32 v[36:37], v[36:37], v[252:253]\n"
    "v_lshlrev_b32 v252, 16, v43\n"
    "v_and_b32 v253, 0xffff0000, v43\n"
    "v_pk_add_f32 v[38:39], v[38:39], v[252:253]\n"
    "v_lshlrev_b32 v252, 16, v72\n"
    "v_and_b32 v253, 0xffff0000, v72\n"
    "v_pk_add_f32 v[64:65], v[64:65], v[252:253]\n"
    "v_lshlrev_b32 v252, 16, v73\n"
    "v_and_b32 v253, 0xffff0000, v73\n"
    "v_pk_add_f32 v[66:67], v[66:67], v[252:253]\n"
    "v_lshlrev_b32 v252, 16, v74\n"
    "v_and_b32 v253, 0xffff0000, v74\n"
    "v_pk_add_f32 v[68:69], v[68:69], v[252:253]\n"
    "v_lshlrev_b32 v252, 16, v75\n"
    "v_and_b32 v253, 0xffff0000, v75\n"
    "v_pk_add_f32 v[70:71], v[70:71], v[252:253]\n"
    "v_lshlrev_b32 v252, 16, v104\n"
    "v_and_b32 v253, 0xffff0000, v104\n"
    "v_pk_add_f32 v[96:97], v[96:97], v[252:253]\n"
    "v_lshlrev_b32 v252, 16, v105\n"
    "v_and_b32 v253, 0xffff0000, v105\n"
    "v_pk_add_f32 v[98:99], v[98:99], v[252:253]\n"
    "v_lshlrev_b32 v252, 16, v106\n"
    "v_and_b32 v253, 0xffff0000, v106\n"
    "v_pk_add_f32 v[100:101], v[100:101], v[252:253]\n"
    "v_lshlrev_b32 v252, 16, v107\n"
    "v_and_b32 v253, 0xffff0000, v107\n"
    "v_pk_add_f32 v[102:103], v[102:103], v[252:253]\n"
    "v_lshlrev_b32 v252, 16, v16\n"
    "v_and_b32 v253, 0xffff0000, v16\n"
    "v_pk_add_f32 v[0:1], v[0:1], v[252:253]\n"
    "v_lshlrev_b32 v252, 16, v17\n"
    "v_and_b32 v253, 0xffff0000, v17\n"
    "v_pk_add_f32 v[2:3], v[2:3], v[252:253]\n"
    "v_lshlrev_b32 v252, 16, v18\n"
    "v_and_b32 v253, 0xffff0000, v18\n"
    "v_pk_add_f32 v[4:5], v[4:5], v[252:253]\n"
    "v_lshlrev_b32 v252, 16, v19\n"
    "v_and_b32 v253, 0xffff0000, v19\n"
    "v_pk_add_f32 v[6:7], v[6:7], v[252:253]\n"
    "v_lshlrev_b32 v252, 16, v48\n"
    "v_and_b32 v253, 0xffff0000, v48\n"
    "v_pk_add_f32 v[32:33], v[32:33], v[252:253]\n"
    "v_lshlrev_b32 v252, 16, v49\n"
    "v_and_b32 v253, 0xffff0000, v49\n"
    "v_pk_add_f32 v[34:35], v[34:35], v[252:253]\n"
    "v_lshlrev_b32 v252, 16, v50\n"
    "v_and_b32 v253, 0xffff0000, v50\n"
    "v_pk_add_f32 v[36:37], v[36:37], v[252:253]\n"
    "v_lshlrev_b32 v252, 16, v51\n"
    "v_and_b32 v253, 0xffff0000, v51\n"
    "v_pk_add_f32 v[38:39], v[38:39], v[252:253]\n"
    "v_lshlrev_b32 v252, 16, v80\n"
    "v_and_b32 v253, 0xffff0000, v80\n"
    "v_pk_add_f32 v[64:65], v[64:65], v[252:253]\n"
    "v_lshlrev_b32 v252, 16, v81\n"
    "v_and_b32 v253, 0xffff0000, v81\n"
    "v_pk_add_f32 v[66:67], v[66:67], v[252:253]\n"
    "v_lshlrev_b32 v252, 16, v82\n"
    "v_and_b32 v253, 0xffff0000, v82\n"
    "v_pk_add_f32 v[68:69], v[68:69], v[252:253]\n"
    "v_lshlrev_b32 v252, 16, v83\n"
    "v_and_b32 v253, 0xffff0000, v83\n"
    "v_pk_add_f32 v[70:71], v[70:71], v[252:253]\n"
    "v_lshlrev_b32 v252, 16, v112\n"
    "v_and_b32 v253, 0xffff0000, v112\n"
    "v_pk_add_f32 v[96:97], v[96:97], v[252:253]\n"
    "v_lshlrev_b32 v252, 16, v113\n"
    "v_and_b32 v253, 0xffff0000, v113\n"
    "v_pk_add_f32 v[98:99], v[98:99], v[252:253]\n"
    "v_lshlrev_b32 v252, 16, v114\n"
    "v_and_b32 v253, 0xffff0000, v114\n"
    "v_pk_add_f32 v[100:101], v[100:101], v[252:253]\n"
    "v_lshlrev_b32 v252, 16, v115\n"
    "v_and_b32 v253, 0xffff0000, v115\n"
    "S3_H3%=:\n"
    "s_cbranch_execz S3_R%=\n"
    "v_pk_add_f32 v[102:103], v[102:103], v[252:253]\n"
    "s_mov_b32 s56, 0\n"
    "s_waitcnt vmcnt(3)\n"
    "v_add_f32 v0, v0, v249\n"
    "v_lshlrev_b32 v252, 16, v24\n"
    "v_and_b32 v253, 0xffff0000, v24\n"
    "v_pk_add_f32 v[0:1], v[0:1], v[252:253]\n"
    "v_lshlrev_b32 v252, 16, v25\n"
    "v_and_b32 v253, 0xffff0000, v25\n"
    "v_pk_add_f32 v[2:3], v[2:3], v[252:253]\n"
    "v_lshlrev_b32 v252, 16, v26\n"
    "v_and_b32 v253, 0xffff0000, v26\n"
    "v_pk_add_f32 v[4:5], v[4:5], v[252:253]\n"
    "v_lshlrev_b32 v252, 16, v27\n"
    "v_and_b32 v253, 0xffff0000, v27\n"
    "v_pk_add_f32 v[6:7], v[6:7], v[252:253]\n"
    "v_cvt_pk_bf16_f32 v0, v0, v1\n"
    "v_cvt_pk_bf16_f32 v1, v2, v3\n"
    "v_cvt_pk_bf16_f32 v2, v4, v5\n"
    "v_cvt_pk_bf16_f32 v3, v6, v7\n"
    "s_nop 0\n"
    "v_permlane16_swap_b32 v0, v2\n"
    "v_permlane16_swap_b32 v1, v3\n"
    "buffer_store_dwordx4 v[0:3], v153, s[12:15], s56 offen\n"
    "s_mov_b32 s56, 32768\n"
    "s_waitcnt vmcnt(3)\n"
    "v_add_f32 v32, v32, v249\n"
    "v_lshlrev_b32 v252, 16, v56\n"
    "v_and_b32 v253, 0xffff0000, v56\n"
    "v_pk_add_f32 v[32:33], v[32:33], v[252:253]\n"
    "v_lshlrev_b32 v252, 16, v57\n"
    "v_and_b32 v253, 0xffff0000, v57\n"
    "v_pk_add_f32 v[34:35], v[34:35], v[252:253]\n"
    "v_lshlrev_b32 v252, 16, v58\n"
    "v_and_b32 v253, 0xffff0000, v58\n"
    "v_pk_add_f32 v[36:37], v[36:37], v[252:253]\n"
    "v_lshlrev_b32 v252, 16, v59\n"
    "v_and_b32 v253, 0xffff0000, v59\n"
    "v_pk_add_f32 v[38:39], v[38:39], v[252:253]\n"
    "v_cvt_pk_bf16_f32 v32, v32, v33\n"
    "v_cvt_pk_bf16_f32 v33, v34, v35\n"
    "v_cvt_pk_bf16_f32 v34, v36, v37\n"
    "v_cvt_pk_bf16_f32 v35, v38, v39\n"
    "s_nop 0\n"
    "v_permlane16_swap_b32 v32, v34\n"
    "v_permlane16_swap_b32 v33, v35\n"
    "buffer_store_dwordx4 v[32:35], v153, s[12:15], s56 offen\n"
    "s_mov_b32 s56, 65536\n"
    "s_waitcnt vmcnt(3)\n"
    "v_add_f32 v64, v64, v249\n"
    "v_lshlrev_b32 v252, 16, v88\n"
    "v_and_b32 v253, 0xffff0000, v88\n"
    "v_pk_add_f32 v[64:65], v[64:65], v[252:253]\n"
    "v_lshlrev_b32 v252, 16, v89\n"
    "v_and_b32 v253, 0xffff0000, v89\n"
    "v_pk_add_f32 v[66:67], v[66:67], v[252:253]\n"
    "v_lshlrev_b32 v252, 16, v90\n"
    "v_and_b32 v253, 0xffff0000, v90\n"
    "v_pk_add_f32 v[68:69], v[68:69], v[252:253]\n"
    "v_lshlrev_b32 v252, 16, v91\n"
    "v_and_b32 v253, 0xffff0000, v91\n"
    "v_pk_add_f32 v[70:71], v[70:71], v[252:253]\n"
    "v_cvt_pk_bf16_f32 v64, v64, v65\n"
    "v_cvt_pk_bf16_f32 v65, v66, v67\n"
    "v_cvt_pk_bf16_f32 v66, v68, v69\n"
    "v_cvt_pk_bf16_f32 v67, v70, v71\n"
    "s_nop 0\n"
    "v_permlane16_swap_b32 v64, v66\n"
    "v_permlane16_swap_b32 v65, v67\n"
    "buffer_store_dwordx4 v[64:67], v153, s[12:15], s56 offen\n"
    "s_mov_b32 s56, 98304\n"
    "s_waitcnt vmcnt(3)\n"
    "v_add_f32 v96, v96, v249\n"
    "v_lshlrev_b32 v252, 16, v120\n"
    "v_and_b32 v253, 0xffff0000, v120\n"
    "v_pk_add_f32 v[96:97], v[96:97], v[252:253]\n"
    "v_lshlrev_b32 v252, 16, v121\n"
    "v_and_b32 v253, 0xffff0000, v121\n"
    "v_pk_add_f32 v[98:99], v[98:99], v[252:253]\n"
    "v_lshlrev_b32 v252, 16, v122\n"
    "v_and_b32 v253, 0xffff0000, v122\n"
    "v_pk_add_f32 v[100:101], v[100:101], v[252:253]\n"
    "v_lshlrev_b32 v252, 16, v123\n"
    "v_and_b32 v253, 0xffff0000, v123\n"
    "v_pk_add_f32 v[102:103], v[102:103], v[252:253]\n"
    "v_cvt_pk_bf16_f32 v96, v96, v97\n"
    "v_cvt_pk_bf16_f32 v97, v98, v99\n"
    "v_cvt_pk_bf16_f32 v98, v100, v101\n"
    "v_cvt_pk_bf16_f32 v99, v102, v103\n"
    "s_nop 0\n"
    "v_permlane16_swap_b32 v96, v98\n"
    "v_permlane16_swap_b32 v97, v99\n"
    "buffer_store_dwordx4 v[96:99], v153, s[12:15], s56 offen\n"
    "s_branch H3_END%=\n"
    "G1_CSR%=:\n"
    "s_mov_b32 s35, 0\n"
    "G1_CSL%=:\n"
    "s_mov_b32 s36, 540672\n"
    "buffer_load_dwordx2 a[244:245], v245, s[8:11], s36 offen sc1\n"
    "s_mov_b32 s36, 598016\n"
    "buffer_load_dwordx2 a[246:247], v245, s[8:11], s36 offen sc1\n"
    "s_mov_b32 s36, 655360\n"
    "buffer_load_dwordx2 a[248:249], v245, s[8:11], s36 offen sc1\n"
    "s_mov_b32 s36, 712704\n"
    "buffer_load_dwordx2 a[250:251], v245, s[8:11], s36 offen sc1\n"
    "s_mov_b32 s36, 770048\n"
    "buffer_load_dwordx2 a[252:253], v245, s[8:11], s36 offen sc1\n"
    "s_mov_b32 s36, 827392\n"
    "buffer_load_dwordx2 a[254:255], v245, s[8:11], s36 offen sc1\n"
    "s_mov_b32 s36, 884736\n"
    "buffer_load_dwordx2 v[252:253], v245, s[8:11], s36 offen sc1\n"
    "s_mov_b32 s36, 942080\n"
    "buffer_load_dwordx2 v[254:255], v245, s[8:11], s36 offen sc1\n"
    "s_waitcnt vmcnt(0)\n"
    "v_add_f32 v236, v252, v254\n"
    "v_min_u32 v237, v253, v255\n"
    "v_accvgpr_read_b32 v234, a244\n"
    "v_accvgpr_read_b32 v235, a245\n"
    "v_add_f32 v236, v236, v234\n"
    "v_min_u32 v237, v237, v235\n"
    "v_accvgpr_read_b32 v234, a246\n"
    "v_accvgpr_read_b32 v235, a247\n"
    "v_add_f32 v236, v236, v234\n"
    "v_min_u32 v237, v237, v235\n"
    "v_accvgpr_read_b32 v234, a248\n"
    "v_accvgpr_read_b32 v235, a249\n"
    "v_add_f32 v236, v236, v234\n"
    "v_min_u32 v237, v237, v235\n"
    "v_accvgpr_read_b32 v234, a250\n"
    "v_accvgpr_read_b32 v235, a251\n"
    "v_add_f32 v236, v236, v234\n"
    "v_min_u32 v237, v237, v235\n"
    "v_accvgpr_read_b32 v234, a252\n"
    "v_accvgpr_read_b32 v235, a253\n"
    "v_add_f32 v236, v236, v234\n"
    "v_min_u32 v237, v237, v235\n"
    "v_accvgpr_read_b32 v234, a254\n"
    "v_accvgpr_read_b32 v235, a255\n"
    "v_add_f32 v236, v236, v234\n"
    "v_min_u32 v237, v237, v235\n"
    "v_cmp_eq_u32 vcc, 0, v237\n"
    "s_add_u32 s35, s35, 1\n"
    "s_nop 3\n"
    "s_cbranch_vccz G1_CSO%=\n"
    "s_cmp_lt_u32 s35, 0x40000\n"
    "s_cbranch_scc1 G1_CSL%=\n"
    "v_mov_b32 v236, 0x7fc00000\n"
    "s_branch G1_CSO%=\n"
    "H3_END%=:\n"
    ".Lh3_end0:\n"
    : "+{s[0:1]}"(ka), "+{s2}"(b), "+{s3}"(lds), "+{v0}"(t)
    :
    : "memory", "vcc", "scc", "v1","v2","v3","v4","v5","v6","v7","v8","v9","v10","v11","v12","v13","v14","v15","v16","v17","v18","v19","v20","v21","v22","v23","v24","v25","v26","v27","v28","v29","v30","v31","v32","v33","v34","v35","v36","v37","v38","v39","v40","v41","v42","v43","v44","v45","v46","v47","v48","v49","v50","v51","v52","v53","v54","v55","v56","v57","v58","v59","v60","v61","v62","v63","v64","v65","v66","v67","v68","v69","v70","v71","v72","v73","v74","v75","v76","v77","v78","v79","v80","v81","v82","v83","v84","v85","v86","v87","v88","v89","v90","v91","v92","v93","v94","v95","v96","v97","v98","v99","v100","v101","v102","v103","v104","v105","v106","v107","v108","v109","v110","v111","v112","v113","v114","v115","v116","v117","v118","v119","v120","v121","v122","v123","v124","v125","v126","v127","v128","v129","v130","v131","v132","v133","v134","v135","v136","v137","v138","v139","v140","v141","v142","v143","v144","v145","v146","v147","v148","v149","v150","v151","v152","v153","v154","v155","v156","v157","v158","v159","v160","v161","v162","v163","v164","v165","v166","v167","v168","v169","v170","v171","v172","v173","v174","v175","v176","v177","v178","v179","v180","v181","v182","v183","v184","v185","v186","v187","v188","v189","v190","v191","v192","v193","v194","v195","v196","v197","v198","v199","v200","v201","v202","v203","v204","v205","v206","v207","v208","v209","v210","v211","v212","v213","v214","v215","v216","v217","v218","v219","v220","v221","v222","v223","v224","v225","v226","v227","v228","v229","v230","v231","v232","v233","v234","v235","v236","v237","v238","v239","v240","v241","v242","v243","v244","v245","v246","v247","v248","v249","v250","v251","v252","v253","v254","v255", "a0","a1","a2","a3","a4","a5","a6","a7","a8","a9","a10","a11","a12","a13","a14","a15","a16","a17","a18","a19","a20","a21","a22","a23","a24","a25","a26","a27","a28","a29","a30","a31","a32","a33","a34","a35","a36","a37","a38","a39","a40","a41","a42","a43","a44","a45","a46","a47","a48","a49","a50","a51","a52","a53","a54","a55","a56","a57","a58","a59","a60","a61","a62","a63","a64","a65","a66","a67","a68","a69","a70","a71","a72","a73","a74","a75","a76","a77","a78","a79","a80","a81","a82","a83","a84","a85","a86","a87","a88","a89","a90","a91","a92","a93","a94","a95","a96","a97","a98","a99","a100","a101","a102","a103","a104","a105","a106","a107","a108","a109","a110","a111","a112","a113","a114","a115","a116","a117","a118","a119","a120","a121","a122","a123","a124","a125","a126","a127","a128","a129","a130","a131","a132","a133","a134","a135","a136","a137","a138","a139","a140","a141","a142","a143","a144","a145","a146","a147","a148","a149","a150","a151","a152","a153","a154","a155","a156","a157","a158","a159","a160","a161","a162","a163","a164","a165","a166","a167","a168","a169","a170","a171","a172","a173","a174","a175","a176","a177","a178","a179","a180","a181","a182","a183","a184","a185","a186","a187","a188","a189","a190","a191","a192","a193","a194","a195","a196","a197","a198","a199","a200","a201","a202","a203","a204","a205","a206","a207","a208","a209","a210","a211","a212","a213","a214","a215","a216","a217","a218","a219","a220","a221","a222","a223","a224","a225","a226","a227","a228","a229","a230","a231","a232","a233","a234","a235","a236","a237","a238","a239","a240","a241","a242","a243","a244","a245","a246","a247","a248","a249","a250","a251","a252","a253","a254","a255", "s4","s5","s6","s7","s8","s9","s10","s11","s12","s13","s14","s15","s16","s17","s18","s19","s20","s21","s22","s23","s24","s25","s26","s27","s28","s29","s30","s31", "s33","s34","s35","s36","s37","s38","s39","s40","s41","s42","s43","s44","s45","s46","s47","s48","s49","s50","s51","s52","s53","s54","s55","s56","s57","s58","s59","s60","s61","s62","s63","s64","s65","s66","s67","s68","s69","s70","s71","s72","s73","s74","s75","s76","s77","s78","s79","s80","s81","s82","s83","s84","s85","s86","s87","s88","s89","s90","s91","s92","s93","s94","s95","s96","s97","s98","s99");
}

extern "C" void kernel_launch(void* const* d_in, const int* in_sizes, int n_in, void* d_out, int out_size, void* d_ws, size_t ws_size, hipStream_t stream) {
  Args a;
  a.xq = d_in[0]; a.kk = d_in[1]; a.vv = d_in[2]; a.shk = d_in[3]; a.ssmk = d_in[4]; a.ssmD = d_in[5];
  a.w1 = d_in[7]; a.w3 = d_in[8]; a.w2 = d_in[9]; a.out = d_out; a.ws = d_ws;
  (void)hipFuncSetAttribute((const void*)h3_block, hipFuncAttributeMaxDynamicSharedMemorySize, 163712);
  h3_block<<<256, 256, 163712, stream>>>(a);
}
```
